```python
import torch

def _softmax(x, axis=-1):
    return torch.softmax(x, dim=axis)

def _gelu(x):
    return torch.nn.functional.gelu(x, approximate='tanh')

def _silu(x):
    return torch.nn.functional.silu(x)

def _sigmoid(x):
    return torch.sigmoid(x)

def _layernorm(x, axis=-1, eps=1e-05):
    m = x.mean(dim=axis, keepdim=True)
    v = x.var(dim=axis, keepdim=True, unbiased=False)
    return (x - m) / torch.sqrt(v + eps)

def _scatter_add(tgt, idx, src, axis=0):
    return tgt.clone().index_add_(axis, idx, src)

def _segment_sum(src, seg, n):
    out = torch.zeros((n,) + src.shape[1:], dtype=src.dtype)
    return out.index_add_(0, seg, src)

def _pad(x, pw):
    flat = tuple((v for pair in reversed(pw) for v in pair))
    return torch.nn.functional.pad(x, flat)
kernel_name = 'gated_mix__memory_aug_recurrent_mem_tr__glu_gated_swiglu__prompt'

def setup_inputs():

    _w = lambda a: (lambda t: t.to(torch.bfloat16) if t.is_floating_point() else t)(torch.as_tensor(a))

    def _A_setup():
        B, S, Dm = (1, 2048, 512)
        H, Dh = (32, 128)
        M, seg = (8, 32)
        seg = min(seg, S)
        S = S // seg * seg
        return {'x': _w((lambda *s: torch.randn(*s, dtype=torch.bfloat16) * 0.05)(B, S, Dm)), 'mem0': _w((lambda *s: torch.randn(*s, dtype=torch.bfloat16) * 0.4)(B, M, Dm)), 'Wq': _w((lambda *s: torch.randn(*s, dtype=torch.bfloat16) * 6.4)(Dm, H, Dh) * Dm ** (-0.5)), 'Wk': _w((lambda *s: torch.randn(*s, dtype=torch.bfloat16) * 6.4)(Dm, H, Dh) * Dm ** (-0.5)), 'Wv': _w((lambda *s: torch.randn(*s, dtype=torch.bfloat16) * 0.4)(Dm, H, Dh) * Dm ** (-0.5)), 'Wo': _w((lambda *s: torch.randn(*s, dtype=torch.bfloat16) * 0.4)(H, Dh, Dm) * (H * Dh) ** (-0.5)), 'seg_len': seg}
    _A_in = _A_setup()

    def _B_setup():
        B, S, D, F = (1, 2048, 512, 1792)
        return {'x': _w((lambda *s: torch.randn(*s, dtype=torch.bfloat16) * 0.05)(B, S, D)), 'w1': _w((lambda *s: torch.randn(*s, dtype=torch.bfloat16) * 0.8)(D, F) * (2.0 / D) ** 0.5), 'w3': _w((lambda *s: torch.randn(*s, dtype=torch.bfloat16) * 0.8)(D, F) * (2.0 / D) ** 0.5), 'w2': _w((lambda *s: torch.randn(*s, dtype=torch.bfloat16) * 0.8)(F, D) * (2.0 / F) ** 0.5)}
    _B_in = _B_setup()
    return {'A_x': _A_in['x'], 'A_mem0': _A_in['mem0'], 'A_Wq': _A_in['Wq'], 'A_Wk': _A_in['Wk'], 'A_Wv': _A_in['Wv'], 'A_Wo': _A_in['Wo'], 'A_seg_len': _A_in['seg_len'], 'B_x': _B_in['x'], 'B_w1': _B_in['w1'], 'B_w3': _B_in['w3'], 'B_w2': _B_in['w2'], 'gate_w': _w((lambda *s: torch.randn(*s, dtype=torch.bfloat16) * 0.05)(512) * 16.0)}

def reference(A_x, A_mem0, A_Wq, A_Wk, A_Wv, A_Wo, A_seg_len, B_x, B_w1, B_w3, B_w2, gate_w):

    def _A_ref(x, mem0, Wq, Wk, Wv, Wo, seg_len):
        B, S, Dm = x.shape
        H, Dh = (Wq.shape[1], Wq.shape[2])
        M = mem0.shape[1]
        scale = Dh ** (-0.5)
        n_seg = S // seg_len
        L = M + seg_len
        idx = torch.arange(L)
        mask = (idx[:, None] < M) | (idx[:, None] >= idx[None, :])
        mem = mem0
        outs = []
        for s in range(n_seg):
            xs = x[:, s * seg_len:(s + 1) * seg_len, :].clone()
            h = torch.concatenate([mem, xs], axis=1)
            q = torch.einsum('btd,dhe->bhte', h, Wq)
            k = torch.einsum('btd,dhe->bhte', h, Wk)
            v = torch.einsum('btd,dhe->bhte', h, Wv)
            scores = torch.einsum('bhqe,bhke->bhqk', q, k) * scale
            scores = torch.where(mask[None, None], scores, -1000000000.0)
            p = _softmax(scores, axis=-1)
            o = torch.einsum('bhqk,bhke->bhqe', p, v)
            o = torch.einsum('bhte,hed->btd', o, Wo)
            mem = o[:, :M, :].clone()
            outs.append(o[:, M:, :])
        return torch.concatenate(outs, axis=1)

    def _B_ref(x, w1, w3, w2):
        gate = torch.einsum('bsd,df->bsf', x, w1)
        up = torch.einsum('bsd,df->bsf', x, w3)
        h = _silu(gate) * up
        out = torch.einsum('bsf,fd->bsd', h, w2)
        return out
    x = A_x
    g = _sigmoid((x * gate_w).sum(-1, keepdims=True))
    a = _A_ref(x, A_mem0, A_Wq, A_Wk, A_Wv, A_Wo, A_seg_len)
    b = _B_ref(x, B_w1, B_w3, B_w2)
    return g * a + (1 - g) * b
```

```cpp
#include <hip/hip_runtime.h>
#include <hip/hip_bf16.h>
#include <cstdint>

constexpr size_t MBy = 1u << 20;
constexpr size_t OFF_P = 0, OFF_G = 1 * MBy, OFF_FLG = 2 * MBy;
constexpr size_t F_MF = 0;
constexpr size_t F_XF = 16384;
constexpr size_t F_QF = 16384 + 4096;
constexpr size_t F_UF = 16384 + 8192;
constexpr size_t F_OF = 32768;
constexpr size_t F_CTL = 49152;
constexpr size_t OFF_KX = 3 * MBy, SZ_KX = 17 * MBy, OFF_VT = OFF_KX + SZ_KX, OFF_QX = OFF_VT + SZ_KX, OFF_KM = OFF_QX + SZ_KX;
constexpr size_t OFF_VM = OFF_KM + 4 * MBy, OFF_HF = OFF_VM + 4 * MBy, OFF_OX = OFF_HF + 8 * MBy, OFF_GZ = OFF_OX + 16 * MBy, OFF_END = OFF_GZ + MBy;

#define CHAIN_ASM \
    "; ---- inputs to fixed registers\n" \
    "s_mov_b64 s[40:41], %1\n" \
    "s_mov_b64 s[42:43], %2\n" \
    "s_mov_b64 s[44:45], %3\n" \
    "s_mov_b64 s[46:47], %4\n" \
    "s_mov_b64 s[48:49], %5\n" \
    "s_mov_b64 s[92:93], %6\n" \
    "s_mov_b64 s[52:53], %7\n" \
    "s_mov_b64 s[54:55], %8\n" \
    "s_mov_b64 s[56:57], %9\n" \
    "s_mov_b64 s[84:85], %10\n" \
    "s_mov_b64 s[90:91], %16\n" \
    "s_mov_b32 s63, %11\n" \
    "s_mov_b32 s62, %12\n" \
    "s_mov_b32 s61, %13\n" \
    "s_mov_b32 s81, %14\n" \
    "s_mov_b32 s82, %15\n" \
    "s_lshl_b32 s64, s63, 1\n" \
    "s_add_u32 s64, s64, s62\n" \
    "s_lshl_b32 s64, s64, 8\n" \
    "s_add_u32 s58, s84, s64\n" \
    "s_addc_u32 s59, s85, 0\n" \
    "s_add_u32 s94, s84, 49152\n" \
    "s_addc_u32 s95, s85, 0\n" \
    "s_lshl_b32 s64, s63, 6\n" \
    "s_add_u32 s64, s64, 16384\n" \
    "s_add_u32 s84, s84, s64\n" \
    "s_addc_u32 s85, s85, 0\n" \
    "s_mov_b32 s66, 0\n" \
    "s_mov_b32 s60, 0\n" \
    "s_lshl_b32 s86, s61, 11\n" \
    "s_mov_b32 s76, 0x00ff00ff\n" \
    "s_mov_b32 s77, 0x00ff00ff\n" \
    "s_mov_b32 s78, 0x55555555\n" \
    "s_mov_b32 s79, 0x55555555\n" \
    "s_mov_b32 s80, 0x3e0293ee\n" \
    "s_mov_b32 s83, 0xbe0293ee\n" \
    "v_mbcnt_lo_u32_b32 v0, -1, 0\n" \
    "v_mbcnt_hi_u32_b32 v0, -1, v0\n" \
    "v_and_b32 v1, 15, v0\n" \
    "v_lshrrev_b32 v2, 4, v0\n" \
    "v_lshlrev_b32 v57, 4, v0\n" \
    "v_add_u32 v58, s86, v57\n" \
    "v_mov_b32 v3, 0x44444444\n" \
    "v_mov_b32 v4, 0xeeeeeeee\n" \
    "v_cndmask_b32 v56, v4, v3, s[76:77]\n" \
    "v_lshrrev_b32 v3, 5, v0\n" \
    "v_and_b32 v4, 1, v2\n" \
    "s_lshl_b32 s64, s61, 9\n" \
    "v_lshlrev_b32 v5, 8, v3\n" \
    "v_lshl_add_u32 v5, v1, 4, v5\n" \
    "v_lshl_add_u32 v5, v4, 3, v5\n" \
    "s_add_u32 s64, s64, 40960\n" \
    "v_add_u32 v59, s64, v5\n" \
    "v_lshrrev_b32 v5, 2, v1\n" \
    "v_lshl_add_u32 v5, v2, 2, v5\n" \
    "v_lshlrev_b32 v5, 4, v5\n" \
    "v_and_b32 v6, 1, v1\n" \
    "v_lshl_add_u32 v5, v6, 3, v5\n" \
    "v_add_u32 v60, 40960, v5\n" \
    "s_lshr_b32 s64, s61, 1\n" \
    "s_lshl_b32 s64, s64, 2\n" \
    "s_and_b32 s67, s61, 1\n" \
    "s_lshl_b32 s67, s67, 1\n" \
    "s_add_u32 s64, s64, s67\n" \
    "s_lshl_b32 s64, s64, 8\n" \
    "s_add_u32 s64, s64, 45056\n" \
    "v_lshlrev_b32 v5, 4, v1\n" \
    "v_lshl_add_u32 v5, v2, 2, v5\n" \
    "v_add_u32 v61, s64, v5\n" \
    "s_lshl_b32 s64, s61, 14\n" \
    "s_add_u32 s64, s64, 65536\n" \
    "v_add_u32 v62, s64, v57\n" \
    "v_lshrrev_b32 v5, 3, v1\n" \
    "v_lshl_add_u32 v5, v3, 1, v5\n" \
    "v_lshlrev_b32 v5, 14, v5\n" \
    "v_lshl_add_u32 v5, v4, 7, v5\n" \
    "v_and_b32 v6, 7, v1\n" \
    "v_lshl_add_u32 v63, v6, 4, v5\n" \
    "v_lshrrev_b32 v5, 3, v0\n" \
    "v_and_b32 v5, 1, v5\n" \
    "v_lshlrev_b32 v5, 2, v5\n" \
    "v_and_b32 v6, 1, v2\n" \
    "v_lshlrev_b32 v6, 1, v6\n" \
    "v_lshrrev_b32 v7, 1, v2\n" \
    "v_add_u32 v6, v6, v7\n" \
    "v_add_u32 v5, v5, v6\n" \
    "v_lshlrev_b32 v5, 4, v5\n" \
    "v_and_b32 v6, 7, v0\n" \
    "v_lshrrev_b32 v6, 1, v6\n" \
    "v_lshl_add_u32 v64, v6, 2, v5\n" \
    "s_sub_u32 s56, s56, 40960\n" \
    "s_subb_u32 s57, s57, 0\n" \
    "; ---- staging addresses: v8,v9 = QKV DMA source offsets; v10,v11 = QKV tr addresses (t = 0,1)\n" \
    "v_lshlrev_b32 v5, 1, v2\n" \
    "v_xor_b32 v5, v5, v1\n" \
    "v_lshlrev_b32 v5, 4, v5\n" \
    "v_lshl_add_u32 v8, v2, 13, v5\n" \
    "v_xor_b32 v9, 0x80, v8\n" \
    "v_lshrrev_b32 v5, 2, v1\n" \
    "v_and_b32 v6, 1, v2\n" \
    "v_lshl_add_u32 v6, v6, 2, v5\n" \
    "v_lshlrev_b32 v6, 1, v6\n" \
    "v_and_b32 v7, 3, v1\n" \
    "v_lshrrev_b32 v7, 1, v7\n" \
    "v_xor_b32 v6, v6, v7\n" \
    "s_lshl_b32 s64, s61, 2\n" \
    "v_xor_b32 v6, s64, v6\n" \
    "v_lshl_add_u32 v5, v2, 3, v5\n" \
    "v_lshlrev_b32 v5, 8, v5\n" \
    "v_lshl_add_u32 v5, v6, 4, v5\n" \
    "v_and_b32 v7, 1, v1\n" \
    "v_lshl_add_u32 v10, v7, 3, v5\n" \
    "v_xor_b32 v11, 32, v10\n" \
    "v_add_u32 v12, 0x10000, v10\n" \
    "v_add_u32 v13, 0x10000, v11\n" \
    "; ---- weight staging pipeline\n" \
    "s_lshl_b32 s64, s61, 19\n" \
    "s_add_u32 s88, s40, s64\n" \
    "s_addc_u32 s89, s41, 0\n" \
    "s_lshl_b32 s67, s61, 14\n" \
    "s_mov_b32 m0, s67\n" \
    "s_add_u32 s67, s67, 1024\n" \
    "global_load_lds_dwordx4 v8, s[88:89]\n" \
    "s_add_u32 s88, s88, 32768\n" \
    "s_addc_u32 s89, s89, 0\n" \
    "s_mov_b32 m0, s67\n" \
    "s_add_u32 s67, s67, 1024\n" \
    "global_load_lds_dwordx4 v8, s[88:89]\n" \
    "s_add_u32 s88, s88, 32768\n" \
    "s_addc_u32 s89, s89, 0\n" \
    "s_mov_b32 m0, s67\n" \
    "s_add_u32 s67, s67, 1024\n" \
    "global_load_lds_dwordx4 v9, s[88:89]\n" \
    "s_add_u32 s88, s88, 32768\n" \
    "s_addc_u32 s89, s89, 0\n" \
    "s_mov_b32 m0, s67\n" \
    "s_add_u32 s67, s67, 1024\n" \
    "global_load_lds_dwordx4 v9, s[88:89]\n" \
    "s_add_u32 s88, s88, 32768\n" \
    "s_addc_u32 s89, s89, 0\n" \
    "s_mov_b32 m0, s67\n" \
    "s_add_u32 s67, s67, 1024\n" \
    "global_load_lds_dwordx4 v8, s[88:89]\n" \
    "s_add_u32 s88, s88, 32768\n" \
    "s_addc_u32 s89, s89, 0\n" \
    "s_mov_b32 m0, s67\n" \
    "s_add_u32 s67, s67, 1024\n" \
    "global_load_lds_dwordx4 v8, s[88:89]\n" \
    "s_add_u32 s88, s88, 32768\n" \
    "s_addc_u32 s89, s89, 0\n" \
    "s_mov_b32 m0, s67\n" \
    "s_add_u32 s67, s67, 1024\n" \
    "global_load_lds_dwordx4 v9, s[88:89]\n" \
    "s_add_u32 s88, s88, 32768\n" \
    "s_addc_u32 s89, s89, 0\n" \
    "s_mov_b32 m0, s67\n" \
    "s_add_u32 s67, s67, 1024\n" \
    "global_load_lds_dwordx4 v9, s[88:89]\n" \
    "s_add_u32 s88, s88, 32768\n" \
    "s_addc_u32 s89, s89, 0\n" \
    "s_mov_b32 m0, s67\n" \
    "s_add_u32 s67, s67, 1024\n" \
    "global_load_lds_dwordx4 v8, s[88:89]\n" \
    "s_add_u32 s88, s88, 32768\n" \
    "s_addc_u32 s89, s89, 0\n" \
    "s_mov_b32 m0, s67\n" \
    "s_add_u32 s67, s67, 1024\n" \
    "global_load_lds_dwordx4 v8, s[88:89]\n" \
    "s_add_u32 s88, s88, 32768\n" \
    "s_addc_u32 s89, s89, 0\n" \
    "s_mov_b32 m0, s67\n" \
    "s_add_u32 s67, s67, 1024\n" \
    "global_load_lds_dwordx4 v9, s[88:89]\n" \
    "s_add_u32 s88, s88, 32768\n" \
    "s_addc_u32 s89, s89, 0\n" \
    "s_mov_b32 m0, s67\n" \
    "s_add_u32 s67, s67, 1024\n" \
    "global_load_lds_dwordx4 v9, s[88:89]\n" \
    "s_add_u32 s88, s88, 32768\n" \
    "s_addc_u32 s89, s89, 0\n" \
    "s_mov_b32 m0, s67\n" \
    "s_add_u32 s67, s67, 1024\n" \
    "global_load_lds_dwordx4 v8, s[88:89]\n" \
    "s_add_u32 s88, s88, 32768\n" \
    "s_addc_u32 s89, s89, 0\n" \
    "s_mov_b32 m0, s67\n" \
    "s_add_u32 s67, s67, 1024\n" \
    "global_load_lds_dwordx4 v8, s[88:89]\n" \
    "s_add_u32 s88, s88, 32768\n" \
    "s_addc_u32 s89, s89, 0\n" \
    "s_mov_b32 m0, s67\n" \
    "s_add_u32 s67, s67, 1024\n" \
    "global_load_lds_dwordx4 v9, s[88:89]\n" \
    "s_add_u32 s88, s88, 32768\n" \
    "s_addc_u32 s89, s89, 0\n" \
    "s_mov_b32 m0, s67\n" \
    "s_add_u32 s67, s67, 1024\n" \
    "global_load_lds_dwordx4 v9, s[88:89]\n" \
    "s_lshl_b32 s64, s61, 19\n" \
    "s_add_u32 s88, s40, s64\n" \
    "s_addc_u32 s89, s41, 0\n" \
    "s_add_u32 s88, s88, 2097152\n" \
    "s_addc_u32 s89, s89, 0\n" \
    "s_lshl_b32 s67, s61, 14\n" \
    "s_add_u32 s67, s67, 0x10000\n" \
    "s_mov_b32 m0, s67\n" \
    "s_add_u32 s67, s67, 1024\n" \
    "global_load_lds_dwordx4 v8, s[88:89]\n" \
    "s_add_u32 s88, s88, 32768\n" \
    "s_addc_u32 s89, s89, 0\n" \
    "s_mov_b32 m0, s67\n" \
    "s_add_u32 s67, s67, 1024\n" \
    "global_load_lds_dwordx4 v8, s[88:89]\n" \
    "s_add_u32 s88, s88, 32768\n" \
    "s_addc_u32 s89, s89, 0\n" \
    "s_mov_b32 m0, s67\n" \
    "s_add_u32 s67, s67, 1024\n" \
    "global_load_lds_dwordx4 v9, s[88:89]\n" \
    "s_add_u32 s88, s88, 32768\n" \
    "s_addc_u32 s89, s89, 0\n" \
    "s_mov_b32 m0, s67\n" \
    "s_add_u32 s67, s67, 1024\n" \
    "global_load_lds_dwordx4 v9, s[88:89]\n" \
    "s_add_u32 s88, s88, 32768\n" \
    "s_addc_u32 s89, s89, 0\n" \
    "s_mov_b32 m0, s67\n" \
    "s_add_u32 s67, s67, 1024\n" \
    "global_load_lds_dwordx4 v8, s[88:89]\n" \
    "s_add_u32 s88, s88, 32768\n" \
    "s_addc_u32 s89, s89, 0\n" \
    "s_mov_b32 m0, s67\n" \
    "s_add_u32 s67, s67, 1024\n" \
    "global_load_lds_dwordx4 v8, s[88:89]\n" \
    "s_add_u32 s88, s88, 32768\n" \
    "s_addc_u32 s89, s89, 0\n" \
    "s_mov_b32 m0, s67\n" \
    "s_add_u32 s67, s67, 1024\n" \
    "global_load_lds_dwordx4 v9, s[88:89]\n" \
    "s_add_u32 s88, s88, 32768\n" \
    "s_addc_u32 s89, s89, 0\n" \
    "s_mov_b32 m0, s67\n" \
    "s_add_u32 s67, s67, 1024\n" \
    "global_load_lds_dwordx4 v9, s[88:89]\n" \
    "s_add_u32 s88, s88, 32768\n" \
    "s_addc_u32 s89, s89, 0\n" \
    "s_mov_b32 m0, s67\n" \
    "s_add_u32 s67, s67, 1024\n" \
    "global_load_lds_dwordx4 v8, s[88:89]\n" \
    "s_add_u32 s88, s88, 32768\n" \
    "s_addc_u32 s89, s89, 0\n" \
    "s_mov_b32 m0, s67\n" \
    "s_add_u32 s67, s67, 1024\n" \
    "global_load_lds_dwordx4 v8, s[88:89]\n" \
    "s_add_u32 s88, s88, 32768\n" \
    "s_addc_u32 s89, s89, 0\n" \
    "s_mov_b32 m0, s67\n" \
    "s_add_u32 s67, s67, 1024\n" \
    "global_load_lds_dwordx4 v9, s[88:89]\n" \
    "s_add_u32 s88, s88, 32768\n" \
    "s_addc_u32 s89, s89, 0\n" \
    "s_mov_b32 m0, s67\n" \
    "s_add_u32 s67, s67, 1024\n" \
    "global_load_lds_dwordx4 v9, s[88:89]\n" \
    "s_add_u32 s88, s88, 32768\n" \
    "s_addc_u32 s89, s89, 0\n" \
    "s_mov_b32 m0, s67\n" \
    "s_add_u32 s67, s67, 1024\n" \
    "global_load_lds_dwordx4 v8, s[88:89]\n" \
    "s_add_u32 s88, s88, 32768\n" \
    "s_addc_u32 s89, s89, 0\n" \
    "s_mov_b32 m0, s67\n" \
    "s_add_u32 s67, s67, 1024\n" \
    "global_load_lds_dwordx4 v8, s[88:89]\n" \
    "s_add_u32 s88, s88, 32768\n" \
    "s_addc_u32 s89, s89, 0\n" \
    "s_mov_b32 m0, s67\n" \
    "s_add_u32 s67, s67, 1024\n" \
    "global_load_lds_dwordx4 v9, s[88:89]\n" \
    "s_add_u32 s88, s88, 32768\n" \
    "s_addc_u32 s89, s89, 0\n" \
    "s_mov_b32 m0, s67\n" \
    "s_add_u32 s67, s67, 1024\n" \
    "global_load_lds_dwordx4 v9, s[88:89]\n" \
    "s_waitcnt vmcnt(16)\n" \
    "s_barrier\n" \
    "ds_read_b64_tr_b16 a[0:1], v10 offset:0\n" \
    "ds_read_b64_tr_b16 a[2:3], v10 offset:1024\n" \
    "ds_read_b64_tr_b16 a[4:5], v10 offset:8192\n" \
    "ds_read_b64_tr_b16 a[6:7], v10 offset:9216\n" \
    "ds_read_b64_tr_b16 a[8:9], v10 offset:16384\n" \
    "ds_read_b64_tr_b16 a[10:11], v10 offset:17408\n" \
    "ds_read_b64_tr_b16 a[12:13], v10 offset:24576\n" \
    "ds_read_b64_tr_b16 a[14:15], v10 offset:25600\n" \
    "ds_read_b64_tr_b16 a[16:17], v10 offset:32768\n" \
    "ds_read_b64_tr_b16 a[18:19], v10 offset:33792\n" \
    "ds_read_b64_tr_b16 a[20:21], v10 offset:40960\n" \
    "ds_read_b64_tr_b16 a[22:23], v10 offset:41984\n" \
    "ds_read_b64_tr_b16 a[24:25], v10 offset:49152\n" \
    "ds_read_b64_tr_b16 a[26:27], v10 offset:50176\n" \
    "ds_read_b64_tr_b16 a[28:29], v10 offset:57344\n" \
    "ds_read_b64_tr_b16 a[30:31], v10 offset:58368\n" \
    "ds_read_b64_tr_b16 a[64:65], v11 offset:0\n" \
    "ds_read_b64_tr_b16 a[66:67], v11 offset:1024\n" \
    "ds_read_b64_tr_b16 a[68:69], v11 offset:8192\n" \
    "ds_read_b64_tr_b16 a[70:71], v11 offset:9216\n" \
    "ds_read_b64_tr_b16 a[72:73], v11 offset:16384\n" \
    "ds_read_b64_tr_b16 a[74:75], v11 offset:17408\n" \
    "ds_read_b64_tr_b16 a[76:77], v11 offset:24576\n" \
    "ds_read_b64_tr_b16 a[78:79], v11 offset:25600\n" \
    "ds_read_b64_tr_b16 a[80:81], v11 offset:32768\n" \
    "ds_read_b64_tr_b16 a[82:83], v11 offset:33792\n" \
    "ds_read_b64_tr_b16 a[84:85], v11 offset:40960\n" \
    "ds_read_b64_tr_b16 a[86:87], v11 offset:41984\n" \
    "ds_read_b64_tr_b16 a[88:89], v11 offset:49152\n" \
    "ds_read_b64_tr_b16 a[90:91], v11 offset:50176\n" \
    "ds_read_b64_tr_b16 a[92:93], v11 offset:57344\n" \
    "ds_read_b64_tr_b16 a[94:95], v11 offset:58368\n" \
    "s_waitcnt lgkmcnt(0)\n" \
    "s_barrier\n" \
    "s_lshl_b32 s64, s61, 19\n" \
    "s_add_u32 s88, s42, s64\n" \
    "s_addc_u32 s89, s43, 0\n" \
    "s_lshl_b32 s67, s61, 14\n" \
    "s_mov_b32 m0, s67\n" \
    "s_add_u32 s67, s67, 1024\n" \
    "global_load_lds_dwordx4 v8, s[88:89]\n" \
    "s_add_u32 s88, s88, 32768\n" \
    "s_addc_u32 s89, s89, 0\n" \
    "s_mov_b32 m0, s67\n" \
    "s_add_u32 s67, s67, 1024\n" \
    "global_load_lds_dwordx4 v8, s[88:89]\n" \
    "s_add_u32 s88, s88, 32768\n" \
    "s_addc_u32 s89, s89, 0\n" \
    "s_mov_b32 m0, s67\n" \
    "s_add_u32 s67, s67, 1024\n" \
    "global_load_lds_dwordx4 v9, s[88:89]\n" \
    "s_add_u32 s88, s88, 32768\n" \
    "s_addc_u32 s89, s89, 0\n" \
    "s_mov_b32 m0, s67\n" \
    "s_add_u32 s67, s67, 1024\n" \
    "global_load_lds_dwordx4 v9, s[88:89]\n" \
    "s_add_u32 s88, s88, 32768\n" \
    "s_addc_u32 s89, s89, 0\n" \
    "s_mov_b32 m0, s67\n" \
    "s_add_u32 s67, s67, 1024\n" \
    "global_load_lds_dwordx4 v8, s[88:89]\n" \
    "s_add_u32 s88, s88, 32768\n" \
    "s_addc_u32 s89, s89, 0\n" \
    "s_mov_b32 m0, s67\n" \
    "s_add_u32 s67, s67, 1024\n" \
    "global_load_lds_dwordx4 v8, s[88:89]\n" \
    "s_add_u32 s88, s88, 32768\n" \
    "s_addc_u32 s89, s89, 0\n" \
    "s_mov_b32 m0, s67\n" \
    "s_add_u32 s67, s67, 1024\n" \
    "global_load_lds_dwordx4 v9, s[88:89]\n" \
    "s_add_u32 s88, s88, 32768\n" \
    "s_addc_u32 s89, s89, 0\n" \
    "s_mov_b32 m0, s67\n" \
    "s_add_u32 s67, s67, 1024\n" \
    "global_load_lds_dwordx4 v9, s[88:89]\n" \
    "s_add_u32 s88, s88, 32768\n" \
    "s_addc_u32 s89, s89, 0\n" \
    "s_mov_b32 m0, s67\n" \
    "s_add_u32 s67, s67, 1024\n" \
    "global_load_lds_dwordx4 v8, s[88:89]\n" \
    "s_add_u32 s88, s88, 32768\n" \
    "s_addc_u32 s89, s89, 0\n" \
    "s_mov_b32 m0, s67\n" \
    "s_add_u32 s67, s67, 1024\n" \
    "global_load_lds_dwordx4 v8, s[88:89]\n" \
    "s_add_u32 s88, s88, 32768\n" \
    "s_addc_u32 s89, s89, 0\n" \
    "s_mov_b32 m0, s67\n" \
    "s_add_u32 s67, s67, 1024\n" \
    "global_load_lds_dwordx4 v9, s[88:89]\n" \
    "s_add_u32 s88, s88, 32768\n" \
    "s_addc_u32 s89, s89, 0\n" \
    "s_mov_b32 m0, s67\n" \
    "s_add_u32 s67, s67, 1024\n" \
    "global_load_lds_dwordx4 v9, s[88:89]\n" \
    "s_add_u32 s88, s88, 32768\n" \
    "s_addc_u32 s89, s89, 0\n" \
    "s_mov_b32 m0, s67\n" \
    "s_add_u32 s67, s67, 1024\n" \
    "global_load_lds_dwordx4 v8, s[88:89]\n" \
    "s_add_u32 s88, s88, 32768\n" \
    "s_addc_u32 s89, s89, 0\n" \
    "s_mov_b32 m0, s67\n" \
    "s_add_u32 s67, s67, 1024\n" \
    "global_load_lds_dwordx4 v8, s[88:89]\n" \
    "s_add_u32 s88, s88, 32768\n" \
    "s_addc_u32 s89, s89, 0\n" \
    "s_mov_b32 m0, s67\n" \
    "s_add_u32 s67, s67, 1024\n" \
    "global_load_lds_dwordx4 v9, s[88:89]\n" \
    "s_add_u32 s88, s88, 32768\n" \
    "s_addc_u32 s89, s89, 0\n" \
    "s_mov_b32 m0, s67\n" \
    "s_add_u32 s67, s67, 1024\n" \
    "global_load_lds_dwordx4 v9, s[88:89]\n" \
    "s_waitcnt vmcnt(16)\n" \
    "s_barrier\n" \
    "ds_read_b64_tr_b16 a[32:33], v12 offset:0\n" \
    "ds_read_b64_tr_b16 a[34:35], v12 offset:1024\n" \
    "ds_read_b64_tr_b16 a[36:37], v12 offset:8192\n" \
    "ds_read_b64_tr_b16 a[38:39], v12 offset:9216\n" \
    "ds_read_b64_tr_b16 a[40:41], v12 offset:16384\n" \
    "ds_read_b64_tr_b16 a[42:43], v12 offset:17408\n" \
    "ds_read_b64_tr_b16 a[44:45], v12 offset:24576\n" \
    "ds_read_b64_tr_b16 a[46:47], v12 offset:25600\n" \
    "ds_read_b64_tr_b16 a[48:49], v12 offset:32768\n" \
    "ds_read_b64_tr_b16 a[50:51], v12 offset:33792\n" \
    "ds_read_b64_tr_b16 a[52:53], v12 offset:40960\n" \
    "ds_read_b64_tr_b16 a[54:55], v12 offset:41984\n" \
    "ds_read_b64_tr_b16 a[56:57], v12 offset:49152\n" \
    "ds_read_b64_tr_b16 a[58:59], v12 offset:50176\n" \
    "ds_read_b64_tr_b16 a[60:61], v12 offset:57344\n" \
    "ds_read_b64_tr_b16 a[62:63], v12 offset:58368\n" \
    "ds_read_b64_tr_b16 a[96:97], v13 offset:0\n" \
    "ds_read_b64_tr_b16 a[98:99], v13 offset:1024\n" \
    "ds_read_b64_tr_b16 a[100:101], v13 offset:8192\n" \
    "ds_read_b64_tr_b16 a[102:103], v13 offset:9216\n" \
    "ds_read_b64_tr_b16 a[104:105], v13 offset:16384\n" \
    "ds_read_b64_tr_b16 a[106:107], v13 offset:17408\n" \
    "ds_read_b64_tr_b16 a[108:109], v13 offset:24576\n" \
    "ds_read_b64_tr_b16 a[110:111], v13 offset:25600\n" \
    "ds_read_b64_tr_b16 a[112:113], v13 offset:32768\n" \
    "ds_read_b64_tr_b16 a[114:115], v13 offset:33792\n" \
    "ds_read_b64_tr_b16 a[116:117], v13 offset:40960\n" \
    "ds_read_b64_tr_b16 a[118:119], v13 offset:41984\n" \
    "ds_read_b64_tr_b16 a[120:121], v13 offset:49152\n" \
    "ds_read_b64_tr_b16 a[122:123], v13 offset:50176\n" \
    "ds_read_b64_tr_b16 a[124:125], v13 offset:57344\n" \
    "ds_read_b64_tr_b16 a[126:127], v13 offset:58368\n" \
    "s_waitcnt lgkmcnt(0)\n" \
    "s_barrier\n" \
    "s_lshl_b32 s64, s61, 19\n" \
    "s_add_u32 s88, s42, s64\n" \
    "s_addc_u32 s89, s43, 0\n" \
    "s_add_u32 s88, s88, 2097152\n" \
    "s_addc_u32 s89, s89, 0\n" \
    "s_lshl_b32 s67, s61, 14\n" \
    "s_add_u32 s67, s67, 0x10000\n" \
    "s_mov_b32 m0, s67\n" \
    "s_add_u32 s67, s67, 1024\n" \
    "global_load_lds_dwordx4 v8, s[88:89]\n" \
    "s_add_u32 s88, s88, 32768\n" \
    "s_addc_u32 s89, s89, 0\n" \
    "s_mov_b32 m0, s67\n" \
    "s_add_u32 s67, s67, 1024\n" \
    "global_load_lds_dwordx4 v8, s[88:89]\n" \
    "s_add_u32 s88, s88, 32768\n" \
    "s_addc_u32 s89, s89, 0\n" \
    "s_mov_b32 m0, s67\n" \
    "s_add_u32 s67, s67, 1024\n" \
    "global_load_lds_dwordx4 v9, s[88:89]\n" \
    "s_add_u32 s88, s88, 32768\n" \
    "s_addc_u32 s89, s89, 0\n" \
    "s_mov_b32 m0, s67\n" \
    "s_add_u32 s67, s67, 1024\n" \
    "global_load_lds_dwordx4 v9, s[88:89]\n" \
    "s_add_u32 s88, s88, 32768\n" \
    "s_addc_u32 s89, s89, 0\n" \
    "s_mov_b32 m0, s67\n" \
    "s_add_u32 s67, s67, 1024\n" \
    "global_load_lds_dwordx4 v8, s[88:89]\n" \
    "s_add_u32 s88, s88, 32768\n" \
    "s_addc_u32 s89, s89, 0\n" \
    "s_mov_b32 m0, s67\n" \
    "s_add_u32 s67, s67, 1024\n" \
    "global_load_lds_dwordx4 v8, s[88:89]\n" \
    "s_add_u32 s88, s88, 32768\n" \
    "s_addc_u32 s89, s89, 0\n" \
    "s_mov_b32 m0, s67\n" \
    "s_add_u32 s67, s67, 1024\n" \
    "global_load_lds_dwordx4 v9, s[88:89]\n" \
    "s_add_u32 s88, s88, 32768\n" \
    "s_addc_u32 s89, s89, 0\n" \
    "s_mov_b32 m0, s67\n" \
    "s_add_u32 s67, s67, 1024\n" \
    "global_load_lds_dwordx4 v9, s[88:89]\n" \
    "s_add_u32 s88, s88, 32768\n" \
    "s_addc_u32 s89, s89, 0\n" \
    "s_mov_b32 m0, s67\n" \
    "s_add_u32 s67, s67, 1024\n" \
    "global_load_lds_dwordx4 v8, s[88:89]\n" \
    "s_add_u32 s88, s88, 32768\n" \
    "s_addc_u32 s89, s89, 0\n" \
    "s_mov_b32 m0, s67\n" \
    "s_add_u32 s67, s67, 1024\n" \
    "global_load_lds_dwordx4 v8, s[88:89]\n" \
    "s_add_u32 s88, s88, 32768\n" \
    "s_addc_u32 s89, s89, 0\n" \
    "s_mov_b32 m0, s67\n" \
    "s_add_u32 s67, s67, 1024\n" \
    "global_load_lds_dwordx4 v9, s[88:89]\n" \
    "s_add_u32 s88, s88, 32768\n" \
    "s_addc_u32 s89, s89, 0\n" \
    "s_mov_b32 m0, s67\n" \
    "s_add_u32 s67, s67, 1024\n" \
    "global_load_lds_dwordx4 v9, s[88:89]\n" \
    "s_add_u32 s88, s88, 32768\n" \
    "s_addc_u32 s89, s89, 0\n" \
    "s_mov_b32 m0, s67\n" \
    "s_add_u32 s67, s67, 1024\n" \
    "global_load_lds_dwordx4 v8, s[88:89]\n" \
    "s_add_u32 s88, s88, 32768\n" \
    "s_addc_u32 s89, s89, 0\n" \
    "s_mov_b32 m0, s67\n" \
    "s_add_u32 s67, s67, 1024\n" \
    "global_load_lds_dwordx4 v8, s[88:89]\n" \
    "s_add_u32 s88, s88, 32768\n" \
    "s_addc_u32 s89, s89, 0\n" \
    "s_mov_b32 m0, s67\n" \
    "s_add_u32 s67, s67, 1024\n" \
    "global_load_lds_dwordx4 v9, s[88:89]\n" \
    "s_add_u32 s88, s88, 32768\n" \
    "s_addc_u32 s89, s89, 0\n" \
    "s_mov_b32 m0, s67\n" \
    "s_add_u32 s67, s67, 1024\n" \
    "global_load_lds_dwordx4 v9, s[88:89]\n" \
    "s_waitcnt vmcnt(16)\n" \
    "s_barrier\n" \
    "ds_read_b64_tr_b16 a[128:129], v10 offset:0\n" \
    "ds_read_b64_tr_b16 a[130:131], v10 offset:1024\n" \
    "ds_read_b64_tr_b16 a[132:133], v10 offset:8192\n" \
    "ds_read_b64_tr_b16 a[134:135], v10 offset:9216\n" \
    "ds_read_b64_tr_b16 a[136:137], v10 offset:16384\n" \
    "ds_read_b64_tr_b16 a[138:139], v10 offset:17408\n" \
    "ds_read_b64_tr_b16 a[140:141], v10 offset:24576\n" \
    "ds_read_b64_tr_b16 a[142:143], v10 offset:25600\n" \
    "ds_read_b64_tr_b16 a[144:145], v10 offset:32768\n" \
    "ds_read_b64_tr_b16 a[146:147], v10 offset:33792\n" \
    "ds_read_b64_tr_b16 a[148:149], v10 offset:40960\n" \
    "ds_read_b64_tr_b16 a[150:151], v10 offset:41984\n" \
    "ds_read_b64_tr_b16 a[152:153], v10 offset:49152\n" \
    "ds_read_b64_tr_b16 a[154:155], v10 offset:50176\n" \
    "ds_read_b64_tr_b16 a[156:157], v10 offset:57344\n" \
    "ds_read_b64_tr_b16 a[158:159], v10 offset:58368\n" \
    "ds_read_b64_tr_b16 a[192:193], v11 offset:0\n" \
    "ds_read_b64_tr_b16 a[194:195], v11 offset:1024\n" \
    "ds_read_b64_tr_b16 a[196:197], v11 offset:8192\n" \
    "ds_read_b64_tr_b16 a[198:199], v11 offset:9216\n" \
    "ds_read_b64_tr_b16 a[200:201], v11 offset:16384\n" \
    "ds_read_b64_tr_b16 a[202:203], v11 offset:17408\n" \
    "ds_read_b64_tr_b16 a[204:205], v11 offset:24576\n" \
    "ds_read_b64_tr_b16 a[206:207], v11 offset:25600\n" \
    "ds_read_b64_tr_b16 a[208:209], v11 offset:32768\n" \
    "ds_read_b64_tr_b16 a[210:211], v11 offset:33792\n" \
    "ds_read_b64_tr_b16 a[212:213], v11 offset:40960\n" \
    "ds_read_b64_tr_b16 a[214:215], v11 offset:41984\n" \
    "ds_read_b64_tr_b16 a[216:217], v11 offset:49152\n" \
    "ds_read_b64_tr_b16 a[218:219], v11 offset:50176\n" \
    "ds_read_b64_tr_b16 a[220:221], v11 offset:57344\n" \
    "ds_read_b64_tr_b16 a[222:223], v11 offset:58368\n" \
    "s_waitcnt lgkmcnt(0)\n" \
    "s_barrier\n" \
    "s_lshl_b32 s64, s61, 19\n" \
    "s_add_u32 s88, s44, s64\n" \
    "s_addc_u32 s89, s45, 0\n" \
    "s_lshl_b32 s67, s61, 14\n" \
    "s_mov_b32 m0, s67\n" \
    "s_add_u32 s67, s67, 1024\n" \
    "global_load_lds_dwordx4 v8, s[88:89]\n" \
    "s_add_u32 s88, s88, 32768\n" \
    "s_addc_u32 s89, s89, 0\n" \
    "s_mov_b32 m0, s67\n" \
    "s_add_u32 s67, s67, 1024\n" \
    "global_load_lds_dwordx4 v8, s[88:89]\n" \
    "s_add_u32 s88, s88, 32768\n" \
    "s_addc_u32 s89, s89, 0\n" \
    "s_mov_b32 m0, s67\n" \
    "s_add_u32 s67, s67, 1024\n" \
    "global_load_lds_dwordx4 v9, s[88:89]\n" \
    "s_add_u32 s88, s88, 32768\n" \
    "s_addc_u32 s89, s89, 0\n" \
    "s_mov_b32 m0, s67\n" \
    "s_add_u32 s67, s67, 1024\n" \
    "global_load_lds_dwordx4 v9, s[88:89]\n" \
    "s_add_u32 s88, s88, 32768\n" \
    "s_addc_u32 s89, s89, 0\n" \
    "s_mov_b32 m0, s67\n" \
    "s_add_u32 s67, s67, 1024\n" \
    "global_load_lds_dwordx4 v8, s[88:89]\n" \
    "s_add_u32 s88, s88, 32768\n" \
    "s_addc_u32 s89, s89, 0\n" \
    "s_mov_b32 m0, s67\n" \
    "s_add_u32 s67, s67, 1024\n" \
    "global_load_lds_dwordx4 v8, s[88:89]\n" \
    "s_add_u32 s88, s88, 32768\n" \
    "s_addc_u32 s89, s89, 0\n" \
    "s_mov_b32 m0, s67\n" \
    "s_add_u32 s67, s67, 1024\n" \
    "global_load_lds_dwordx4 v9, s[88:89]\n" \
    "s_add_u32 s88, s88, 32768\n" \
    "s_addc_u32 s89, s89, 0\n" \
    "s_mov_b32 m0, s67\n" \
    "s_add_u32 s67, s67, 1024\n" \
    "global_load_lds_dwordx4 v9, s[88:89]\n" \
    "s_add_u32 s88, s88, 32768\n" \
    "s_addc_u32 s89, s89, 0\n" \
    "s_mov_b32 m0, s67\n" \
    "s_add_u32 s67, s67, 1024\n" \
    "global_load_lds_dwordx4 v8, s[88:89]\n" \
    "s_add_u32 s88, s88, 32768\n" \
    "s_addc_u32 s89, s89, 0\n" \
    "s_mov_b32 m0, s67\n" \
    "s_add_u32 s67, s67, 1024\n" \
    "global_load_lds_dwordx4 v8, s[88:89]\n" \
    "s_add_u32 s88, s88, 32768\n" \
    "s_addc_u32 s89, s89, 0\n" \
    "s_mov_b32 m0, s67\n" \
    "s_add_u32 s67, s67, 1024\n" \
    "global_load_lds_dwordx4 v9, s[88:89]\n" \
    "s_add_u32 s88, s88, 32768\n" \
    "s_addc_u32 s89, s89, 0\n" \
    "s_mov_b32 m0, s67\n" \
    "s_add_u32 s67, s67, 1024\n" \
    "global_load_lds_dwordx4 v9, s[88:89]\n" \
    "s_add_u32 s88, s88, 32768\n" \
    "s_addc_u32 s89, s89, 0\n" \
    "s_mov_b32 m0, s67\n" \
    "s_add_u32 s67, s67, 1024\n" \
    "global_load_lds_dwordx4 v8, s[88:89]\n" \
    "s_add_u32 s88, s88, 32768\n" \
    "s_addc_u32 s89, s89, 0\n" \
    "s_mov_b32 m0, s67\n" \
    "s_add_u32 s67, s67, 1024\n" \
    "global_load_lds_dwordx4 v8, s[88:89]\n" \
    "s_add_u32 s88, s88, 32768\n" \
    "s_addc_u32 s89, s89, 0\n" \
    "s_mov_b32 m0, s67\n" \
    "s_add_u32 s67, s67, 1024\n" \
    "global_load_lds_dwordx4 v9, s[88:89]\n" \
    "s_add_u32 s88, s88, 32768\n" \
    "s_addc_u32 s89, s89, 0\n" \
    "s_mov_b32 m0, s67\n" \
    "s_add_u32 s67, s67, 1024\n" \
    "global_load_lds_dwordx4 v9, s[88:89]\n" \
    "s_waitcnt vmcnt(16)\n" \
    "s_barrier\n" \
    "ds_read_b64_tr_b16 a[160:161], v12 offset:0\n" \
    "ds_read_b64_tr_b16 a[162:163], v12 offset:1024\n" \
    "ds_read_b64_tr_b16 a[164:165], v12 offset:8192\n" \
    "ds_read_b64_tr_b16 a[166:167], v12 offset:9216\n" \
    "ds_read_b64_tr_b16 a[168:169], v12 offset:16384\n" \
    "ds_read_b64_tr_b16 a[170:171], v12 offset:17408\n" \
    "ds_read_b64_tr_b16 a[172:173], v12 offset:24576\n" \
    "ds_read_b64_tr_b16 a[174:175], v12 offset:25600\n" \
    "ds_read_b64_tr_b16 a[176:177], v12 offset:32768\n" \
    "ds_read_b64_tr_b16 a[178:179], v12 offset:33792\n" \
    "ds_read_b64_tr_b16 a[180:181], v12 offset:40960\n" \
    "ds_read_b64_tr_b16 a[182:183], v12 offset:41984\n" \
    "ds_read_b64_tr_b16 a[184:185], v12 offset:49152\n" \
    "ds_read_b64_tr_b16 a[186:187], v12 offset:50176\n" \
    "ds_read_b64_tr_b16 a[188:189], v12 offset:57344\n" \
    "ds_read_b64_tr_b16 a[190:191], v12 offset:58368\n" \
    "ds_read_b64_tr_b16 a[224:225], v13 offset:0\n" \
    "ds_read_b64_tr_b16 a[226:227], v13 offset:1024\n" \
    "ds_read_b64_tr_b16 a[228:229], v13 offset:8192\n" \
    "ds_read_b64_tr_b16 a[230:231], v13 offset:9216\n" \
    "ds_read_b64_tr_b16 a[232:233], v13 offset:16384\n" \
    "ds_read_b64_tr_b16 a[234:235], v13 offset:17408\n" \
    "ds_read_b64_tr_b16 a[236:237], v13 offset:24576\n" \
    "ds_read_b64_tr_b16 a[238:239], v13 offset:25600\n" \
    "ds_read_b64_tr_b16 a[240:241], v13 offset:32768\n" \
    "ds_read_b64_tr_b16 a[242:243], v13 offset:33792\n" \
    "ds_read_b64_tr_b16 a[244:245], v13 offset:40960\n" \
    "ds_read_b64_tr_b16 a[246:247], v13 offset:41984\n" \
    "ds_read_b64_tr_b16 a[248:249], v13 offset:49152\n" \
    "ds_read_b64_tr_b16 a[250:251], v13 offset:50176\n" \
    "ds_read_b64_tr_b16 a[252:253], v13 offset:57344\n" \
    "ds_read_b64_tr_b16 a[254:255], v13 offset:58368\n" \
    "s_waitcnt lgkmcnt(0)\n" \
    "s_barrier\n" \
    "s_lshl_b32 s64, s61, 19\n" \
    "s_add_u32 s88, s44, s64\n" \
    "s_addc_u32 s89, s45, 0\n" \
    "s_add_u32 s88, s88, 2097152\n" \
    "s_addc_u32 s89, s89, 0\n" \
    "s_lshl_b32 s67, s61, 14\n" \
    "s_add_u32 s67, s67, 0x10000\n" \
    "s_mov_b32 m0, s67\n" \
    "s_add_u32 s67, s67, 1024\n" \
    "global_load_lds_dwordx4 v8, s[88:89]\n" \
    "s_add_u32 s88, s88, 32768\n" \
    "s_addc_u32 s89, s89, 0\n" \
    "s_mov_b32 m0, s67\n" \
    "s_add_u32 s67, s67, 1024\n" \
    "global_load_lds_dwordx4 v8, s[88:89]\n" \
    "s_add_u32 s88, s88, 32768\n" \
    "s_addc_u32 s89, s89, 0\n" \
    "s_mov_b32 m0, s67\n" \
    "s_add_u32 s67, s67, 1024\n" \
    "global_load_lds_dwordx4 v9, s[88:89]\n" \
    "s_add_u32 s88, s88, 32768\n" \
    "s_addc_u32 s89, s89, 0\n" \
    "s_mov_b32 m0, s67\n" \
    "s_add_u32 s67, s67, 1024\n" \
    "global_load_lds_dwordx4 v9, s[88:89]\n" \
    "s_add_u32 s88, s88, 32768\n" \
    "s_addc_u32 s89, s89, 0\n" \
    "s_mov_b32 m0, s67\n" \
    "s_add_u32 s67, s67, 1024\n" \
    "global_load_lds_dwordx4 v8, s[88:89]\n" \
    "s_add_u32 s88, s88, 32768\n" \
    "s_addc_u32 s89, s89, 0\n" \
    "s_mov_b32 m0, s67\n" \
    "s_add_u32 s67, s67, 1024\n" \
    "global_load_lds_dwordx4 v8, s[88:89]\n" \
    "s_add_u32 s88, s88, 32768\n" \
    "s_addc_u32 s89, s89, 0\n" \
    "s_mov_b32 m0, s67\n" \
    "s_add_u32 s67, s67, 1024\n" \
    "global_load_lds_dwordx4 v9, s[88:89]\n" \
    "s_add_u32 s88, s88, 32768\n" \
    "s_addc_u32 s89, s89, 0\n" \
    "s_mov_b32 m0, s67\n" \
    "s_add_u32 s67, s67, 1024\n" \
    "global_load_lds_dwordx4 v9, s[88:89]\n" \
    "s_add_u32 s88, s88, 32768\n" \
    "s_addc_u32 s89, s89, 0\n" \
    "s_mov_b32 m0, s67\n" \
    "s_add_u32 s67, s67, 1024\n" \
    "global_load_lds_dwordx4 v8, s[88:89]\n" \
    "s_add_u32 s88, s88, 32768\n" \
    "s_addc_u32 s89, s89, 0\n" \
    "s_mov_b32 m0, s67\n" \
    "s_add_u32 s67, s67, 1024\n" \
    "global_load_lds_dwordx4 v8, s[88:89]\n" \
    "s_add_u32 s88, s88, 32768\n" \
    "s_addc_u32 s89, s89, 0\n" \
    "s_mov_b32 m0, s67\n" \
    "s_add_u32 s67, s67, 1024\n" \
    "global_load_lds_dwordx4 v9, s[88:89]\n" \
    "s_add_u32 s88, s88, 32768\n" \
    "s_addc_u32 s89, s89, 0\n" \
    "s_mov_b32 m0, s67\n" \
    "s_add_u32 s67, s67, 1024\n" \
    "global_load_lds_dwordx4 v9, s[88:89]\n" \
    "s_add_u32 s88, s88, 32768\n" \
    "s_addc_u32 s89, s89, 0\n" \
    "s_mov_b32 m0, s67\n" \
    "s_add_u32 s67, s67, 1024\n" \
    "global_load_lds_dwordx4 v8, s[88:89]\n" \
    "s_add_u32 s88, s88, 32768\n" \
    "s_addc_u32 s89, s89, 0\n" \
    "s_mov_b32 m0, s67\n" \
    "s_add_u32 s67, s67, 1024\n" \
    "global_load_lds_dwordx4 v8, s[88:89]\n" \
    "s_add_u32 s88, s88, 32768\n" \
    "s_addc_u32 s89, s89, 0\n" \
    "s_mov_b32 m0, s67\n" \
    "s_add_u32 s67, s67, 1024\n" \
    "global_load_lds_dwordx4 v9, s[88:89]\n" \
    "s_add_u32 s88, s88, 32768\n" \
    "s_addc_u32 s89, s89, 0\n" \
    "s_mov_b32 m0, s67\n" \
    "s_add_u32 s67, s67, 1024\n" \
    "global_load_lds_dwordx4 v9, s[88:89]\n" \
    "s_waitcnt vmcnt(16)\n" \
    "s_barrier\n" \
    "ds_read_b64_tr_b16 v[128:129], v10 offset:0\n" \
    "ds_read_b64_tr_b16 v[130:131], v10 offset:1024\n" \
    "ds_read_b64_tr_b16 v[132:133], v10 offset:8192\n" \
    "ds_read_b64_tr_b16 v[134:135], v10 offset:9216\n" \
    "ds_read_b64_tr_b16 v[136:137], v10 offset:16384\n" \
    "ds_read_b64_tr_b16 v[138:139], v10 offset:17408\n" \
    "ds_read_b64_tr_b16 v[140:141], v10 offset:24576\n" \
    "ds_read_b64_tr_b16 v[142:143], v10 offset:25600\n" \
    "ds_read_b64_tr_b16 v[144:145], v10 offset:32768\n" \
    "ds_read_b64_tr_b16 v[146:147], v10 offset:33792\n" \
    "ds_read_b64_tr_b16 v[148:149], v10 offset:40960\n" \
    "ds_read_b64_tr_b16 v[150:151], v10 offset:41984\n" \
    "ds_read_b64_tr_b16 v[152:153], v10 offset:49152\n" \
    "ds_read_b64_tr_b16 v[154:155], v10 offset:50176\n" \
    "ds_read_b64_tr_b16 v[156:157], v10 offset:57344\n" \
    "ds_read_b64_tr_b16 v[158:159], v10 offset:58368\n" \
    "ds_read_b64_tr_b16 v[192:193], v11 offset:0\n" \
    "ds_read_b64_tr_b16 v[194:195], v11 offset:1024\n" \
    "ds_read_b64_tr_b16 v[196:197], v11 offset:8192\n" \
    "ds_read_b64_tr_b16 v[198:199], v11 offset:9216\n" \
    "ds_read_b64_tr_b16 v[200:201], v11 offset:16384\n" \
    "ds_read_b64_tr_b16 v[202:203], v11 offset:17408\n" \
    "ds_read_b64_tr_b16 v[204:205], v11 offset:24576\n" \
    "ds_read_b64_tr_b16 v[206:207], v11 offset:25600\n" \
    "ds_read_b64_tr_b16 v[208:209], v11 offset:32768\n" \
    "ds_read_b64_tr_b16 v[210:211], v11 offset:33792\n" \
    "ds_read_b64_tr_b16 v[212:213], v11 offset:40960\n" \
    "ds_read_b64_tr_b16 v[214:215], v11 offset:41984\n" \
    "ds_read_b64_tr_b16 v[216:217], v11 offset:49152\n" \
    "ds_read_b64_tr_b16 v[218:219], v11 offset:50176\n" \
    "ds_read_b64_tr_b16 v[220:221], v11 offset:57344\n" \
    "ds_read_b64_tr_b16 v[222:223], v11 offset:58368\n" \
    "s_waitcnt lgkmcnt(0)\n" \
    "s_barrier\n" \
    "v_lshrrev_b32 v5, 5, v0\n" \
    "v_and_b32 v6, 31, v0\n" \
    "v_lshlrev_b32 v7, 1, v5\n" \
    "v_xor_b32 v6, v6, v7\n" \
    "v_lshlrev_b32 v6, 4, v6\n" \
    "v_lshl_add_u32 v14, v5, 10, v6\n" \
    "s_lshl_b32 s64, s61, 15\n" \
    "s_add_u32 s88, s46, s64\n" \
    "s_addc_u32 s89, s47, 0\n" \
    "s_lshl_b32 s67, s61, 14\n" \
    "v_xor_b32 v15, 0, v14\n" \
    "s_mov_b32 m0, s67\n" \
    "s_add_u32 s67, s67, 1024\n" \
    "global_load_lds_dwordx4 v15, s[88:89]\n" \
    "s_add_u32 s88, s88, 2048\n" \
    "s_addc_u32 s89, s89, 0\n" \
    "v_xor_b32 v15, 64, v14\n" \
    "s_mov_b32 m0, s67\n" \
    "s_add_u32 s67, s67, 1024\n" \
    "global_load_lds_dwordx4 v15, s[88:89]\n" \
    "s_add_u32 s88, s88, 2048\n" \
    "s_addc_u32 s89, s89, 0\n" \
    "v_xor_b32 v15, 0, v14\n" \
    "s_mov_b32 m0, s67\n" \
    "s_add_u32 s67, s67, 1024\n" \
    "global_load_lds_dwordx4 v15, s[88:89]\n" \
    "s_add_u32 s88, s88, 2048\n" \
    "s_addc_u32 s89, s89, 0\n" \
    "v_xor_b32 v15, 64, v14\n" \
    "s_mov_b32 m0, s67\n" \
    "s_add_u32 s67, s67, 1024\n" \
    "global_load_lds_dwordx4 v15, s[88:89]\n" \
    "s_add_u32 s88, s88, 2048\n" \
    "s_addc_u32 s89, s89, 0\n" \
    "v_xor_b32 v15, 128, v14\n" \
    "s_mov_b32 m0, s67\n" \
    "s_add_u32 s67, s67, 1024\n" \
    "global_load_lds_dwordx4 v15, s[88:89]\n" \
    "s_add_u32 s88, s88, 2048\n" \
    "s_addc_u32 s89, s89, 0\n" \
    "v_xor_b32 v15, 192, v14\n" \
    "s_mov_b32 m0, s67\n" \
    "s_add_u32 s67, s67, 1024\n" \
    "global_load_lds_dwordx4 v15, s[88:89]\n" \
    "s_add_u32 s88, s88, 2048\n" \
    "s_addc_u32 s89, s89, 0\n" \
    "v_xor_b32 v15, 128, v14\n" \
    "s_mov_b32 m0, s67\n" \
    "s_add_u32 s67, s67, 1024\n" \
    "global_load_lds_dwordx4 v15, s[88:89]\n" \
    "s_add_u32 s88, s88, 2048\n" \
    "s_addc_u32 s89, s89, 0\n" \
    "v_xor_b32 v15, 192, v14\n" \
    "s_mov_b32 m0, s67\n" \
    "s_add_u32 s67, s67, 1024\n" \
    "global_load_lds_dwordx4 v15, s[88:89]\n" \
    "s_add_u32 s88, s88, 2048\n" \
    "s_addc_u32 s89, s89, 0\n" \
    "v_xor_b32 v15, 0, v14\n" \
    "s_mov_b32 m0, s67\n" \
    "s_add_u32 s67, s67, 1024\n" \
    "global_load_lds_dwordx4 v15, s[88:89]\n" \
    "s_add_u32 s88, s88, 2048\n" \
    "s_addc_u32 s89, s89, 0\n" \
    "v_xor_b32 v15, 64, v14\n" \
    "s_mov_b32 m0, s67\n" \
    "s_add_u32 s67, s67, 1024\n" \
    "global_load_lds_dwordx4 v15, s[88:89]\n" \
    "s_add_u32 s88, s88, 2048\n" \
    "s_addc_u32 s89, s89, 0\n" \
    "v_xor_b32 v15, 0, v14\n" \
    "s_mov_b32 m0, s67\n" \
    "s_add_u32 s67, s67, 1024\n" \
    "global_load_lds_dwordx4 v15, s[88:89]\n" \
    "s_add_u32 s88, s88, 2048\n" \
    "s_addc_u32 s89, s89, 0\n" \
    "v_xor_b32 v15, 64, v14\n" \
    "s_mov_b32 m0, s67\n" \
    "s_add_u32 s67, s67, 1024\n" \
    "global_load_lds_dwordx4 v15, s[88:89]\n" \
    "s_add_u32 s88, s88, 2048\n" \
    "s_addc_u32 s89, s89, 0\n" \
    "v_xor_b32 v15, 128, v14\n" \
    "s_mov_b32 m0, s67\n" \
    "s_add_u32 s67, s67, 1024\n" \
    "global_load_lds_dwordx4 v15, s[88:89]\n" \
    "s_add_u32 s88, s88, 2048\n" \
    "s_addc_u32 s89, s89, 0\n" \
    "v_xor_b32 v15, 192, v14\n" \
    "s_mov_b32 m0, s67\n" \
    "s_add_u32 s67, s67, 1024\n" \
    "global_load_lds_dwordx4 v15, s[88:89]\n" \
    "s_add_u32 s88, s88, 2048\n" \
    "s_addc_u32 s89, s89, 0\n" \
    "v_xor_b32 v15, 128, v14\n" \
    "s_mov_b32 m0, s67\n" \
    "s_add_u32 s67, s67, 1024\n" \
    "global_load_lds_dwordx4 v15, s[88:89]\n" \
    "s_add_u32 s88, s88, 2048\n" \
    "s_addc_u32 s89, s89, 0\n" \
    "v_xor_b32 v15, 192, v14\n" \
    "s_mov_b32 m0, s67\n" \
    "s_add_u32 s67, s67, 1024\n" \
    "global_load_lds_dwordx4 v15, s[88:89]\n" \
    "s_waitcnt vmcnt(16)\n" \
    "s_barrier\n" \
    "ds_read_b64_tr_b16 v[160:161], v12 offset:0\n" \
    "ds_read_b64_tr_b16 v[162:163], v12 offset:1024\n" \
    "ds_read_b64_tr_b16 v[164:165], v12 offset:8192\n" \
    "ds_read_b64_tr_b16 v[166:167], v12 offset:9216\n" \
    "ds_read_b64_tr_b16 v[168:169], v12 offset:16384\n" \
    "ds_read_b64_tr_b16 v[170:171], v12 offset:17408\n" \
    "ds_read_b64_tr_b16 v[172:173], v12 offset:24576\n" \
    "ds_read_b64_tr_b16 v[174:175], v12 offset:25600\n" \
    "ds_read_b64_tr_b16 v[176:177], v12 offset:32768\n" \
    "ds_read_b64_tr_b16 v[178:179], v12 offset:33792\n" \
    "ds_read_b64_tr_b16 v[180:181], v12 offset:40960\n" \
    "ds_read_b64_tr_b16 v[182:183], v12 offset:41984\n" \
    "ds_read_b64_tr_b16 v[184:185], v12 offset:49152\n" \
    "ds_read_b64_tr_b16 v[186:187], v12 offset:50176\n" \
    "ds_read_b64_tr_b16 v[188:189], v12 offset:57344\n" \
    "ds_read_b64_tr_b16 v[190:191], v12 offset:58368\n" \
    "ds_read_b64_tr_b16 v[224:225], v13 offset:0\n" \
    "ds_read_b64_tr_b16 v[226:227], v13 offset:1024\n" \
    "ds_read_b64_tr_b16 v[228:229], v13 offset:8192\n" \
    "ds_read_b64_tr_b16 v[230:231], v13 offset:9216\n" \
    "ds_read_b64_tr_b16 v[232:233], v13 offset:16384\n" \
    "ds_read_b64_tr_b16 v[234:235], v13 offset:17408\n" \
    "ds_read_b64_tr_b16 v[236:237], v13 offset:24576\n" \
    "ds_read_b64_tr_b16 v[238:239], v13 offset:25600\n" \
    "ds_read_b64_tr_b16 v[240:241], v13 offset:32768\n" \
    "ds_read_b64_tr_b16 v[242:243], v13 offset:33792\n" \
    "ds_read_b64_tr_b16 v[244:245], v13 offset:40960\n" \
    "ds_read_b64_tr_b16 v[246:247], v13 offset:41984\n" \
    "ds_read_b64_tr_b16 v[248:249], v13 offset:49152\n" \
    "ds_read_b64_tr_b16 v[250:251], v13 offset:50176\n" \
    "ds_read_b64_tr_b16 v[252:253], v13 offset:57344\n" \
    "ds_read_b64_tr_b16 v[254:255], v13 offset:58368\n" \
    "s_waitcnt lgkmcnt(0)\n" \
    "s_barrier\n" \
    "s_waitcnt vmcnt(0)\n" \
    "s_barrier\n" \
    "v_lshrrev_b32 v5, 2, v1\n" \
    "v_and_b32 v6, 1, v2\n" \
    "v_lshl_add_u32 v6, v6, 2, v5\n" \
    "v_lshlrev_b32 v6, 1, v6\n" \
    "v_and_b32 v7, 3, v1\n" \
    "v_lshrrev_b32 v7, 1, v7\n" \
    "v_xor_b32 v6, v6, v7\n" \
    "v_lshl_add_u32 v5, v2, 3, v5\n" \
    "v_lshlrev_b32 v5, 9, v5\n" \
    "v_lshl_add_u32 v5, v6, 4, v5\n" \
    "v_and_b32 v7, 1, v1\n" \
    "v_lshl_add_u32 v14, v7, 3, v5\n" \
    "s_lshl_b32 s64, s61, 7\n" \
    "v_xor_b32 v14, s64, v14\n" \
    "v_lshrrev_b32 v5, 1, v1\n" \
    "v_and_b32 v5, 1, v5\n" \
    "v_lshlrev_b32 v5, 3, v5\n" \
    "v_lshrrev_b32 v6, 2, v1\n" \
    "v_lshl_add_u32 v5, v6, 1, v5\n" \
    "v_and_b32 v6, 1, v1\n" \
    "v_add_u32 v5, v5, v6\n" \
    "v_lshl_add_u32 v5, v2, 4, v5\n" \
    "v_lshlrev_b32 v5, 4, v5\n" \
    "s_lshl_b32 s64, s61, 14\n" \
    "s_add_u32 s64, s64, 65536\n" \
    "v_add_u32 v16, s64, v5\n" \
    "v_xor_b32 v15, 0, v14\n" \
    "ds_read_b64_tr_b16 v[20:21], v15 offset:0\n" \
    "ds_read_b64_tr_b16 v[22:23], v15 offset:2048\n" \
    "ds_read_b64_tr_b16 v[24:25], v15 offset:16384\n" \
    "ds_read_b64_tr_b16 v[26:27], v15 offset:18432\n" \
    "ds_read_b64_tr_b16 v[28:29], v15 offset:32768\n" \
    "ds_read_b64_tr_b16 v[30:31], v15 offset:34816\n" \
    "ds_read_b64_tr_b16 v[32:33], v15 offset:49152\n" \
    "ds_read_b64_tr_b16 v[34:35], v15 offset:51200\n" \
    "s_waitcnt lgkmcnt(0)\n" \
    "ds_write_b128 v16, v[20:23] offset:0\n" \
    "ds_write_b128 v16, v[24:27] offset:1024\n" \
    "ds_write_b128 v16, v[28:31] offset:2048\n" \
    "ds_write_b128 v16, v[32:35] offset:3072\n" \
    "s_waitcnt lgkmcnt(0)\n" \
    "v_xor_b32 v15, 32, v14\n" \
    "ds_read_b64_tr_b16 v[20:21], v15 offset:0\n" \
    "ds_read_b64_tr_b16 v[22:23], v15 offset:2048\n" \
    "ds_read_b64_tr_b16 v[24:25], v15 offset:16384\n" \
    "ds_read_b64_tr_b16 v[26:27], v15 offset:18432\n" \
    "ds_read_b64_tr_b16 v[28:29], v15 offset:32768\n" \
    "ds_read_b64_tr_b16 v[30:31], v15 offset:34816\n" \
    "ds_read_b64_tr_b16 v[32:33], v15 offset:49152\n" \
    "ds_read_b64_tr_b16 v[34:35], v15 offset:51200\n" \
    "s_waitcnt lgkmcnt(0)\n" \
    "ds_write_b128 v16, v[20:23] offset:4096\n" \
    "ds_write_b128 v16, v[24:27] offset:5120\n" \
    "ds_write_b128 v16, v[28:31] offset:6144\n" \
    "ds_write_b128 v16, v[32:35] offset:7168\n" \
    "s_waitcnt lgkmcnt(0)\n" \
    "v_xor_b32 v15, 64, v14\n" \
    "ds_read_b64_tr_b16 v[20:21], v15 offset:0\n" \
    "ds_read_b64_tr_b16 v[22:23], v15 offset:2048\n" \
    "ds_read_b64_tr_b16 v[24:25], v15 offset:16384\n" \
    "ds_read_b64_tr_b16 v[26:27], v15 offset:18432\n" \
    "ds_read_b64_tr_b16 v[28:29], v15 offset:32768\n" \
    "ds_read_b64_tr_b16 v[30:31], v15 offset:34816\n" \
    "ds_read_b64_tr_b16 v[32:33], v15 offset:49152\n" \
    "ds_read_b64_tr_b16 v[34:35], v15 offset:51200\n" \
    "s_waitcnt lgkmcnt(0)\n" \
    "ds_write_b128 v16, v[20:23] offset:8192\n" \
    "ds_write_b128 v16, v[24:27] offset:9216\n" \
    "ds_write_b128 v16, v[28:31] offset:10240\n" \
    "ds_write_b128 v16, v[32:35] offset:11264\n" \
    "s_waitcnt lgkmcnt(0)\n" \
    "v_xor_b32 v15, 96, v14\n" \
    "ds_read_b64_tr_b16 v[20:21], v15 offset:0\n" \
    "ds_read_b64_tr_b16 v[22:23], v15 offset:2048\n" \
    "ds_read_b64_tr_b16 v[24:25], v15 offset:16384\n" \
    "ds_read_b64_tr_b16 v[26:27], v15 offset:18432\n" \
    "ds_read_b64_tr_b16 v[28:29], v15 offset:32768\n" \
    "ds_read_b64_tr_b16 v[30:31], v15 offset:34816\n" \
    "ds_read_b64_tr_b16 v[32:33], v15 offset:49152\n" \
    "ds_read_b64_tr_b16 v[34:35], v15 offset:51200\n" \
    "s_waitcnt lgkmcnt(0)\n" \
    "ds_write_b128 v16, v[20:23] offset:12288\n" \
    "ds_write_b128 v16, v[24:27] offset:13312\n" \
    "ds_write_b128 v16, v[28:31] offset:14336\n" \
    "ds_write_b128 v16, v[32:35] offset:15360\n" \
    "s_waitcnt lgkmcnt(0)\n" \
    "s_barrier\n" \
    "; ---- address parity of the exchange buffers, then the first memory slice\n" \
    "v_mov_b32 v3, 0\n" \
    "s_mov_b32 s65, 0x80000\n" \
    "CT_%=:\n" \
    "global_load_dword v4, v3, s[94:95] sc1\n" \
    "s_waitcnt vmcnt(0)\n" \
    "v_cmp_eq_u32 vcc, -1, v4\n" \
    "s_nop 1\n" \
    "s_cbranch_vccz CT_ok_%=\n" \
    "s_sleep 2\n" \
    "s_sub_u32 s65, s65, 1\n" \
    "s_cmp_lg_u32 s65, 0\n" \
    "s_cbranch_scc1 CT_%=\n" \
    "s_mov_b32 s66, 7\n" \
    "s_branch FIN_%=\n" \
    "CT_ok_%=:\n" \
    "v_readfirstlane_b32 s64, v4\n" \
    "s_nop 3\n" \
    "s_and_b32 s64, s64, 1\n" \
    "s_lshl_b32 s64, s64, 13\n" \
    "s_add_u32 s48, s48, s64\n" \
    "s_addc_u32 s49, s49, 0\n" \
    "s_add_u32 s50, s48, 0x100000\n" \
    "s_addc_u32 s51, s49, 0\n" \
    "s_lshl_b32 s64, s62, 5\n" \
    "s_lshl_b32 s67, s61, 3\n" \
    "s_add_u32 s64, s64, s67\n" \
    "s_lshl_b32 s64, s64, 14\n" \
    "s_lshl_b32 s67, s63, 8\n" \
    "s_add_u32 s64, s64, s67\n" \
    "s_add_u32 s68, s48, s64\n" \
    "s_addc_u32 s69, s49, 0\n" \
    "s_add_u32 s70, s68, 65536\n" \
    "s_addc_u32 s71, s69, 0\n" \
    "s_lshl_b32 s64, s62, 5\n" \
    "s_add_u32 s64, s64, s63\n" \
    "s_lshl_b32 s64, s64, 14\n" \
    "s_add_u32 s72, s48, s64\n" \
    "s_addc_u32 s73, s49, 0\n" \
    "s_add_u32 s74, s72, 4096\n" \
    "s_addc_u32 s75, s73, 0\n" \
    "s_cmp_lg_u32 s61, 0\n" \
    "s_cbranch_scc1 G0_%=\n" \
    "s_lshl_b32 s64, s62, 5\n" \
    "s_add_u32 s64, s64, s63\n" \
    "s_lshr_b32 s67, s64, 1\n" \
    "s_lshl_b32 s67, s67, 5\n" \
    "s_and_b32 s88, s64, 1\n" \
    "s_lshl_b32 s88, s88, 2\n" \
    "s_add_u32 s67, s67, s88\n" \
    "s_add_u32 s92, s92, s67\n" \
    "s_addc_u32 s93, s93, 0\n" \
    "s_lshl_b32 s64, s64, 7\n" \
    "s_add_u32 s88, s50, s64\n" \
    "s_addc_u32 s89, s51, 0\n" \
    "v_mbcnt_lo_u32_b32 v0, -1, 0\n" \
    "v_mbcnt_hi_u32_b32 v0, -1, v0\n" \
    "v_lshrrev_b32 v1, 2, v0\n" \
    "v_and_b32 v2, 3, v0\n" \
    "v_lshlrev_b32 v3, 10, v1\n" \
    "v_lshl_add_u32 v3, v2, 3, v3\n" \
    "v_lshlrev_b32 v4, 4, v1\n" \
    "v_lshl_add_u32 v4, v2, 2, v4\n" \
    "s_mov_b32 exec_hi, 0\n" \
    "global_load_dword v5, v3, s[92:93]\n" \
    "s_waitcnt vmcnt(0)\n" \
    "v_min_u32 v5, 0xfffffffe, v5\n" \
    "global_store_dword v4, v5, s[88:89] sc1\n" \
    "s_mov_b64 exec, -1\n" \
    "G0_%=:\n" \
    "; ---- first segment k_x / v_x^T, accumulators\n" \
    "v_mov_b32 v67, 0\n" \
    "s_mov_b32 s65, 0x80000\n" \
    "X0_%=:\n" \
    "global_load_dword v65, v67, s[84:85] sc1\n" \
    "s_waitcnt vmcnt(0)\n" \
    "v_cmp_ne_u32 vcc, 0, v65\n" \
    "s_nop 1\n" \
    "s_cbranch_vccz X0_ok_%=\n" \
    "s_sleep 2\n" \
    "s_sub_u32 s65, s65, 1\n" \
    "s_cmp_lg_u32 s65, 0\n" \
    "s_cbranch_scc1 X0_%=\n" \
    "s_mov_b32 s66, 9\n" \
    "s_branch FIN_%=\n" \
    "X0_ok_%=:\n" \
    "s_add_u32 s67, s86, 8192\n" \
    "s_mov_b32 m0, s67\n" \
    "s_add_u32 s67, s86, 24576\n" \
    "global_load_lds_dwordx4 v58, s[52:53] sc1\n" \
    "global_load_lds_dwordx4 v58, s[52:53] offset:1024 sc1\n" \
    "s_mov_b32 m0, s67\n" \
    "s_add_u32 s52, s52, 262144\n" \
    "global_load_lds_dwordx4 v58, s[54:55] sc1\n" \
    "global_load_lds_dwordx4 v58, s[54:55] offset:1024 sc1\n" \
    "s_addc_u32 s53, s53, 0\n" \
    "s_add_u32 s54, s54, 262144\n" \
    "s_addc_u32 s55, s55, 0\n" \
    "v_mov_b32 v32, 0\n" \
    "v_mov_b32 v33, 0\n" \
    "v_mov_b32 v34, 0\n" \
    "v_mov_b32 v35, 0\n" \
    "v_mov_b32 v36, 0\n" \
    "v_mov_b32 v37, 0\n" \
    "v_mov_b32 v38, 0\n" \
    "v_mov_b32 v39, 0\n" \
    "v_mov_b32 v40, 0\n" \
    "v_mov_b32 v41, 0\n" \
    "v_mov_b32 v42, 0\n" \
    "v_mov_b32 v43, 0\n" \
    "v_mov_b32 v44, 0\n" \
    "v_mov_b32 v45, 0\n" \
    "v_mov_b32 v46, 0\n" \
    "v_mov_b32 v47, 0\n" \
    "v_mov_b32 v48, 0\n" \
    "v_mov_b32 v49, 0\n" \
    "v_mov_b32 v50, 0\n" \
    "v_mov_b32 v51, 0\n" \
    "v_mov_b32 v52, 0\n" \
    "v_mov_b32 v53, 0\n" \
    "v_mov_b32 v54, 0\n" \
    "v_mov_b32 v55, 0\n" \
    "s_waitcnt vmcnt(0)\n" \
    "s_barrier\n" \
    "s_mov_b32 s65, 0x40000\n" \
    "; ================ step, parity 0\n" \
    "GA0_%=:\n" \
    "s_add_u32 s67, s86, 0\n" \
    "s_mov_b32 m0, s67\n" \
    "s_nop 0\n" \
    "global_load_lds_dwordx4 v58, s[50:51] sc1\n" \
    "global_load_lds_dwordx4 v58, s[50:51] offset:1024 sc1\n" \
    "s_waitcnt vmcnt(0)\n" \
    "s_barrier\n" \
    "ds_read_b128 v[0:3], v57 offset:0\n" \
    "ds_read_b128 v[4:7], v57 offset:1024\n" \
    "ds_read_b128 v[8:11], v57 offset:2048\n" \
    "ds_read_b128 v[12:15], v57 offset:3072\n" \
    "ds_read_b128 v[16:19], v57 offset:4096\n" \
    "ds_read_b128 v[20:23], v57 offset:5120\n" \
    "ds_read_b128 v[24:27], v57 offset:6144\n" \
    "ds_read_b128 v[28:31], v57 offset:7168\n" \
    "s_waitcnt lgkmcnt(7)\n" \
    "v_smfmac_f32_16x16x64_bf16 v[32:35], v[0:3], a[0:7], v56\n" \
    "v_max_u32 v66, v0, v1\n" \
    "v_smfmac_f32_16x16x64_bf16 v[36:39], v[0:3], a[64:71], v56\n" \
    "v_max3_u32 v66, v2, v3, v66\n" \
    "s_waitcnt lgkmcnt(6)\n" \
    "v_smfmac_f32_16x16x64_bf16 v[32:35], v[4:7], a[8:15], v56\n" \
    "v_max3_u32 v66, v4, v5, v66\n" \
    "v_smfmac_f32_16x16x64_bf16 v[36:39], v[4:7], a[72:79], v56\n" \
    "v_max3_u32 v66, v6, v7, v66\n" \
    "s_waitcnt lgkmcnt(5)\n" \
    "v_smfmac_f32_16x16x64_bf16 v[32:35], v[8:11], a[16:23], v56\n" \
    "v_max3_u32 v66, v8, v9, v66\n" \
    "v_smfmac_f32_16x16x64_bf16 v[36:39], v[8:11], a[80:87], v56\n" \
    "v_max3_u32 v66, v10, v11, v66\n" \
    "s_waitcnt lgkmcnt(4)\n" \
    "v_smfmac_f32_16x16x64_bf16 v[32:35], v[12:15], a[24:31], v56\n" \
    "v_max3_u32 v66, v12, v13, v66\n" \
    "v_smfmac_f32_16x16x64_bf16 v[36:39], v[12:15], a[88:95], v56\n" \
    "v_max3_u32 v66, v14, v15, v66\n" \
    "s_waitcnt lgkmcnt(3)\n" \
    "v_smfmac_f32_16x16x64_bf16 v[32:35], v[16:19], a[32:39], v56\n" \
    "v_max3_u32 v66, v16, v17, v66\n" \
    "v_smfmac_f32_16x16x64_bf16 v[36:39], v[16:19], a[96:103], v56\n" \
    "v_max3_u32 v66, v18, v19, v66\n" \
    "s_waitcnt lgkmcnt(2)\n" \
    "v_smfmac_f32_16x16x64_bf16 v[32:35], v[20:23], a[40:47], v56\n" \
    "v_max3_u32 v66, v20, v21, v66\n" \
    "v_smfmac_f32_16x16x64_bf16 v[36:39], v[20:23], a[104:111], v56\n" \
    "v_max3_u32 v66, v22, v23, v66\n" \
    "s_waitcnt lgkmcnt(1)\n" \
    "v_smfmac_f32_16x16x64_bf16 v[32:35], v[24:27], a[48:55], v56\n" \
    "v_max3_u32 v66, v24, v25, v66\n" \
    "v_smfmac_f32_16x16x64_bf16 v[36:39], v[24:27], a[112:119], v56\n" \
    "v_max3_u32 v66, v26, v27, v66\n" \
    "s_waitcnt lgkmcnt(0)\n" \
    "v_smfmac_f32_16x16x64_bf16 v[32:35], v[28:31], a[56:63], v56\n" \
    "v_max3_u32 v66, v28, v29, v66\n" \
    "v_smfmac_f32_16x16x64_bf16 v[36:39], v[28:31], a[120:127], v56\n" \
    "v_max3_u32 v66, v30, v31, v66\n" \
    "v_cmp_eq_u32 vcc, -1, v66\n" \
    "v_smfmac_f32_16x16x64_bf16 v[40:43], v[0:3], a[128:135], v56\n" \
    "s_cbranch_vccnz GF0_%=\n" \
    "v_smfmac_f32_16x16x64_bf16 v[44:47], v[0:3], a[192:199], v56\n" \
    "ds_read_b128 v[68:71], v57 offset:8192\n" \
    "ds_read_b128 v[72:75], v57 offset:9216\n" \
    "v_smfmac_f32_16x16x64_bf16 v[40:43], v[4:7], a[136:143], v56\n" \
    "ds_read_b128 v[76:79], v57 offset:10240\n" \
    "v_permlane32_swap_b32 v32, v36\n" \
    "v_smfmac_f32_16x16x64_bf16 v[44:47], v[4:7], a[200:207], v56\n" \
    "v_permlane32_swap_b32 v33, v37\n" \
    "v_permlane32_swap_b32 v34, v38\n" \
    "v_smfmac_f32_16x16x64_bf16 v[40:43], v[8:11], a[144:151], v56\n" \
    "v_permlane32_swap_b32 v35, v39\n" \
    "v_add_f32 v32, v32, v36\n" \
    "v_smfmac_f32_16x16x64_bf16 v[44:47], v[8:11], a[208:215], v56\n" \
    "v_add_f32 v33, v33, v37\n" \
    "v_add_f32 v34, v34, v38\n" \
    "v_smfmac_f32_16x16x64_bf16 v[40:43], v[12:15], a[152:159], v56\n" \
    "v_add_f32 v35, v35, v39\n" \
    "v_cvt_pk_bf16_f32 v66, v32, v33\n" \
    "v_smfmac_f32_16x16x64_bf16 v[44:47], v[12:15], a[216:223], v56\n" \
    "v_cvt_pk_bf16_f32 v67, v34, v35\n" \
    "ds_write_b64 v59, v[66:67] offset:0\n" \
    "v_smfmac_f32_16x16x64_bf16 v[40:43], v[16:19], a[160:167], v56\n" \
    "ds_read_b128 v[80:83], v57 offset:11264\n" \
    "ds_read_b128 v[84:87], v57 offset:12288\n" \
    "v_smfmac_f32_16x16x64_bf16 v[44:47], v[16:19], a[224:231], v56\n" \
    "ds_read_b128 v[88:91], v57 offset:13312\n" \
    "ds_read_b128 v[92:95], v57 offset:14336\n" \
    "v_smfmac_f32_16x16x64_bf16 v[40:43], v[20:23], a[168:175], v56\n" \
    "ds_read_b128 v[96:99], v57 offset:15360\n" \
    "s_add_u32 s67, s86, 16384\n" \
    "v_smfmac_f32_16x16x64_bf16 v[44:47], v[20:23], a[232:239], v56\n" \
    "s_mov_b32 m0, s67\n" \
    "s_add_u32 s67, s86, 32768\n" \
    "v_smfmac_f32_16x16x64_bf16 v[40:43], v[24:27], a[176:183], v56\n" \
    "global_load_lds_dwordx4 v58, s[52:53] sc1\n" \
    "global_load_lds_dwordx4 v58, s[52:53] offset:1024 sc1\n" \
    "v_smfmac_f32_16x16x64_bf16 v[44:47], v[24:27], a[240:247], v56\n" \
    "s_mov_b32 m0, s67\n" \
    "s_add_u32 s52, s52, 262144\n" \
    "v_smfmac_f32_16x16x64_bf16 v[40:43], v[28:31], a[184:191], v56\n" \
    "global_load_lds_dwordx4 v58, s[54:55] sc1\n" \
    "global_load_lds_dwordx4 v58, s[54:55] offset:1024 sc1\n" \
    "v_smfmac_f32_16x16x64_bf16 v[44:47], v[28:31], a[248:255], v56\n" \
    "s_addc_u32 s53, s53, 0\n" \
    "s_add_u32 s54, s54, 262144\n" \
    "s_addc_u32 s55, s55, 0\n" \
    "v_smfmac_f32_16x16x64_bf16 v[48:51], v[0:3], v[128:135], v56\n" \
    "v_mov_b32 v116, 0\n" \
    "v_smfmac_f32_16x16x64_bf16 v[52:55], v[0:3], v[192:199], v56\n" \
    "v_mov_b32 v117, 0xf149f2ca\n" \
    "v_smfmac_f32_16x16x64_bf16 v[48:51], v[4:7], v[136:143], v56\n" \
    "s_mov_b32 vcc_lo, -1\n" \
    "s_mov_b32 vcc_hi, 0\n" \
    "v_cndmask_b32 v116, v117, v116, vcc\n" \
    "v_smfmac_f32_16x16x64_bf16 v[52:55], v[4:7], v[200:207], v56\n" \
    "v_permlane32_swap_b32 v40, v44\n" \
    "v_permlane32_swap_b32 v41, v45\n" \
    "v_smfmac_f32_16x16x64_bf16 v[48:51], v[8:11], v[144:151], v56\n" \
    "v_permlane32_swap_b32 v42, v46\n" \
    "v_permlane32_swap_b32 v43, v47\n" \
    "v_smfmac_f32_16x16x64_bf16 v[52:55], v[8:11], v[208:215], v56\n" \
    "v_add_f32 v40, v40, v44\n" \
    "v_add_f32 v41, v41, v45\n" \
    "v_add_f32 v42, v42, v46\n" \
    "v_smfmac_f32_16x16x64_bf16 v[48:51], v[12:15], v[152:159], v56\n" \
    "v_add_f32 v43, v43, v47\n" \
    "v_cvt_pk_bf16_f32 v66, v40, v41\n" \
    "v_cvt_pk_bf16_f32 v67, v42, v43\n" \
    "ds_write_b64 v59, v[66:67] offset:2048\n" \
    "s_cmp_lg_u32 s62, 0\n" \
    "s_cbranch_scc1 NK0_%=\n" \
    "global_store_dwordx2 v59, v[66:67], s[56:57] sc1\n" \
    "NK0_%=:\n" \
    "v_smfmac_f32_16x16x64_bf16 v[52:55], v[12:15], v[216:223], v56\n" \
    "v_mov_b32 v117, v116\n" \
    "v_mov_b32 v118, v116\n" \
    "v_mov_b32 v119, v116\n" \
    "s_waitcnt lgkmcnt(0)\n" \
    "s_barrier\n" \
    "ds_read_b64_tr_b16 v[32:33], v60 offset:0\n" \
    "ds_read_b64_tr_b16 v[34:35], v60 offset:256\n" \
    "ds_read_b64_tr_b16 v[100:101], v60 offset:2048\n" \
    "v_smfmac_f32_16x16x64_bf16 v[48:51], v[16:19], v[160:167], v56\n" \
    "ds_read_b64_tr_b16 v[102:103], v60 offset:2304\n" \
    "v_smfmac_f32_16x16x64_bf16 v[52:55], v[16:19], v[224:231], v56\n" \
    "ds_read_b64_tr_b16 v[36:37], v60 offset:512\n" \
    "v_smfmac_f32_16x16x64_bf16 v[48:51], v[20:23], v[168:175], v56\n" \
    "ds_read_b64_tr_b16 v[38:39], v60 offset:768\n" \
    "v_smfmac_f32_16x16x64_bf16 v[52:55], v[20:23], v[232:239], v56\n" \
    "ds_read_b64_tr_b16 v[104:105], v60 offset:2560\n" \
    "v_smfmac_f32_16x16x64_bf16 v[48:51], v[24:27], v[176:183], v56\n" \
    "ds_read_b64_tr_b16 v[106:107], v60 offset:2816\n" \
    "v_smfmac_f32_16x16x64_bf16 v[52:55], v[24:27], v[240:247], v56\n" \
    "ds_read_b64_tr_b16 v[40:41], v60 offset:1024\n" \
    "v_smfmac_f32_16x16x64_bf16 v[48:51], v[28:31], v[184:191], v56\n" \
    "ds_read_b64_tr_b16 v[42:43], v60 offset:1280\n" \
    "v_smfmac_f32_16x16x64_bf16 v[52:55], v[28:31], v[248:255], v56\n" \
    "ds_read_b64_tr_b16 v[108:109], v60 offset:3072\n" \
    "ds_read_b64_tr_b16 v[110:111], v60 offset:3328\n" \
    "ds_read_b64_tr_b16 v[44:45], v60 offset:1536\n" \
    "ds_read_b64_tr_b16 v[46:47], v60 offset:1792\n" \
    "ds_read_b64_tr_b16 v[112:113], v60 offset:3584\n" \
    "ds_read_b64_tr_b16 v[114:115], v60 offset:3840\n" \
    "s_waitcnt lgkmcnt(14)\n" \
    "v_mfma_f32_16x16x32_bf16 v[120:123], v[68:71], v[32:35], 0\n" \
    "v_mfma_f32_16x16x32_bf16 v[124:127], v[84:87], v[32:35], 0\n" \
    "s_waitcnt lgkmcnt(12)\n" \
    "v_mfma_f32_16x16x32_bf16 v[116:119], v[100:103], v[32:35], v[116:119]\n" \
    "v_permlane32_swap_b32 v48, v52\n" \
    "v_permlane32_swap_b32 v49, v53\n" \
    "s_waitcnt lgkmcnt(10)\n" \
    "v_mfma_f32_16x16x32_bf16 v[120:123], v[72:75], v[36:39], v[120:123]\n" \
    "v_permlane32_swap_b32 v50, v54\n" \
    "v_permlane32_swap_b32 v51, v55\n" \
    "v_mfma_f32_16x16x32_bf16 v[124:127], v[88:91], v[36:39], v[124:127]\n" \
    "v_add_f32 v48, v48, v52\n" \
    "v_add_f32 v49, v49, v53\n" \
    "s_waitcnt lgkmcnt(8)\n" \
    "v_mfma_f32_16x16x32_bf16 v[116:119], v[104:107], v[36:39], v[116:119]\n" \
    "v_add_f32 v50, v50, v54\n" \
    "v_add_f32 v51, v51, v55\n" \
    "s_waitcnt lgkmcnt(6)\n" \
    "v_mfma_f32_16x16x32_bf16 v[120:123], v[76:79], v[40:43], v[120:123]\n" \
    "v_cvt_pk_bf16_f32 v52, v48, v49\n" \
    "v_cvt_pk_bf16_f32 v53, v50, v51\n" \
    "v_mfma_f32_16x16x32_bf16 v[124:127], v[92:95], v[40:43], v[124:127]\n" \
    "s_waitcnt lgkmcnt(4)\n" \
    "v_mfma_f32_16x16x32_bf16 v[116:119], v[108:111], v[40:43], v[116:119]\n" \
    "s_waitcnt lgkmcnt(2)\n" \
    "v_mfma_f32_16x16x32_bf16 v[120:123], v[80:83], v[44:47], v[120:123]\n" \
    "v_mfma_f32_16x16x32_bf16 v[124:127], v[96:99], v[44:47], v[124:127]\n" \
    "s_waitcnt lgkmcnt(0)\n" \
    "v_mfma_f32_16x16x32_bf16 v[116:119], v[112:115], v[44:47], v[116:119]\n" \
    "s_cmp_eq_u32 s62, 0\n" \
    "s_cbranch_scc1 NV0_%=\n" \
    "global_store_dwordx2 v59, v[52:53], s[56:57] sc1\n" \
    "NV0_%=:\n" \
    "v_mov_b32 v54, v52\n" \
    "v_mov_b32 v55, v53\n" \
    "s_nop 0\n" \
    "v_permlane32_swap_b32 v52, v54\n" \
    "v_permlane32_swap_b32 v53, v55\n" \
    "ds_read_b128 v[68:71], v58 offset:24576\n" \
    "ds_read_b128 v[72:75], v58 offset:25600\n" \
    "v_max3_f32 v112, v120, v121, v122\n" \
    "ds_read_b128 v[0:3], v62 offset:0\n" \
    "v_max3_f32 v113, v123, v124, v125\n" \
    "ds_read_b128 v[4:7], v62 offset:1024\n" \
    "v_max3_f32 v66, v126, v127, v116\n" \
    "ds_read_b128 v[8:11], v62 offset:2048\n" \
    "v_max3_f32 v67, v117, v118, v119\n" \
    "ds_read_b128 v[12:15], v62 offset:3072\n" \
    "v_max3_f32 v112, v112, v113, v66\n" \
    "ds_read_b128 v[16:19], v62 offset:4096\n" \
    "v_max_f32 v112, v112, v67\n" \
    "ds_read_b128 v[20:23], v62 offset:5120\n" \
    "v_mov_b32 v113, v112\n" \
    "ds_read_b128 v[24:27], v62 offset:6144\n" \
    "s_nop 0\n" \
    "v_permlane16_swap_b32 v112, v113\n" \
    "ds_read_b128 v[28:31], v62 offset:7168\n" \
    "v_max_f32 v112, v112, v113\n" \
    "ds_read_b128 v[32:35], v62 offset:8192\n" \
    "v_mov_b32 v113, v112\n" \
    "ds_read_b128 v[36:39], v62 offset:9216\n" \
    "s_nop 0\n" \
    "v_permlane32_swap_b32 v112, v113\n" \
    "ds_read_b128 v[40:43], v62 offset:10240\n" \
    "v_max_f32 v112, v112, v113\n" \
    "ds_read_b128 v[44:47], v62 offset:11264\n" \
    "v_mul_f32 v112, s83, v112\n" \
    "v_fma_f32 v120, v120, s80, v112\n" \
    "v_fma_f32 v121, v121, s80, v112\n" \
    "v_exp_f32 v120, v120\n" \
    "v_fma_f32 v122, v122, s80, v112\n" \
    "v_exp_f32 v121, v121\n" \
    "v_fma_f32 v123, v123, s80, v112\n" \
    "v_exp_f32 v122, v122\n" \
    "v_fma_f32 v124, v124, s80, v112\n" \
    "v_cvt_pk_bf16_f32 v76, v120, v121\n" \
    "v_exp_f32 v123, v123\n" \
    "v_fma_f32 v125, v125, s80, v112\n" \
    "v_exp_f32 v124, v124\n" \
    "v_fma_f32 v126, v126, s80, v112\n" \
    "v_cvt_pk_bf16_f32 v77, v122, v123\n" \
    "v_exp_f32 v125, v125\n" \
    "v_fma_f32 v127, v127, s80, v112\n" \
    "v_exp_f32 v126, v126\n" \
    "v_fma_f32 v116, v116, s80, v112\n" \
    "v_cvt_pk_bf16_f32 v78, v124, v125\n" \
    "v_exp_f32 v127, v127\n" \
    "v_fma_f32 v117, v117, s80, v112\n" \
    "v_exp_f32 v116, v116\n" \
    "v_fma_f32 v118, v118, s80, v112\n" \
    "v_cvt_pk_bf16_f32 v79, v126, v127\n" \
    "s_waitcnt lgkmcnt(12)\n" \
    "v_add_f32 v113, v120, v121\n" \
    "v_mfma_f32_16x16x32_bf16 v[100:103], v[68:71], v[76:79], 0\n" \
    "v_exp_f32 v117, v117\n" \
    "v_fma_f32 v119, v119, s80, v112\n" \
    "v_mfma_f32_16x16x32_bf16 v[104:107], v[72:75], v[76:79], 0\n" \
    "v_exp_f32 v118, v118\n" \
    "v_cvt_pk_bf16_f32 v80, v116, v117\n" \
    "v_exp_f32 v119, v119\n" \
    "v_add_f32 v66, v122, v123\n" \
    "v_cvt_pk_bf16_f32 v81, v118, v119\n" \
    "v_add_f32 v67, v124, v125\n" \
    "v_add_f32 v113, v113, v66\n" \
    "v_mfma_f32_16x16x16_bf16 v[100:103], v[52:53], v[80:81], v[100:103]\n" \
    "v_add_f32 v66, v126, v127\n" \
    "v_mfma_f32_16x16x16_bf16 v[104:107], v[54:55], v[80:81], v[104:107]\n" \
    "v_add_f32 v67, v67, v66\n" \
    "v_add_f32 v66, v116, v117\n" \
    "v_add_f32 v113, v113, v67\n" \
    "v_add_f32 v67, v118, v119\n" \
    "v_add_f32 v66, v66, v67\n" \
    "v_add_f32 v113, v113, v66\n" \
    "v_mov_b32 v66, v113\n" \
    "s_nop 1\n" \
    "v_permlane16_swap_b32 v113, v66\n" \
    "v_add_f32 v113, v113, v66\n" \
    "v_mov_b32 v66, v113\n" \
    "s_nop 1\n" \
    "v_permlane32_swap_b32 v113, v66\n" \
    "v_add_f32 v113, v113, v66\n" \
    "v_rcp_f32 v113, v113\n" \
    "ds_read_b128 v[68:71], v62 offset:12288\n" \
    "ds_read_b128 v[72:75], v62 offset:13312\n" \
    "ds_read_b128 v[76:79], v62 offset:14336\n" \
    "ds_read_b128 v[80:83], v62 offset:15360\n" \
    "v_cndmask_b32 v100, v102, v100, s[76:77]\n" \
    "v_cndmask_b32 v101, v103, v101, s[76:77]\n" \
    "v_mul_f32 v100, v100, v113\n" \
    "v_mul_f32 v101, v101, v113\n" \
    "v_cvt_pk_bf16_f32 v100, v100, v101\n" \
    "ds_write_b32 v61, v100 offset:0\n" \
    "v_cndmask_b32 v104, v106, v104, s[76:77]\n" \
    "v_cndmask_b32 v105, v107, v105, s[76:77]\n" \
    "v_mul_f32 v104, v104, v113\n" \
    "v_mul_f32 v105, v105, v113\n" \
    "v_cvt_pk_bf16_f32 v104, v104, v105\n" \
    "ds_write_b32 v61, v104 offset:256\n" \
    "v_mov_b32 v84, 0\n" \
    "v_mov_b32 v85, 0\n" \
    "v_mov_b32 v86, 0\n" \
    "v_mov_b32 v87, 0\n" \
    "v_mov_b32 v88, 0\n" \
    "v_mov_b32 v89, 0\n" \
    "v_mov_b32 v90, 0\n" \
    "v_mov_b32 v91, 0\n" \
    "v_mov_b32 v92, 0\n" \
    "v_mov_b32 v93, 0\n" \
    "v_mov_b32 v94, 0\n" \
    "v_mov_b32 v95, 0\n" \
    "v_mov_b32 v96, 0\n" \
    "v_mov_b32 v97, 0\n" \
    "v_mov_b32 v98, 0\n" \
    "v_mov_b32 v99, 0\n" \
    "s_waitcnt lgkmcnt(0)\n" \
    "s_barrier\n" \
    "ds_read_b128 v[108:111], v57 offset:45056\n" \
    "ds_read_b128 v[112:115], v57 offset:46080\n" \
    "s_waitcnt lgkmcnt(1)\n" \
    "v_smfmac_f32_16x16x64_bf16 v[84:87], v[108:111], v[0:7], v56\n" \
    "v_smfmac_f32_16x16x64_bf16 v[88:91], v[108:111], v[16:23], v56\n" \
    "s_waitcnt lgkmcnt(0)\n" \
    "v_smfmac_f32_16x16x64_bf16 v[84:87], v[112:115], v[8:15], v56\n" \
    "v_smfmac_f32_16x16x64_bf16 v[88:91], v[112:115], v[24:31], v56\n" \
    "v_smfmac_f32_16x16x64_bf16 v[92:95], v[108:111], v[32:39], v56\n" \
    "v_smfmac_f32_16x16x64_bf16 v[96:99], v[108:111], v[68:75], v56\n" \
    "v_smfmac_f32_16x16x64_bf16 v[92:95], v[112:115], v[40:47], v56\n" \
    "v_smfmac_f32_16x16x64_bf16 v[96:99], v[112:115], v[76:83], v56\n" \
    "s_nop 3\n" \
    "v_permlane32_swap_b32 v84, v88\n" \
    "v_permlane32_swap_b32 v85, v89\n" \
    "v_permlane32_swap_b32 v86, v90\n" \
    "v_permlane32_swap_b32 v87, v91\n" \
    "v_add_f32 v84, v84, v88\n" \
    "v_add_f32 v85, v85, v89\n" \
    "v_add_f32 v86, v86, v90\n" \
    "v_add_f32 v87, v87, v91\n" \
    "v_and_b32 v84, -2, v84\n" \
    "v_and_b32 v85, -2, v85\n" \
    "v_and_b32 v86, -2, v86\n" \
    "v_and_b32 v87, -2, v87\n" \
    "global_store_dwordx4 v63, v[84:87], s[68:69] sc1\n" \
    "v_permlane32_swap_b32 v92, v96\n" \
    "v_permlane32_swap_b32 v93, v97\n" \
    "v_permlane32_swap_b32 v94, v98\n" \
    "v_permlane32_swap_b32 v95, v99\n" \
    "v_add_f32 v92, v92, v96\n" \
    "v_add_f32 v93, v93, v97\n" \
    "v_add_f32 v94, v94, v98\n" \
    "v_add_f32 v95, v95, v99\n" \
    "v_and_b32 v92, -2, v92\n" \
    "v_and_b32 v93, -2, v93\n" \
    "v_and_b32 v94, -2, v94\n" \
    "v_and_b32 v95, -2, v95\n" \
    "global_store_dwordx4 v63, v[92:95], s[70:71] sc1\n" \
    "s_cmp_lg_u32 s61, 0\n" \
    "s_cbranch_scc1 RN0_%=\n" \
    "s_mov_b32 s64, s81\n" \
    "s_cmp_eq_u32 s64, 0\n" \
    "s_cbranch_scc1 RP0_e_%=\n" \
    "RP0_%=:\n" \
    "s_sleep 1\n" \
    "s_sub_u32 s64, s64, 1\n" \
    "s_cmp_lg_u32 s64, 0\n" \
    "s_cbranch_scc1 RP0_%=\n" \
    "RP0_e_%=:\n" \
    "s_mov_b32 s65, 0x10000\n" \
    "RG0_%=:\n" \
    "global_load_dwordx4 v[0:3], v57, s[72:73] offset:0 sc1\n" \
    "global_load_dwordx4 v[4:7], v57, s[72:73] offset:1024 sc1\n" \
    "global_load_dwordx4 v[8:11], v57, s[72:73] offset:2048 sc1\n" \
    "global_load_dwordx4 v[12:15], v57, s[72:73] offset:3072 sc1\n" \
    "global_load_dwordx4 v[16:19], v57, s[74:75] offset:0 sc1\n" \
    "global_load_dwordx4 v[20:23], v57, s[74:75] offset:1024 sc1\n" \
    "global_load_dwordx4 v[24:27], v57, s[74:75] offset:2048 sc1\n" \
    "global_load_dwordx4 v[28:31], v57, s[74:75] offset:3072 sc1\n" \
    "s_waitcnt vmcnt(0)\n" \
    "v_or3_b32 v32, v0, v1, v2\n" \
    "v_or3_b32 v33, v16, v17, v18\n" \
    "v_or3_b32 v32, v32, v3, v4\n" \
    "v_or3_b32 v33, v33, v19, v20\n" \
    "v_or3_b32 v32, v32, v5, v6\n" \
    "v_or3_b32 v33, v33, v21, v22\n" \
    "v_or3_b32 v32, v32, v7, v8\n" \
    "v_or3_b32 v33, v33, v23, v24\n" \
    "v_or3_b32 v32, v32, v9, v10\n" \
    "v_or3_b32 v33, v33, v25, v26\n" \
    "v_or3_b32 v32, v32, v11, v12\n" \
    "v_or3_b32 v33, v33, v27, v28\n" \
    "v_or3_b32 v32, v32, v13, v14\n" \
    "v_or3_b32 v33, v33, v29, v30\n" \
    "v_or3_b32 v32, v32, v15, v15\n" \
    "v_or3_b32 v33, v33, v31, v31\n" \
    "v_or3_b32 v32, v32, v33, v33\n" \
    "v_and_b32 v32, 1, v32\n" \
    "v_cmp_ne_u32 vcc, 0, v32\n" \
    "v_pk_add_f32 v[0:1], v[0:1], v[4:5]\n" \
    "v_pk_add_f32 v[2:3], v[2:3], v[6:7]\n" \
    "v_pk_add_f32 v[8:9], v[8:9], v[12:13]\n" \
    "v_pk_add_f32 v[10:11], v[10:11], v[14:15]\n" \
    "v_pk_add_f32 v[16:17], v[16:17], v[20:21]\n" \
    "v_pk_add_f32 v[18:19], v[18:19], v[22:23]\n" \
    "v_pk_add_f32 v[24:25], v[24:25], v[28:29]\n" \
    "v_pk_add_f32 v[26:27], v[26:27], v[30:31]\n" \
    "s_cbranch_vccz RD0_%=\n" \
    "s_sub_u32 s65, s65, 1\n" \
    "s_cmp_lg_u32 s65, 0\n" \
    "s_cbranch_scc1 RG0_%=\n" \
    "s_mov_b32 s66, 1\n" \
    "s_branch FIN_%=\n" \
    "RD0_%=:\n" \
    "v_pk_add_f32 v[0:1], v[0:1], v[8:9]\n" \
    "v_pk_add_f32 v[2:3], v[2:3], v[10:11]\n" \
    "v_pk_add_f32 v[16:17], v[16:17], v[24:25]\n" \
    "v_pk_add_f32 v[18:19], v[18:19], v[26:27]\n" \
    "v_pk_add_f32 v[0:1], v[0:1], v[16:17]\n" \
    "v_pk_add_f32 v[2:3], v[2:3], v[18:19]\n" \
    "s_nop 0\n" \
    "v_permlane32_swap_b32 v0, v1\n" \
    "v_permlane32_swap_b32 v2, v3\n" \
    "v_add_f32 v0, v0, v1\n" \
    "v_add_f32 v2, v2, v3\n" \
    "s_nop 1\n" \
    "v_permlane16_swap_b32 v0, v2\n" \
    "v_add_f32 v0, v0, v2\n" \
    "s_nop 1\n" \
    "v_mov_b32_dpp v1, v0 quad_perm:[1,0,3,2] row_mask:0xf bank_mask:0xf bound_ctrl:1\n" \
    "v_cvt_pk_bf16_f32 v0, v0, v1\n" \
    "v_min_u32 v0, 0xfffffffe, v0\n" \
    "s_lshl_b32 s64, s62, 5\n" \
    "s_add_u32 s64, s64, s63\n" \
    "s_lshl_b32 s64, s64, 7\n" \
    "s_add_u32 s64, s64, 16384\n" \
    "s_add_u32 s88, s50, s64\n" \
    "s_addc_u32 s89, s51, 0\n" \
    "s_mov_b64 exec, s[78:79]\n" \
    "global_store_dword v64, v0, s[88:89] sc1\n" \
    "s_mov_b64 exec, -1\n" \
    "s_branch RB0_%=\n" \
    "RN0_%=:\n" \
    "s_waitcnt vmcnt(0)\n" \
    "RB0_%=:\n" \
    "s_barrier\n" \
    "s_cmp_lg_u32 s61, 1\n" \
    "s_cbranch_scc1 NF0_%=\n" \
    "v_mov_b32 v0, 0\n" \
    "s_mov_b64 exec, 1\n" \
    "global_store_dword v0, v0, s[58:59] sc1\n" \
    "s_mov_b64 exec, -1\n" \
    "NF0_%=:\n" \
    "v_mov_b32 v32, 0\n" \
    "v_mov_b32 v33, 0\n" \
    "v_mov_b32 v34, 0\n" \
    "v_mov_b32 v35, 0\n" \
    "v_mov_b32 v36, 0\n" \
    "v_mov_b32 v37, 0\n" \
    "v_mov_b32 v38, 0\n" \
    "v_mov_b32 v39, 0\n" \
    "v_mov_b32 v40, 0\n" \
    "v_mov_b32 v41, 0\n" \
    "v_mov_b32 v42, 0\n" \
    "v_mov_b32 v43, 0\n" \
    "v_mov_b32 v44, 0\n" \
    "v_mov_b32 v45, 0\n" \
    "v_mov_b32 v46, 0\n" \
    "v_mov_b32 v47, 0\n" \
    "v_mov_b32 v48, 0\n" \
    "v_mov_b32 v49, 0\n" \
    "v_mov_b32 v50, 0\n" \
    "v_mov_b32 v51, 0\n" \
    "v_mov_b32 v52, 0\n" \
    "v_mov_b32 v53, 0\n" \
    "v_mov_b32 v54, 0\n" \
    "v_mov_b32 v55, 0\n" \
    "s_add_u32 s50, s50, 16384\n" \
    "s_addc_u32 s51, s51, 0\n" \
    "s_add_u32 s56, s56, 65536\n" \
    "s_addc_u32 s57, s57, 0\n" \
    "s_add_u32 s58, s58, 4\n" \
    "s_addc_u32 s59, s59, 0\n" \
    "s_add_u32 s60, s60, 1\n" \
    "s_add_u32 s64, s60, 1\n" \
    "s_lshr_b32 s64, s64, 2\n" \
    "s_min_u32 s64, s64, 15\n" \
    "s_lshl_b32 s64, s64, 2\n" \
    "v_mov_b32 v67, s64\n" \
    "global_load_dword v65, v67, s[84:85] sc1\n" \
    "s_mov_b32 s64, s82\n" \
    "s_cmp_eq_u32 s64, 0\n" \
    "s_cbranch_scc1 GP0_e_%=\n" \
    "GP0_%=:\n" \
    "s_sleep 1\n" \
    "s_sub_u32 s64, s64, 1\n" \
    "s_cmp_lg_u32 s64, 0\n" \
    "s_cbranch_scc1 GP0_%=\n" \
    "GP0_e_%=:\n" \
    "s_waitcnt vmcnt(0)\n" \
    "v_cmp_ne_u32 vcc, 0, v65\n" \
    "s_nop 1\n" \
    "s_cbranch_vccz XW0_d_%=\n" \
    "s_mov_b32 s65, 0x80000\n" \
    "XW0_%=:\n" \
    "global_load_dword v65, v67, s[84:85] sc1\n" \
    "s_waitcnt vmcnt(0)\n" \
    "v_cmp_ne_u32 vcc, 0, v65\n" \
    "s_nop 1\n" \
    "s_cbranch_vccz XW0_ok_%=\n" \
    "s_sleep 2\n" \
    "s_sub_u32 s65, s65, 1\n" \
    "s_cmp_lg_u32 s65, 0\n" \
    "s_cbranch_scc1 XW0_%=\n" \
    "s_mov_b32 s66, 8\n" \
    "s_branch FIN_%=\n" \
    "XW0_ok_%=:\n" \
    "XW0_d_%=:\n" \
    "s_mov_b32 s65, 0x10000\n" \
    "; ================ step, parity 1\n" \
    "GA1_%=:\n" \
    "s_add_u32 s67, s86, 0\n" \
    "s_mov_b32 m0, s67\n" \
    "s_nop 0\n" \
    "global_load_lds_dwordx4 v58, s[50:51] sc1\n" \
    "global_load_lds_dwordx4 v58, s[50:51] offset:1024 sc1\n" \
    "s_waitcnt vmcnt(0)\n" \
    "s_barrier\n" \
    "ds_read_b128 v[0:3], v57 offset:0\n" \
    "ds_read_b128 v[4:7], v57 offset:1024\n" \
    "ds_read_b128 v[8:11], v57 offset:2048\n" \
    "ds_read_b128 v[12:15], v57 offset:3072\n" \
    "ds_read_b128 v[16:19], v57 offset:4096\n" \
    "ds_read_b128 v[20:23], v57 offset:5120\n" \
    "ds_read_b128 v[24:27], v57 offset:6144\n" \
    "ds_read_b128 v[28:31], v57 offset:7168\n" \
    "s_waitcnt lgkmcnt(7)\n" \
    "v_smfmac_f32_16x16x64_bf16 v[32:35], v[0:3], a[0:7], v56\n" \
    "v_max_u32 v66, v0, v1\n" \
    "v_smfmac_f32_16x16x64_bf16 v[36:39], v[0:3], a[64:71], v56\n" \
    "v_max3_u32 v66, v2, v3, v66\n" \
    "s_waitcnt lgkmcnt(6)\n" \
    "v_smfmac_f32_16x16x64_bf16 v[32:35], v[4:7], a[8:15], v56\n" \
    "v_max3_u32 v66, v4, v5, v66\n" \
    "v_smfmac_f32_16x16x64_bf16 v[36:39], v[4:7], a[72:79], v56\n" \
    "v_max3_u32 v66, v6, v7, v66\n" \
    "s_waitcnt lgkmcnt(5)\n" \
    "v_smfmac_f32_16x16x64_bf16 v[32:35], v[8:11], a[16:23], v56\n" \
    "v_max3_u32 v66, v8, v9, v66\n" \
    "v_smfmac_f32_16x16x64_bf16 v[36:39], v[8:11], a[80:87], v56\n" \
    "v_max3_u32 v66, v10, v11, v66\n" \
    "s_waitcnt lgkmcnt(4)\n" \
    "v_smfmac_f32_16x16x64_bf16 v[32:35], v[12:15], a[24:31], v56\n" \
    "v_max3_u32 v66, v12, v13, v66\n" \
    "v_smfmac_f32_16x16x64_bf16 v[36:39], v[12:15], a[88:95], v56\n" \
    "v_max3_u32 v66, v14, v15, v66\n" \
    "s_waitcnt lgkmcnt(3)\n" \
    "v_smfmac_f32_16x16x64_bf16 v[32:35], v[16:19], a[32:39], v56\n" \
    "v_max3_u32 v66, v16, v17, v66\n" \
    "v_smfmac_f32_16x16x64_bf16 v[36:39], v[16:19], a[96:103], v56\n" \
    "v_max3_u32 v66, v18, v19, v66\n" \
    "s_waitcnt lgkmcnt(2)\n" \
    "v_smfmac_f32_16x16x64_bf16 v[32:35], v[20:23], a[40:47], v56\n" \
    "v_max3_u32 v66, v20, v21, v66\n" \
    "v_smfmac_f32_16x16x64_bf16 v[36:39], v[20:23], a[104:111], v56\n" \
    "v_max3_u32 v66, v22, v23, v66\n" \
    "s_waitcnt lgkmcnt(1)\n" \
    "v_smfmac_f32_16x16x64_bf16 v[32:35], v[24:27], a[48:55], v56\n" \
    "v_max3_u32 v66, v24, v25, v66\n" \
    "v_smfmac_f32_16x16x64_bf16 v[36:39], v[24:27], a[112:119], v56\n" \
    "v_max3_u32 v66, v26, v27, v66\n" \
    "s_waitcnt lgkmcnt(0)\n" \
    "v_smfmac_f32_16x16x64_bf16 v[32:35], v[28:31], a[56:63], v56\n" \
    "v_max3_u32 v66, v28, v29, v66\n" \
    "v_smfmac_f32_16x16x64_bf16 v[36:39], v[28:31], a[120:127], v56\n" \
    "v_max3_u32 v66, v30, v31, v66\n" \
    "v_cmp_eq_u32 vcc, -1, v66\n" \
    "v_smfmac_f32_16x16x64_bf16 v[40:43], v[0:3], a[128:135], v56\n" \
    "s_cbranch_vccnz GF1_%=\n" \
    "v_smfmac_f32_16x16x64_bf16 v[44:47], v[0:3], a[192:199], v56\n" \
    "ds_read_b128 v[68:71], v57 offset:16384\n" \
    "ds_read_b128 v[72:75], v57 offset:17408\n" \
    "v_smfmac_f32_16x16x64_bf16 v[40:43], v[4:7], a[136:143], v56\n" \
    "ds_read_b128 v[76:79], v57 offset:18432\n" \
    "v_permlane32_swap_b32 v32, v36\n" \
    "v_smfmac_f32_16x16x64_bf16 v[44:47], v[4:7], a[200:207], v56\n" \
    "v_permlane32_swap_b32 v33, v37\n" \
    "v_permlane32_swap_b32 v34, v38\n" \
    "v_smfmac_f32_16x16x64_bf16 v[40:43], v[8:11], a[144:151], v56\n" \
    "v_permlane32_swap_b32 v35, v39\n" \
    "v_add_f32 v32, v32, v36\n" \
    "v_smfmac_f32_16x16x64_bf16 v[44:47], v[8:11], a[208:215], v56\n" \
    "v_add_f32 v33, v33, v37\n" \
    "v_add_f32 v34, v34, v38\n" \
    "v_smfmac_f32_16x16x64_bf16 v[40:43], v[12:15], a[152:159], v56\n" \
    "v_add_f32 v35, v35, v39\n" \
    "v_cvt_pk_bf16_f32 v66, v32, v33\n" \
    "v_smfmac_f32_16x16x64_bf16 v[44:47], v[12:15], a[216:223], v56\n" \
    "v_cvt_pk_bf16_f32 v67, v34, v35\n" \
    "ds_write_b64 v59, v[66:67] offset:0\n" \
    "v_smfmac_f32_16x16x64_bf16 v[40:43], v[16:19], a[160:167], v56\n" \
    "ds_read_b128 v[80:83], v57 offset:19456\n" \
    "ds_read_b128 v[84:87], v57 offset:20480\n" \
    "v_smfmac_f32_16x16x64_bf16 v[44:47], v[16:19], a[224:231], v56\n" \
    "ds_read_b128 v[88:91], v57 offset:21504\n" \
    "ds_read_b128 v[92:95], v57 offset:22528\n" \
    "v_smfmac_f32_16x16x64_bf16 v[40:43], v[20:23], a[168:175], v56\n" \
    "ds_read_b128 v[96:99], v57 offset:23552\n" \
    "s_add_u32 s67, s86, 8192\n" \
    "v_smfmac_f32_16x16x64_bf16 v[44:47], v[20:23], a[232:239], v56\n" \
    "s_mov_b32 m0, s67\n" \
    "s_add_u32 s67, s86, 24576\n" \
    "v_smfmac_f32_16x16x64_bf16 v[40:43], v[24:27], a[176:183], v56\n" \
    "global_load_lds_dwordx4 v58, s[52:53] sc1\n" \
    "global_load_lds_dwordx4 v58, s[52:53] offset:1024 sc1\n" \
    "v_smfmac_f32_16x16x64_bf16 v[44:47], v[24:27], a[240:247], v56\n" \
    "s_mov_b32 m0, s67\n" \
    "s_add_u32 s52, s52, 262144\n" \
    "v_smfmac_f32_16x16x64_bf16 v[40:43], v[28:31], a[184:191], v56\n" \
    "global_load_lds_dwordx4 v58, s[54:55] sc1\n" \
    "global_load_lds_dwordx4 v58, s[54:55] offset:1024 sc1\n" \
    "v_smfmac_f32_16x16x64_bf16 v[44:47], v[28:31], a[248:255], v56\n" \
    "s_addc_u32 s53, s53, 0\n" \
    "s_add_u32 s54, s54, 262144\n" \
    "s_addc_u32 s55, s55, 0\n" \
    "v_smfmac_f32_16x16x64_bf16 v[48:51], v[0:3], v[128:135], v56\n" \
    "v_mov_b32 v116, 0\n" \
    "v_smfmac_f32_16x16x64_bf16 v[52:55], v[0:3], v[192:199], v56\n" \
    "v_mov_b32 v117, 0xf149f2ca\n" \
    "v_smfmac_f32_16x16x64_bf16 v[48:51], v[4:7], v[136:143], v56\n" \
    "s_mov_b32 vcc_lo, -1\n" \
    "s_mov_b32 vcc_hi, 0\n" \
    "v_cndmask_b32 v116, v117, v116, vcc\n" \
    "v_smfmac_f32_16x16x64_bf16 v[52:55], v[4:7], v[200:207], v56\n" \
    "v_permlane32_swap_b32 v40, v44\n" \
    "v_permlane32_swap_b32 v41, v45\n" \
    "v_smfmac_f32_16x16x64_bf16 v[48:51], v[8:11], v[144:151], v56\n" \
    "v_permlane32_swap_b32 v42, v46\n" \
    "v_permlane32_swap_b32 v43, v47\n" \
    "v_smfmac_f32_16x16x64_bf16 v[52:55], v[8:11], v[208:215], v56\n" \
    "v_add_f32 v40, v40, v44\n" \
    "v_add_f32 v41, v41, v45\n" \
    "v_add_f32 v42, v42, v46\n" \
    "v_smfmac_f32_16x16x64_bf16 v[48:51], v[12:15], v[152:159], v56\n" \
    "v_add_f32 v43, v43, v47\n" \
    "v_cvt_pk_bf16_f32 v66, v40, v41\n" \
    "v_cvt_pk_bf16_f32 v67, v42, v43\n" \
    "ds_write_b64 v59, v[66:67] offset:2048\n" \
    "s_cmp_lg_u32 s62, 0\n" \
    "s_cbranch_scc1 NK1_%=\n" \
    "global_store_dwordx2 v59, v[66:67], s[56:57] sc1\n" \
    "NK1_%=:\n" \
    "v_smfmac_f32_16x16x64_bf16 v[52:55], v[12:15], v[216:223], v56\n" \
    "v_mov_b32 v117, v116\n" \
    "v_mov_b32 v118, v116\n" \
    "v_mov_b32 v119, v116\n" \
    "s_waitcnt lgkmcnt(0)\n" \
    "s_barrier\n" \
    "ds_read_b64_tr_b16 v[32:33], v60 offset:0\n" \
    "ds_read_b64_tr_b16 v[34:35], v60 offset:256\n" \
    "ds_read_b64_tr_b16 v[100:101], v60 offset:2048\n" \
    "v_smfmac_f32_16x16x64_bf16 v[48:51], v[16:19], v[160:167], v56\n" \
    "ds_read_b64_tr_b16 v[102:103], v60 offset:2304\n" \
    "v_smfmac_f32_16x16x64_bf16 v[52:55], v[16:19], v[224:231], v56\n" \
    "ds_read_b64_tr_b16 v[36:37], v60 offset:512\n" \
    "v_smfmac_f32_16x16x64_bf16 v[48:51], v[20:23], v[168:175], v56\n" \
    "ds_read_b64_tr_b16 v[38:39], v60 offset:768\n" \
    "v_smfmac_f32_16x16x64_bf16 v[52:55], v[20:23], v[232:239], v56\n" \
    "ds_read_b64_tr_b16 v[104:105], v60 offset:2560\n" \
    "v_smfmac_f32_16x16x64_bf16 v[48:51], v[24:27], v[176:183], v56\n" \
    "ds_read_b64_tr_b16 v[106:107], v60 offset:2816\n" \
    "v_smfmac_f32_16x16x64_bf16 v[52:55], v[24:27], v[240:247], v56\n" \
    "ds_read_b64_tr_b16 v[40:41], v60 offset:1024\n" \
    "v_smfmac_f32_16x16x64_bf16 v[48:51], v[28:31], v[184:191], v56\n" \
    "ds_read_b64_tr_b16 v[42:43], v60 offset:1280\n" \
    "v_smfmac_f32_16x16x64_bf16 v[52:55], v[28:31], v[248:255], v56\n" \
    "ds_read_b64_tr_b16 v[108:109], v60 offset:3072\n" \
    "ds_read_b64_tr_b16 v[110:111], v60 offset:3328\n" \
    "ds_read_b64_tr_b16 v[44:45], v60 offset:1536\n" \
    "ds_read_b64_tr_b16 v[46:47], v60 offset:1792\n" \
    "ds_read_b64_tr_b16 v[112:113], v60 offset:3584\n" \
    "ds_read_b64_tr_b16 v[114:115], v60 offset:3840\n" \
    "s_waitcnt lgkmcnt(14)\n" \
    "v_mfma_f32_16x16x32_bf16 v[120:123], v[68:71], v[32:35], 0\n" \
    "v_mfma_f32_16x16x32_bf16 v[124:127], v[84:87], v[32:35], 0\n" \
    "s_waitcnt lgkmcnt(12)\n" \
    "v_mfma_f32_16x16x32_bf16 v[116:119], v[100:103], v[32:35], v[116:119]\n" \
    "v_permlane32_swap_b32 v48, v52\n" \
    "v_permlane32_swap_b32 v49, v53\n" \
    "s_waitcnt lgkmcnt(10)\n" \
    "v_mfma_f32_16x16x32_bf16 v[120:123], v[72:75], v[36:39], v[120:123]\n" \
    "v_permlane32_swap_b32 v50, v54\n" \
    "v_permlane32_swap_b32 v51, v55\n" \
    "v_mfma_f32_16x16x32_bf16 v[124:127], v[88:91], v[36:39], v[124:127]\n" \
    "v_add_f32 v48, v48, v52\n" \
    "v_add_f32 v49, v49, v53\n" \
    "s_waitcnt lgkmcnt(8)\n" \
    "v_mfma_f32_16x16x32_bf16 v[116:119], v[104:107], v[36:39], v[116:119]\n" \
    "v_add_f32 v50, v50, v54\n" \
    "v_add_f32 v51, v51, v55\n" \
    "s_waitcnt lgkmcnt(6)\n" \
    "v_mfma_f32_16x16x32_bf16 v[120:123], v[76:79], v[40:43], v[120:123]\n" \
    "v_cvt_pk_bf16_f32 v52, v48, v49\n" \
    "v_cvt_pk_bf16_f32 v53, v50, v51\n" \
    "v_mfma_f32_16x16x32_bf16 v[124:127], v[92:95], v[40:43], v[124:127]\n" \
    "s_waitcnt lgkmcnt(4)\n" \
    "v_mfma_f32_16x16x32_bf16 v[116:119], v[108:111], v[40:43], v[116:119]\n" \
    "s_waitcnt lgkmcnt(2)\n" \
    "v_mfma_f32_16x16x32_bf16 v[120:123], v[80:83], v[44:47], v[120:123]\n" \
    "v_mfma_f32_16x16x32_bf16 v[124:127], v[96:99], v[44:47], v[124:127]\n" \
    "s_waitcnt lgkmcnt(0)\n" \
    "v_mfma_f32_16x16x32_bf16 v[116:119], v[112:115], v[44:47], v[116:119]\n" \
    "s_cmp_eq_u32 s62, 0\n" \
    "s_cbranch_scc1 NV1_%=\n" \
    "global_store_dwordx2 v59, v[52:53], s[56:57] sc1\n" \
    "NV1_%=:\n" \
    "s_cmp_eq_u32 s60, 63\n" \
    "s_cbranch_scc1 FIN_%=\n" \
    "v_mov_b32 v54, v52\n" \
    "v_mov_b32 v55, v53\n" \
    "s_nop 0\n" \
    "v_permlane32_swap_b32 v52, v54\n" \
    "v_permlane32_swap_b32 v53, v55\n" \
    "ds_read_b128 v[68:71], v58 offset:32768\n" \
    "ds_read_b128 v[72:75], v58 offset:33792\n" \
    "v_max3_f32 v112, v120, v121, v122\n" \
    "ds_read_b128 v[0:3], v62 offset:0\n" \
    "v_max3_f32 v113, v123, v124, v125\n" \
    "ds_read_b128 v[4:7], v62 offset:1024\n" \
    "v_max3_f32 v66, v126, v127, v116\n" \
    "ds_read_b128 v[8:11], v62 offset:2048\n" \
    "v_max3_f32 v67, v117, v118, v119\n" \
    "ds_read_b128 v[12:15], v62 offset:3072\n" \
    "v_max3_f32 v112, v112, v113, v66\n" \
    "ds_read_b128 v[16:19], v62 offset:4096\n" \
    "v_max_f32 v112, v112, v67\n" \
    "ds_read_b128 v[20:23], v62 offset:5120\n" \
    "v_mov_b32 v113, v112\n" \
    "ds_read_b128 v[24:27], v62 offset:6144\n" \
    "s_nop 0\n" \
    "v_permlane16_swap_b32 v112, v113\n" \
    "ds_read_b128 v[28:31], v62 offset:7168\n" \
    "v_max_f32 v112, v112, v113\n" \
    "ds_read_b128 v[32:35], v62 offset:8192\n" \
    "v_mov_b32 v113, v112\n" \
    "ds_read_b128 v[36:39], v62 offset:9216\n" \
    "s_nop 0\n" \
    "v_permlane32_swap_b32 v112, v113\n" \
    "ds_read_b128 v[40:43], v62 offset:10240\n" \
    "v_max_f32 v112, v112, v113\n" \
    "ds_read_b128 v[44:47], v62 offset:11264\n" \
    "v_mul_f32 v112, s83, v112\n" \
    "v_fma_f32 v120, v120, s80, v112\n" \
    "v_fma_f32 v121, v121, s80, v112\n" \
    "v_exp_f32 v120, v120\n" \
    "v_fma_f32 v122, v122, s80, v112\n" \
    "v_exp_f32 v121, v121\n" \
    "v_fma_f32 v123, v123, s80, v112\n" \
    "v_exp_f32 v122, v122\n" \
    "v_fma_f32 v124, v124, s80, v112\n" \
    "v_cvt_pk_bf16_f32 v76, v120, v121\n" \
    "v_exp_f32 v123, v123\n" \
    "v_fma_f32 v125, v125, s80, v112\n" \
    "v_exp_f32 v124, v124\n" \
    "v_fma_f32 v126, v126, s80, v112\n" \
    "v_cvt_pk_bf16_f32 v77, v122, v123\n" \
    "v_exp_f32 v125, v125\n" \
    "v_fma_f32 v127, v127, s80, v112\n" \
    "v_exp_f32 v126, v126\n" \
    "v_fma_f32 v116, v116, s80, v112\n" \
    "v_cvt_pk_bf16_f32 v78, v124, v125\n" \
    "v_exp_f32 v127, v127\n" \
    "v_fma_f32 v117, v117, s80, v112\n" \
    "v_exp_f32 v116, v116\n" \
    "v_fma_f32 v118, v118, s80, v112\n" \
    "v_cvt_pk_bf16_f32 v79, v126, v127\n" \
    "s_waitcnt lgkmcnt(12)\n" \
    "v_add_f32 v113, v120, v121\n" \
    "v_mfma_f32_16x16x32_bf16 v[100:103], v[68:71], v[76:79], 0\n" \
    "v_exp_f32 v117, v117\n" \
    "v_fma_f32 v119, v119, s80, v112\n" \
    "v_mfma_f32_16x16x32_bf16 v[104:107], v[72:75], v[76:79], 0\n" \
    "v_exp_f32 v118, v118\n" \
    "v_cvt_pk_bf16_f32 v80, v116, v117\n" \
    "v_exp_f32 v119, v119\n" \
    "v_add_f32 v66, v122, v123\n" \
    "v_cvt_pk_bf16_f32 v81, v118, v119\n" \
    "v_add_f32 v67, v124, v125\n" \
    "v_add_f32 v113, v113, v66\n" \
    "v_mfma_f32_16x16x16_bf16 v[100:103], v[52:53], v[80:81], v[100:103]\n" \
    "v_add_f32 v66, v126, v127\n" \
    "v_mfma_f32_16x16x16_bf16 v[104:107], v[54:55], v[80:81], v[104:107]\n" \
    "v_add_f32 v67, v67, v66\n" \
    "v_add_f32 v66, v116, v117\n" \
    "v_add_f32 v113, v113, v67\n" \
    "v_add_f32 v67, v118, v119\n" \
    "v_add_f32 v66, v66, v67\n" \
    "v_add_f32 v113, v113, v66\n" \
    "v_mov_b32 v66, v113\n" \
    "s_nop 1\n" \
    "v_permlane16_swap_b32 v113, v66\n" \
    "v_add_f32 v113, v113, v66\n" \
    "v_mov_b32 v66, v113\n" \
    "s_nop 1\n" \
    "v_permlane32_swap_b32 v113, v66\n" \
    "v_add_f32 v113, v113, v66\n" \
    "v_rcp_f32 v113, v113\n" \
    "ds_read_b128 v[68:71], v62 offset:12288\n" \
    "ds_read_b128 v[72:75], v62 offset:13312\n" \
    "ds_read_b128 v[76:79], v62 offset:14336\n" \
    "ds_read_b128 v[80:83], v62 offset:15360\n" \
    "v_cndmask_b32 v100, v102, v100, s[76:77]\n" \
    "v_cndmask_b32 v101, v103, v101, s[76:77]\n" \
    "v_mul_f32 v100, v100, v113\n" \
    "v_mul_f32 v101, v101, v113\n" \
    "v_cvt_pk_bf16_f32 v100, v100, v101\n" \
    "ds_write_b32 v61, v100 offset:0\n" \
    "v_cndmask_b32 v104, v106, v104, s[76:77]\n" \
    "v_cndmask_b32 v105, v107, v105, s[76:77]\n" \
    "v_mul_f32 v104, v104, v113\n" \
    "v_mul_f32 v105, v105, v113\n" \
    "v_cvt_pk_bf16_f32 v104, v104, v105\n" \
    "ds_write_b32 v61, v104 offset:256\n" \
    "v_mov_b32 v84, 0\n" \
    "v_mov_b32 v85, 0\n" \
    "v_mov_b32 v86, 0\n" \
    "v_mov_b32 v87, 0\n" \
    "v_mov_b32 v88, 0\n" \
    "v_mov_b32 v89, 0\n" \
    "v_mov_b32 v90, 0\n" \
    "v_mov_b32 v91, 0\n" \
    "v_mov_b32 v92, 0\n" \
    "v_mov_b32 v93, 0\n" \
    "v_mov_b32 v94, 0\n" \
    "v_mov_b32 v95, 0\n" \
    "v_mov_b32 v96, 0\n" \
    "v_mov_b32 v97, 0\n" \
    "v_mov_b32 v98, 0\n" \
    "v_mov_b32 v99, 0\n" \
    "s_waitcnt lgkmcnt(0)\n" \
    "s_barrier\n" \
    "ds_read_b128 v[108:111], v57 offset:45056\n" \
    "ds_read_b128 v[112:115], v57 offset:46080\n" \
    "s_waitcnt lgkmcnt(1)\n" \
    "v_smfmac_f32_16x16x64_bf16 v[84:87], v[108:111], v[0:7], v56\n" \
    "v_smfmac_f32_16x16x64_bf16 v[88:91], v[108:111], v[16:23], v56\n" \
    "s_waitcnt lgkmcnt(0)\n" \
    "v_smfmac_f32_16x16x64_bf16 v[84:87], v[112:115], v[8:15], v56\n" \
    "v_smfmac_f32_16x16x64_bf16 v[88:91], v[112:115], v[24:31], v56\n" \
    "v_smfmac_f32_16x16x64_bf16 v[92:95], v[108:111], v[32:39], v56\n" \
    "v_smfmac_f32_16x16x64_bf16 v[96:99], v[108:111], v[68:75], v56\n" \
    "v_smfmac_f32_16x16x64_bf16 v[92:95], v[112:115], v[40:47], v56\n" \
    "v_smfmac_f32_16x16x64_bf16 v[96:99], v[112:115], v[76:83], v56\n" \
    "s_nop 3\n" \
    "v_permlane32_swap_b32 v84, v88\n" \
    "v_permlane32_swap_b32 v85, v89\n" \
    "v_permlane32_swap_b32 v86, v90\n" \
    "v_permlane32_swap_b32 v87, v91\n" \
    "v_add_f32 v84, v84, v88\n" \
    "v_add_f32 v85, v85, v89\n" \
    "v_add_f32 v86, v86, v90\n" \
    "v_add_f32 v87, v87, v91\n" \
    "v_or_b32 v84, 1, v84\n" \
    "v_or_b32 v85, 1, v85\n" \
    "v_or_b32 v86, 1, v86\n" \
    "v_or_b32 v87, 1, v87\n" \
    "global_store_dwordx4 v63, v[84:87], s[68:69] sc1\n" \
    "v_permlane32_swap_b32 v92, v96\n" \
    "v_permlane32_swap_b32 v93, v97\n" \
    "v_permlane32_swap_b32 v94, v98\n" \
    "v_permlane32_swap_b32 v95, v99\n" \
    "v_add_f32 v92, v92, v96\n" \
    "v_add_f32 v93, v93, v97\n" \
    "v_add_f32 v94, v94, v98\n" \
    "v_add_f32 v95, v95, v99\n" \
    "v_or_b32 v92, 1, v92\n" \
    "v_or_b32 v93, 1, v93\n" \
    "v_or_b32 v94, 1, v94\n" \
    "v_or_b32 v95, 1, v95\n" \
    "global_store_dwordx4 v63, v[92:95], s[70:71] sc1\n" \
    "s_cmp_lg_u32 s61, 0\n" \
    "s_cbranch_scc1 RN1_%=\n" \
    "s_mov_b32 s64, s81\n" \
    "s_cmp_eq_u32 s64, 0\n" \
    "s_cbranch_scc1 RP1_e_%=\n" \
    "RP1_%=:\n" \
    "s_sleep 1\n" \
    "s_sub_u32 s64, s64, 1\n" \
    "s_cmp_lg_u32 s64, 0\n" \
    "s_cbranch_scc1 RP1_%=\n" \
    "RP1_e_%=:\n" \
    "s_mov_b32 s65, 0x10000\n" \
    "RG1_%=:\n" \
    "global_load_dwordx4 v[0:3], v57, s[72:73] offset:0 sc1\n" \
    "global_load_dwordx4 v[4:7], v57, s[72:73] offset:1024 sc1\n" \
    "global_load_dwordx4 v[8:11], v57, s[72:73] offset:2048 sc1\n" \
    "global_load_dwordx4 v[12:15], v57, s[72:73] offset:3072 sc1\n" \
    "global_load_dwordx4 v[16:19], v57, s[74:75] offset:0 sc1\n" \
    "global_load_dwordx4 v[20:23], v57, s[74:75] offset:1024 sc1\n" \
    "global_load_dwordx4 v[24:27], v57, s[74:75] offset:2048 sc1\n" \
    "global_load_dwordx4 v[28:31], v57, s[74:75] offset:3072 sc1\n" \
    "s_waitcnt vmcnt(0)\n" \
    "v_bitop3_b32 v32, v0, v1, v2 bitop3:0x80\n" \
    "v_bitop3_b32 v33, v16, v17, v18 bitop3:0x80\n" \
    "v_bitop3_b32 v32, v32, v3, v4 bitop3:0x80\n" \
    "v_bitop3_b32 v33, v33, v19, v20 bitop3:0x80\n" \
    "v_bitop3_b32 v32, v32, v5, v6 bitop3:0x80\n" \
    "v_bitop3_b32 v33, v33, v21, v22 bitop3:0x80\n" \
    "v_bitop3_b32 v32, v32, v7, v8 bitop3:0x80\n" \
    "v_bitop3_b32 v33, v33, v23, v24 bitop3:0x80\n" \
    "v_bitop3_b32 v32, v32, v9, v10 bitop3:0x80\n" \
    "v_bitop3_b32 v33, v33, v25, v26 bitop3:0x80\n" \
    "v_bitop3_b32 v32, v32, v11, v12 bitop3:0x80\n" \
    "v_bitop3_b32 v33, v33, v27, v28 bitop3:0x80\n" \
    "v_bitop3_b32 v32, v32, v13, v14 bitop3:0x80\n" \
    "v_bitop3_b32 v33, v33, v29, v30 bitop3:0x80\n" \
    "v_bitop3_b32 v32, v32, v15, v15 bitop3:0x80\n" \
    "v_bitop3_b32 v33, v33, v31, v31 bitop3:0x80\n" \
    "v_bitop3_b32 v32, v32, v33, v33 bitop3:0x80\n" \
    "v_and_b32 v32, 1, v32\n" \
    "v_cmp_ne_u32 vcc, 1, v32\n" \
    "v_pk_add_f32 v[0:1], v[0:1], v[4:5]\n" \
    "v_pk_add_f32 v[2:3], v[2:3], v[6:7]\n" \
    "v_pk_add_f32 v[8:9], v[8:9], v[12:13]\n" \
    "v_pk_add_f32 v[10:11], v[10:11], v[14:15]\n" \
    "v_pk_add_f32 v[16:17], v[16:17], v[20:21]\n" \
    "v_pk_add_f32 v[18:19], v[18:19], v[22:23]\n" \
    "v_pk_add_f32 v[24:25], v[24:25], v[28:29]\n" \
    "v_pk_add_f32 v[26:27], v[26:27], v[30:31]\n" \
    "s_cbranch_vccz RD1_%=\n" \
    "s_sub_u32 s65, s65, 1\n" \
    "s_cmp_lg_u32 s65, 0\n" \
    "s_cbranch_scc1 RG1_%=\n" \
    "s_mov_b32 s66, 1\n" \
    "s_branch FIN_%=\n" \
    "RD1_%=:\n" \
    "v_pk_add_f32 v[0:1], v[0:1], v[8:9]\n" \
    "v_pk_add_f32 v[2:3], v[2:3], v[10:11]\n" \
    "v_pk_add_f32 v[16:17], v[16:17], v[24:25]\n" \
    "v_pk_add_f32 v[18:19], v[18:19], v[26:27]\n" \
    "v_pk_add_f32 v[0:1], v[0:1], v[16:17]\n" \
    "v_pk_add_f32 v[2:3], v[2:3], v[18:19]\n" \
    "s_nop 0\n" \
    "v_permlane32_swap_b32 v0, v1\n" \
    "v_permlane32_swap_b32 v2, v3\n" \
    "v_add_f32 v0, v0, v1\n" \
    "v_add_f32 v2, v2, v3\n" \
    "s_nop 1\n" \
    "v_permlane16_swap_b32 v0, v2\n" \
    "v_add_f32 v0, v0, v2\n" \
    "s_nop 1\n" \
    "v_mov_b32_dpp v1, v0 quad_perm:[1,0,3,2] row_mask:0xf bank_mask:0xf bound_ctrl:1\n" \
    "v_cvt_pk_bf16_f32 v0, v0, v1\n" \
    "v_min_u32 v0, 0xfffffffe, v0\n" \
    "s_lshl_b32 s64, s62, 5\n" \
    "s_add_u32 s64, s64, s63\n" \
    "s_lshl_b32 s64, s64, 7\n" \
    "s_add_u32 s64, s64, 16384\n" \
    "s_add_u32 s88, s50, s64\n" \
    "s_addc_u32 s89, s51, 0\n" \
    "s_mov_b64 exec, s[78:79]\n" \
    "global_store_dword v64, v0, s[88:89] sc1\n" \
    "s_mov_b64 exec, -1\n" \
    "s_branch RB1_%=\n" \
    "RN1_%=:\n" \
    "s_waitcnt vmcnt(0)\n" \
    "RB1_%=:\n" \
    "s_barrier\n" \
    "s_cmp_lg_u32 s61, 1\n" \
    "s_cbranch_scc1 NF1_%=\n" \
    "v_mov_b32 v0, 0\n" \
    "s_mov_b64 exec, 1\n" \
    "global_store_dword v0, v0, s[58:59] sc1\n" \
    "s_mov_b64 exec, -1\n" \
    "NF1_%=:\n" \
    "v_mov_b32 v32, 0\n" \
    "v_mov_b32 v33, 0\n" \
    "v_mov_b32 v34, 0\n" \
    "v_mov_b32 v35, 0\n" \
    "v_mov_b32 v36, 0\n" \
    "v_mov_b32 v37, 0\n" \
    "v_mov_b32 v38, 0\n" \
    "v_mov_b32 v39, 0\n" \
    "v_mov_b32 v40, 0\n" \
    "v_mov_b32 v41, 0\n" \
    "v_mov_b32 v42, 0\n" \
    "v_mov_b32 v43, 0\n" \
    "v_mov_b32 v44, 0\n" \
    "v_mov_b32 v45, 0\n" \
    "v_mov_b32 v46, 0\n" \
    "v_mov_b32 v47, 0\n" \
    "v_mov_b32 v48, 0\n" \
    "v_mov_b32 v49, 0\n" \
    "v_mov_b32 v50, 0\n" \
    "v_mov_b32 v51, 0\n" \
    "v_mov_b32 v52, 0\n" \
    "v_mov_b32 v53, 0\n" \
    "v_mov_b32 v54, 0\n" \
    "v_mov_b32 v55, 0\n" \
    "s_add_u32 s50, s50, 16384\n" \
    "s_addc_u32 s51, s51, 0\n" \
    "s_add_u32 s56, s56, 65536\n" \
    "s_addc_u32 s57, s57, 0\n" \
    "s_add_u32 s58, s58, 4\n" \
    "s_addc_u32 s59, s59, 0\n" \
    "s_add_u32 s60, s60, 1\n" \
    "s_add_u32 s64, s60, 1\n" \
    "s_lshr_b32 s64, s64, 2\n" \
    "s_min_u32 s64, s64, 15\n" \
    "s_lshl_b32 s64, s64, 2\n" \
    "v_mov_b32 v67, s64\n" \
    "global_load_dword v65, v67, s[84:85] sc1\n" \
    "s_mov_b32 s64, s82\n" \
    "s_cmp_eq_u32 s64, 0\n" \
    "s_cbranch_scc1 GP1_e_%=\n" \
    "GP1_%=:\n" \
    "s_sleep 1\n" \
    "s_sub_u32 s64, s64, 1\n" \
    "s_cmp_lg_u32 s64, 0\n" \
    "s_cbranch_scc1 GP1_%=\n" \
    "GP1_e_%=:\n" \
    "s_waitcnt vmcnt(0)\n" \
    "v_cmp_ne_u32 vcc, 0, v65\n" \
    "s_nop 1\n" \
    "s_cbranch_vccz XW1_d_%=\n" \
    "s_mov_b32 s65, 0x80000\n" \
    "XW1_%=:\n" \
    "global_load_dword v65, v67, s[84:85] sc1\n" \
    "s_waitcnt vmcnt(0)\n" \
    "v_cmp_ne_u32 vcc, 0, v65\n" \
    "s_nop 1\n" \
    "s_cbranch_vccz XW1_ok_%=\n" \
    "s_sleep 2\n" \
    "s_sub_u32 s65, s65, 1\n" \
    "s_cmp_lg_u32 s65, 0\n" \
    "s_cbranch_scc1 XW1_%=\n" \
    "s_mov_b32 s66, 8\n" \
    "s_branch FIN_%=\n" \
    "XW1_ok_%=:\n" \
    "XW1_d_%=:\n" \
    "s_mov_b32 s65, 0x10000\n" \
    "s_branch GA0_%=\n" \
    "GF0_%=:\n" \
    "s_nop 7\n" \
    "v_mov_b32 v32, 0\n" \
    "v_mov_b32 v33, 0\n" \
    "v_mov_b32 v34, 0\n" \
    "v_mov_b32 v35, 0\n" \
    "v_mov_b32 v36, 0\n" \
    "v_mov_b32 v37, 0\n" \
    "v_mov_b32 v38, 0\n" \
    "v_mov_b32 v39, 0\n" \
    "v_mov_b32 v40, 0\n" \
    "v_mov_b32 v41, 0\n" \
    "v_mov_b32 v42, 0\n" \
    "v_mov_b32 v43, 0\n" \
    "s_barrier\n" \
    "s_sub_u32 s65, s65, 1\n" \
    "s_cmp_lg_u32 s65, 0\n" \
    "s_cbranch_scc1 GA0_%=\n" \
    "s_mov_b32 s66, 2\n" \
    "s_branch FIN_%=\n" \
    "GF1_%=:\n" \
    "s_nop 7\n" \
    "v_mov_b32 v32, 0\n" \
    "v_mov_b32 v33, 0\n" \
    "v_mov_b32 v34, 0\n" \
    "v_mov_b32 v35, 0\n" \
    "v_mov_b32 v36, 0\n" \
    "v_mov_b32 v37, 0\n" \
    "v_mov_b32 v38, 0\n" \
    "v_mov_b32 v39, 0\n" \
    "v_mov_b32 v40, 0\n" \
    "v_mov_b32 v41, 0\n" \
    "v_mov_b32 v42, 0\n" \
    "v_mov_b32 v43, 0\n" \
    "s_barrier\n" \
    "s_sub_u32 s65, s65, 1\n" \
    "s_cmp_lg_u32 s65, 0\n" \
    "s_cbranch_scc1 GA1_%=\n" \
    "s_mov_b32 s66, 2\n" \
    "s_branch FIN_%=\n" \
    "FIN_%=:\n" \
    "s_waitcnt vmcnt(0) lgkmcnt(0)\n" \
    "s_barrier\n" \
    "s_cmp_lg_u32 s61, 1\n" \
    "s_cbranch_scc1 FN2_%=\n" \
    "v_mov_b32 v0, 0\n" \
    "s_mov_b64 exec, 1\n" \
    "global_store_dword v0, v0, s[58:59] sc1\n" \
    "s_mov_b64 exec, -1\n" \
    "FN2_%=:\n" \
    "s_waitcnt vmcnt(0)\n" \
    "s_mov_b32 %0, s66\n"
#define CHAIN_CLOBBERS "v0", "v1", "v2", "v3", "v4", "v5", "v6", "v7", "v8", "v9", "v10", "v11", "v12", "v13", "v14", "v15", "v16", "v17", "v18", "v19", "v20", "v21", "v22", "v23", "v24", "v25", "v26", "v27", "v28", "v29", "v30", "v31", "v32", "v33", "v34", "v35", "v36", "v37", "v38", "v39", "v40", "v41", "v42", "v43", "v44", "v45", "v46", "v47", "v48", "v49", "v50", "v51", "v52", "v53", "v54", "v55", "v56", "v57", "v58", "v59", "v60", "v61", "v62", "v63", "v64", "v65", "v66", "v67", "v68", "v69", "v70", "v71", "v72", "v73", "v74", "v75", "v76", "v77", "v78", "v79", "v80", "v81", "v82", "v83", "v84", "v85", "v86", "v87", "v88", "v89", "v90", "v91", "v92", "v93", "v94", "v95", "v96", "v97", "v98", "v99", "v100", "v101", "v102", "v103", "v104", "v105", "v106", "v107", "v108", "v109", "v110", "v111", "v112", "v113", "v114", "v115", "v116", "v117", "v118", "v119", "v120", "v121", "v122", "v123", "v124", "v125", "v126", "v127", "v128", "v129", "v130", "v131", "v132", "v133", "v134", "v135", "v136", "v137", "v138", "v139", "v140", "v141", "v142", "v143", "v144", "v145", "v146", "v147", "v148", "v149", "v150", "v151", "v152", "v153", "v154", "v155", "v156", "v157", "v158", "v159", "v160", "v161", "v162", "v163", "v164", "v165", "v166", "v167", "v168", "v169", "v170", "v171", "v172", "v173", "v174", "v175", "v176", "v177", "v178", "v179", "v180", "v181", "v182", "v183", "v184", "v185", "v186", "v187", "v188", "v189", "v190", "v191", "v192", "v193", "v194", "v195", "v196", "v197", "v198", "v199", "v200", "v201", "v202", "v203", "v204", "v205", "v206", "v207", "v208", "v209", "v210", "v211", "v212", "v213", "v214", "v215", "v216", "v217", "v218", "v219", "v220", "v221", "v222", "v223", "v224", "v225", "v226", "v227", "v228", "v229", "v230", "v231", "v232", "v233", "v234", "v235", "v236", "v237", "v238", "v239", "v240", "v241", "v242", "v243", "v244", "v245", "v246", "v247", "v248", "v249", "v250", "v251", "v252", "v253", "v254", "v255", "a0", "a1", "a2", "a3", "a4", "a5", "a6", "a7", "a8", "a9", "a10", "a11", "a12", "a13", "a14", "a15", "a16", "a17", "a18", "a19", "a20", "a21", "a22", "a23", "a24", "a25", "a26", "a27", "a28", "a29", "a30", "a31", "a32", "a33", "a34", "a35", "a36", "a37", "a38", "a39", "a40", "a41", "a42", "a43", "a44", "a45", "a46", "a47", "a48", "a49", "a50", "a51", "a52", "a53", "a54", "a55", "a56", "a57", "a58", "a59", "a60", "a61", "a62", "a63", "a64", "a65", "a66", "a67", "a68", "a69", "a70", "a71", "a72", "a73", "a74", "a75", "a76", "a77", "a78", "a79", "a80", "a81", "a82", "a83", "a84", "a85", "a86", "a87", "a88", "a89", "a90", "a91", "a92", "a93", "a94", "a95", "a96", "a97", "a98", "a99", "a100", "a101", "a102", "a103", "a104", "a105", "a106", "a107", "a108", "a109", "a110", "a111", "a112", "a113", "a114", "a115", "a116", "a117", "a118", "a119", "a120", "a121", "a122", "a123", "a124", "a125", "a126", "a127", "a128", "a129", "a130", "a131", "a132", "a133", "a134", "a135", "a136", "a137", "a138", "a139", "a140", "a141", "a142", "a143", "a144", "a145", "a146", "a147", "a148", "a149", "a150", "a151", "a152", "a153", "a154", "a155", "a156", "a157", "a158", "a159", "a160", "a161", "a162", "a163", "a164", "a165", "a166", "a167", "a168", "a169", "a170", "a171", "a172", "a173", "a174", "a175", "a176", "a177", "a178", "a179", "a180", "a181", "a182", "a183", "a184", "a185", "a186", "a187", "a188", "a189", "a190", "a191", "a192", "a193", "a194", "a195", "a196", "a197", "a198", "a199", "a200", "a201", "a202", "a203", "a204", "a205", "a206", "a207", "a208", "a209", "a210", "a211", "a212", "a213", "a214", "a215", "a216", "a217", "a218", "a219", "a220", "a221", "a222", "a223", "a224", "a225", "a226", "a227", "a228", "a229", "a230", "a231", "a232", "a233", "a234", "a235", "a236", "a237", "a238", "a239", "a240", "a241", "a242", "a243", "a244", "a245", "a246", "a247", "a248", "a249", "a250", "a251", "a252", "a253", "a254", "a255", "s40", "s41", "s42", "s43", "s44", "s45", "s46", "s47", "s48", "s49", "s50", "s51", "s52", "s53", "s54", "s55", "s56", "s57", "s58", "s59", "s60", "s61", "s62", "s63", "s64", "s65", "s66", "s67", "s68", "s69", "s70", "s71", "s72", "s73", "s74", "s75", "s76", "s77", "s78", "s79", "s80", "s81", "s82", "s83", "s84", "s85", "s86", "s87", "s88", "s89", "s90", "s91", "s92", "s93", "s94", "s95", "s96", "s97", "s98", "s99", "vcc", "scc", "memory"

#define GEMM_ASM \
    "s_mov_b64 s[40:41], %0\n" \
    "s_mov_b64 s[42:43], %1\n" \
    "s_mov_b64 s[44:45], %2\n" \
    "s_mov_b64 s[46:47], %3\n" \
    "s_mov_b64 s[48:49], %4\n" \
    "s_mov_b64 s[50:51], %5\n" \
    "s_mov_b32 s52, %6\n" \
    "s_mov_b32 s53, %7\n" \
    "s_mov_b32 s54, %8\n" \
    "s_mov_b32 s55, %9\n" \
    "s_mov_b32 s76, 0xbfb8aa3b\n" \
    "s_mov_b32 s77, 0\n" \
    "s_mov_b32 s56, 0\n" \
    "s_lshl_b32 s68, s53, 13\n" \
    "v_mbcnt_lo_u32_b32 v0, -1, 0\n" \
    "v_mbcnt_hi_u32_b32 v0, -1, v0\n" \
    "v_and_b32 v1, 15, v0\n" \
    "v_lshrrev_b32 v2, 4, v0\n" \
    "v_lshlrev_b32 v140, 4, v0\n" \
    "v_add_u32 v3, 0, v2\n" \
    "v_xor_b32 v4, v3, v1\n" \
    "v_lshlrev_b32 v4, 4, v4\n" \
    "v_lshl_add_u32 v128, v3, 10, v4\n" \
    "v_add_u32 v3, 4, v2\n" \
    "v_xor_b32 v4, v3, v1\n" \
    "v_lshlrev_b32 v4, 4, v4\n" \
    "v_lshl_add_u32 v129, v3, 10, v4\n" \
    "v_add_u32 v3, 8, v2\n" \
    "v_xor_b32 v4, v3, v1\n" \
    "v_lshlrev_b32 v4, 4, v4\n" \
    "v_lshl_add_u32 v130, v3, 10, v4\n" \
    "v_add_u32 v3, 12, v2\n" \
    "v_xor_b32 v4, v3, v1\n" \
    "v_lshlrev_b32 v4, 4, v4\n" \
    "v_lshl_add_u32 v131, v3, 10, v4\n" \
    "v_xor_b32 v3, v2, v1\n" \
    "v_lshlrev_b32 v3, 4, v3\n" \
    "v_lshl_add_u32 v3, v1, 8, v3\n" \
    "v_xor_b32 v132, 0, v3\n" \
    "v_add_u32 v136, 0x10000, v132\n" \
    "v_xor_b32 v133, 64, v3\n" \
    "v_add_u32 v137, 0x10000, v133\n" \
    "v_xor_b32 v134, 128, v3\n" \
    "v_add_u32 v138, 0x10000, v134\n" \
    "v_xor_b32 v135, 192, v3\n" \
    "v_add_u32 v139, 0x10000, v135\n" \
    "; ---- weights: 8 stages of 64 k-rows, [W0 16 KB | W1 16 KB] per stage, ring of four 32 KB buffers\n" \
    "v_add_u32 v3, 0, v2\n" \
    "v_mul_lo_u32 v3, v3, s54\n" \
    "v_lshlrev_b32 v4, 1, v2\n" \
    "v_xor_b32 v4, 0, v4\n" \
    "v_xor_b32 v4, v4, v1\n" \
    "v_lshl_add_u32 v8, v4, 4, v3\n" \
    "v_add_u32 v3, 4, v2\n" \
    "v_mul_lo_u32 v3, v3, s54\n" \
    "v_lshlrev_b32 v4, 1, v2\n" \
    "v_xor_b32 v4, 0, v4\n" \
    "v_xor_b32 v4, v4, v1\n" \
    "v_lshl_add_u32 v9, v4, 4, v3\n" \
    "v_add_u32 v3, 8, v2\n" \
    "v_mul_lo_u32 v3, v3, s54\n" \
    "v_lshlrev_b32 v4, 1, v2\n" \
    "v_xor_b32 v4, 8, v4\n" \
    "v_xor_b32 v4, v4, v1\n" \
    "v_lshl_add_u32 v10, v4, 4, v3\n" \
    "v_add_u32 v3, 12, v2\n" \
    "v_mul_lo_u32 v3, v3, s54\n" \
    "v_lshlrev_b32 v4, 1, v2\n" \
    "v_xor_b32 v4, 8, v4\n" \
    "v_xor_b32 v4, v4, v1\n" \
    "v_lshl_add_u32 v11, v4, 4, v3\n" \
    "s_lshl_b32 s57, s53, 4\n" \
    "s_mul_i32 s57, s57, s54\n" \
    "s_add_u32 s40, s40, s57\n" \
    "s_addc_u32 s41, s41, 0\n" \
    "s_add_u32 s42, s42, s57\n" \
    "s_addc_u32 s43, s43, 0\n" \
    "s_lshl_b32 s58, s54, 6\n" \
    "s_mov_b32 s66, s58\n" \
    "s_lshl_b32 s67, s53, 12\n" \
    "v_lshrrev_b32 v5, 2, v1\n" \
    "v_and_b32 v6, 1, v2\n" \
    "v_lshl_add_u32 v6, v6, 2, v5\n" \
    "v_lshlrev_b32 v6, 1, v6\n" \
    "v_and_b32 v7, 3, v1\n" \
    "v_lshrrev_b32 v7, 1, v7\n" \
    "v_xor_b32 v6, v6, v7\n" \
    "v_lshl_add_u32 v5, v2, 3, v5\n" \
    "v_lshlrev_b32 v5, 8, v5\n" \
    "v_lshl_add_u32 v5, v6, 4, v5\n" \
    "v_and_b32 v7, 1, v1\n" \
    "v_lshl_add_u32 v12, v7, 3, v5\n" \
    "s_and_b32 s57, s53, 1\n" \
    "s_lshl_b32 s57, s57, 2\n" \
    "s_add_u32 s57, s57, 0\n" \
    "s_lshr_b32 s58, s53, 1\n" \
    "s_lshl_b32 s64, s53, 1\n" \
    "s_add_u32 s64, s64, 0\n" \
    "s_cmp_eq_u32 s52, 2\n" \
    "s_cselect_b32 s57, s64, s57\n" \
    "s_cselect_b32 s58, 0, s58\n" \
    "s_lshl_b32 s57, s57, 5\n" \
    "s_lshl_b32 s58, s58, 14\n" \
    "v_xor_b32 v13, s57, v12\n" \
    "v_add_u32 v13, s58, v13\n" \
    "v_add_u32 v17, 0x10000, v13\n" \
    "s_and_b32 s57, s53, 1\n" \
    "s_lshl_b32 s57, s57, 2\n" \
    "s_add_u32 s57, s57, 1\n" \
    "s_lshr_b32 s58, s53, 1\n" \
    "s_lshl_b32 s64, s53, 1\n" \
    "s_add_u32 s64, s64, 1\n" \
    "s_cmp_eq_u32 s52, 2\n" \
    "s_cselect_b32 s57, s64, s57\n" \
    "s_cselect_b32 s58, 0, s58\n" \
    "s_lshl_b32 s57, s57, 5\n" \
    "s_lshl_b32 s58, s58, 14\n" \
    "v_xor_b32 v14, s57, v12\n" \
    "v_add_u32 v14, s58, v14\n" \
    "v_add_u32 v18, 0x10000, v14\n" \
    "s_and_b32 s57, s53, 1\n" \
    "s_lshl_b32 s57, s57, 2\n" \
    "s_add_u32 s57, s57, 2\n" \
    "s_lshr_b32 s58, s53, 1\n" \
    "s_lshl_b32 s64, s53, 1\n" \
    "s_add_u32 s64, s64, 0\n" \
    "s_cmp_eq_u32 s52, 2\n" \
    "s_cselect_b32 s57, s64, s57\n" \
    "s_cselect_b32 s58, 1, s58\n" \
    "s_lshl_b32 s57, s57, 5\n" \
    "s_lshl_b32 s58, s58, 14\n" \
    "v_xor_b32 v15, s57, v12\n" \
    "v_add_u32 v15, s58, v15\n" \
    "v_add_u32 v19, 0x10000, v15\n" \
    "s_and_b32 s57, s53, 1\n" \
    "s_lshl_b32 s57, s57, 2\n" \
    "s_add_u32 s57, s57, 3\n" \
    "s_lshr_b32 s58, s53, 1\n" \
    "s_lshl_b32 s64, s53, 1\n" \
    "s_add_u32 s64, s64, 1\n" \
    "s_cmp_eq_u32 s52, 2\n" \
    "s_cselect_b32 s57, s64, s57\n" \
    "s_cselect_b32 s58, 1, s58\n" \
    "s_lshl_b32 s57, s57, 5\n" \
    "s_lshl_b32 s58, s58, 14\n" \
    "v_xor_b32 v16, s57, v12\n" \
    "v_add_u32 v16, s58, v16\n" \
    "v_add_u32 v20, 0x10000, v16\n" \
    "s_add_u32 m0, s67, 0\n" \
    "s_nop 0\n" \
    "global_load_lds_dwordx4 v8, s[40:41]\n" \
    "s_add_u32 m0, s67, 1024\n" \
    "s_nop 0\n" \
    "global_load_lds_dwordx4 v9, s[40:41]\n" \
    "s_add_u32 m0, s67, 2048\n" \
    "s_nop 0\n" \
    "global_load_lds_dwordx4 v10, s[40:41]\n" \
    "s_add_u32 m0, s67, 3072\n" \
    "s_nop 0\n" \
    "global_load_lds_dwordx4 v11, s[40:41]\n" \
    "s_add_u32 s40, s40, s66\n" \
    "s_addc_u32 s41, s41, 0\n" \
    "s_add_u32 m0, s67, 16384\n" \
    "s_nop 0\n" \
    "global_load_lds_dwordx4 v8, s[42:43]\n" \
    "s_add_u32 m0, s67, 17408\n" \
    "s_nop 0\n" \
    "global_load_lds_dwordx4 v9, s[42:43]\n" \
    "s_add_u32 m0, s67, 18432\n" \
    "s_nop 0\n" \
    "global_load_lds_dwordx4 v10, s[42:43]\n" \
    "s_add_u32 m0, s67, 19456\n" \
    "s_nop 0\n" \
    "global_load_lds_dwordx4 v11, s[42:43]\n" \
    "s_add_u32 s42, s42, s66\n" \
    "s_addc_u32 s43, s43, 0\n" \
    "s_add_u32 m0, s67, 32768\n" \
    "s_nop 0\n" \
    "global_load_lds_dwordx4 v8, s[40:41]\n" \
    "s_add_u32 m0, s67, 33792\n" \
    "s_nop 0\n" \
    "global_load_lds_dwordx4 v9, s[40:41]\n" \
    "s_add_u32 m0, s67, 34816\n" \
    "s_nop 0\n" \
    "global_load_lds_dwordx4 v10, s[40:41]\n" \
    "s_add_u32 m0, s67, 35840\n" \
    "s_nop 0\n" \
    "global_load_lds_dwordx4 v11, s[40:41]\n" \
    "s_add_u32 s40, s40, s66\n" \
    "s_addc_u32 s41, s41, 0\n" \
    "s_add_u32 m0, s67, 49152\n" \
    "s_nop 0\n" \
    "global_load_lds_dwordx4 v8, s[42:43]\n" \
    "s_add_u32 m0, s67, 50176\n" \
    "s_nop 0\n" \
    "global_load_lds_dwordx4 v9, s[42:43]\n" \
    "s_add_u32 m0, s67, 51200\n" \
    "s_nop 0\n" \
    "global_load_lds_dwordx4 v10, s[42:43]\n" \
    "s_add_u32 m0, s67, 52224\n" \
    "s_nop 0\n" \
    "global_load_lds_dwordx4 v11, s[42:43]\n" \
    "s_add_u32 s42, s42, s66\n" \
    "s_addc_u32 s43, s43, 0\n" \
    "s_add_u32 m0, s67, 65536\n" \
    "s_nop 0\n" \
    "global_load_lds_dwordx4 v8, s[40:41]\n" \
    "s_add_u32 m0, s67, 66560\n" \
    "s_nop 0\n" \
    "global_load_lds_dwordx4 v9, s[40:41]\n" \
    "s_add_u32 m0, s67, 67584\n" \
    "s_nop 0\n" \
    "global_load_lds_dwordx4 v10, s[40:41]\n" \
    "s_add_u32 m0, s67, 68608\n" \
    "s_nop 0\n" \
    "global_load_lds_dwordx4 v11, s[40:41]\n" \
    "s_add_u32 s40, s40, s66\n" \
    "s_addc_u32 s41, s41, 0\n" \
    "s_add_u32 m0, s67, 81920\n" \
    "s_nop 0\n" \
    "global_load_lds_dwordx4 v8, s[42:43]\n" \
    "s_add_u32 m0, s67, 82944\n" \
    "s_nop 0\n" \
    "global_load_lds_dwordx4 v9, s[42:43]\n" \
    "s_add_u32 m0, s67, 83968\n" \
    "s_nop 0\n" \
    "global_load_lds_dwordx4 v10, s[42:43]\n" \
    "s_add_u32 m0, s67, 84992\n" \
    "s_nop 0\n" \
    "global_load_lds_dwordx4 v11, s[42:43]\n" \
    "s_add_u32 s42, s42, s66\n" \
    "s_addc_u32 s43, s43, 0\n" \
    "s_add_u32 m0, s67, 98304\n" \
    "s_nop 0\n" \
    "global_load_lds_dwordx4 v8, s[40:41]\n" \
    "s_add_u32 m0, s67, 99328\n" \
    "s_nop 0\n" \
    "global_load_lds_dwordx4 v9, s[40:41]\n" \
    "s_add_u32 m0, s67, 100352\n" \
    "s_nop 0\n" \
    "global_load_lds_dwordx4 v10, s[40:41]\n" \
    "s_add_u32 m0, s67, 101376\n" \
    "s_nop 0\n" \
    "global_load_lds_dwordx4 v11, s[40:41]\n" \
    "s_add_u32 s40, s40, s66\n" \
    "s_addc_u32 s41, s41, 0\n" \
    "s_add_u32 m0, s67, 114688\n" \
    "s_nop 0\n" \
    "global_load_lds_dwordx4 v8, s[42:43]\n" \
    "s_add_u32 m0, s67, 115712\n" \
    "s_nop 0\n" \
    "global_load_lds_dwordx4 v9, s[42:43]\n" \
    "s_add_u32 m0, s67, 116736\n" \
    "s_nop 0\n" \
    "global_load_lds_dwordx4 v10, s[42:43]\n" \
    "s_add_u32 m0, s67, 117760\n" \
    "s_nop 0\n" \
    "global_load_lds_dwordx4 v11, s[42:43]\n" \
    "s_add_u32 s42, s42, s66\n" \
    "s_addc_u32 s43, s43, 0\n" \
    "s_waitcnt vmcnt(24)\n" \
    "s_barrier\n" \
    "ds_read_b64_tr_b16 a[0:1], v13 offset:0\n" \
    "ds_read_b64_tr_b16 a[2:3], v13 offset:1024\n" \
    "ds_read_b64_tr_b16 a[4:5], v13 offset:8192\n" \
    "ds_read_b64_tr_b16 a[6:7], v13 offset:9216\n" \
    "ds_read_b64_tr_b16 a[64:65], v14 offset:0\n" \
    "ds_read_b64_tr_b16 a[66:67], v14 offset:1024\n" \
    "ds_read_b64_tr_b16 a[68:69], v14 offset:8192\n" \
    "ds_read_b64_tr_b16 a[70:71], v14 offset:9216\n" \
    "ds_read_b64_tr_b16 a[128:129], v15 offset:0\n" \
    "ds_read_b64_tr_b16 a[130:131], v15 offset:1024\n" \
    "ds_read_b64_tr_b16 a[132:133], v15 offset:8192\n" \
    "ds_read_b64_tr_b16 a[134:135], v15 offset:9216\n" \
    "ds_read_b64_tr_b16 a[192:193], v16 offset:0\n" \
    "ds_read_b64_tr_b16 a[194:195], v16 offset:1024\n" \
    "ds_read_b64_tr_b16 a[196:197], v16 offset:8192\n" \
    "ds_read_b64_tr_b16 a[198:199], v16 offset:9216\n" \
    "s_waitcnt lgkmcnt(0)\n" \
    "s_barrier\n" \
    "s_add_u32 m0, s67, 0\n" \
    "s_nop 0\n" \
    "global_load_lds_dwordx4 v8, s[40:41]\n" \
    "s_add_u32 m0, s67, 1024\n" \
    "s_nop 0\n" \
    "global_load_lds_dwordx4 v9, s[40:41]\n" \
    "s_add_u32 m0, s67, 2048\n" \
    "s_nop 0\n" \
    "global_load_lds_dwordx4 v10, s[40:41]\n" \
    "s_add_u32 m0, s67, 3072\n" \
    "s_nop 0\n" \
    "global_load_lds_dwordx4 v11, s[40:41]\n" \
    "s_add_u32 s40, s40, s66\n" \
    "s_addc_u32 s41, s41, 0\n" \
    "s_add_u32 m0, s67, 16384\n" \
    "s_nop 0\n" \
    "global_load_lds_dwordx4 v8, s[42:43]\n" \
    "s_add_u32 m0, s67, 17408\n" \
    "s_nop 0\n" \
    "global_load_lds_dwordx4 v9, s[42:43]\n" \
    "s_add_u32 m0, s67, 18432\n" \
    "s_nop 0\n" \
    "global_load_lds_dwordx4 v10, s[42:43]\n" \
    "s_add_u32 m0, s67, 19456\n" \
    "s_nop 0\n" \
    "global_load_lds_dwordx4 v11, s[42:43]\n" \
    "s_add_u32 s42, s42, s66\n" \
    "s_addc_u32 s43, s43, 0\n" \
    "s_waitcnt vmcnt(24)\n" \
    "s_barrier\n" \
    "ds_read_b64_tr_b16 a[8:9], v13 offset:32768\n" \
    "ds_read_b64_tr_b16 a[10:11], v13 offset:33792\n" \
    "ds_read_b64_tr_b16 a[12:13], v13 offset:40960\n" \
    "ds_read_b64_tr_b16 a[14:15], v13 offset:41984\n" \
    "ds_read_b64_tr_b16 a[72:73], v14 offset:32768\n" \
    "ds_read_b64_tr_b16 a[74:75], v14 offset:33792\n" \
    "ds_read_b64_tr_b16 a[76:77], v14 offset:40960\n" \
    "ds_read_b64_tr_b16 a[78:79], v14 offset:41984\n" \
    "ds_read_b64_tr_b16 a[136:137], v15 offset:32768\n" \
    "ds_read_b64_tr_b16 a[138:139], v15 offset:33792\n" \
    "ds_read_b64_tr_b16 a[140:141], v15 offset:40960\n" \
    "ds_read_b64_tr_b16 a[142:143], v15 offset:41984\n" \
    "ds_read_b64_tr_b16 a[200:201], v16 offset:32768\n" \
    "ds_read_b64_tr_b16 a[202:203], v16 offset:33792\n" \
    "ds_read_b64_tr_b16 a[204:205], v16 offset:40960\n" \
    "ds_read_b64_tr_b16 a[206:207], v16 offset:41984\n" \
    "s_waitcnt lgkmcnt(0)\n" \
    "s_barrier\n" \
    "s_add_u32 m0, s67, 32768\n" \
    "s_nop 0\n" \
    "global_load_lds_dwordx4 v8, s[40:41]\n" \
    "s_add_u32 m0, s67, 33792\n" \
    "s_nop 0\n" \
    "global_load_lds_dwordx4 v9, s[40:41]\n" \
    "s_add_u32 m0, s67, 34816\n" \
    "s_nop 0\n" \
    "global_load_lds_dwordx4 v10, s[40:41]\n" \
    "s_add_u32 m0, s67, 35840\n" \
    "s_nop 0\n" \
    "global_load_lds_dwordx4 v11, s[40:41]\n" \
    "s_add_u32 s40, s40, s66\n" \
    "s_addc_u32 s41, s41, 0\n" \
    "s_add_u32 m0, s67, 49152\n" \
    "s_nop 0\n" \
    "global_load_lds_dwordx4 v8, s[42:43]\n" \
    "s_add_u32 m0, s67, 50176\n" \
    "s_nop 0\n" \
    "global_load_lds_dwordx4 v9, s[42:43]\n" \
    "s_add_u32 m0, s67, 51200\n" \
    "s_nop 0\n" \
    "global_load_lds_dwordx4 v10, s[42:43]\n" \
    "s_add_u32 m0, s67, 52224\n" \
    "s_nop 0\n" \
    "global_load_lds_dwordx4 v11, s[42:43]\n" \
    "s_add_u32 s42, s42, s66\n" \
    "s_addc_u32 s43, s43, 0\n" \
    "s_waitcnt vmcnt(24)\n" \
    "s_barrier\n" \
    "ds_read_b64_tr_b16 a[16:17], v17 offset:0\n" \
    "ds_read_b64_tr_b16 a[18:19], v17 offset:1024\n" \
    "ds_read_b64_tr_b16 a[20:21], v17 offset:8192\n" \
    "ds_read_b64_tr_b16 a[22:23], v17 offset:9216\n" \
    "ds_read_b64_tr_b16 a[80:81], v18 offset:0\n" \
    "ds_read_b64_tr_b16 a[82:83], v18 offset:1024\n" \
    "ds_read_b64_tr_b16 a[84:85], v18 offset:8192\n" \
    "ds_read_b64_tr_b16 a[86:87], v18 offset:9216\n" \
    "ds_read_b64_tr_b16 a[144:145], v19 offset:0\n" \
    "ds_read_b64_tr_b16 a[146:147], v19 offset:1024\n" \
    "ds_read_b64_tr_b16 a[148:149], v19 offset:8192\n" \
    "ds_read_b64_tr_b16 a[150:151], v19 offset:9216\n" \
    "ds_read_b64_tr_b16 a[208:209], v20 offset:0\n" \
    "ds_read_b64_tr_b16 a[210:211], v20 offset:1024\n" \
    "ds_read_b64_tr_b16 a[212:213], v20 offset:8192\n" \
    "ds_read_b64_tr_b16 a[214:215], v20 offset:9216\n" \
    "s_waitcnt lgkmcnt(0)\n" \
    "s_barrier\n" \
    "s_add_u32 m0, s67, 65536\n" \
    "s_nop 0\n" \
    "global_load_lds_dwordx4 v8, s[40:41]\n" \
    "s_add_u32 m0, s67, 66560\n" \
    "s_nop 0\n" \
    "global_load_lds_dwordx4 v9, s[40:41]\n" \
    "s_add_u32 m0, s67, 67584\n" \
    "s_nop 0\n" \
    "global_load_lds_dwordx4 v10, s[40:41]\n" \
    "s_add_u32 m0, s67, 68608\n" \
    "s_nop 0\n" \
    "global_load_lds_dwordx4 v11, s[40:41]\n" \
    "s_add_u32 s40, s40, s66\n" \
    "s_addc_u32 s41, s41, 0\n" \
    "s_add_u32 m0, s67, 81920\n" \
    "s_nop 0\n" \
    "global_load_lds_dwordx4 v8, s[42:43]\n" \
    "s_add_u32 m0, s67, 82944\n" \
    "s_nop 0\n" \
    "global_load_lds_dwordx4 v9, s[42:43]\n" \
    "s_add_u32 m0, s67, 83968\n" \
    "s_nop 0\n" \
    "global_load_lds_dwordx4 v10, s[42:43]\n" \
    "s_add_u32 m0, s67, 84992\n" \
    "s_nop 0\n" \
    "global_load_lds_dwordx4 v11, s[42:43]\n" \
    "s_add_u32 s42, s42, s66\n" \
    "s_addc_u32 s43, s43, 0\n" \
    "s_waitcnt vmcnt(24)\n" \
    "s_barrier\n" \
    "ds_read_b64_tr_b16 a[24:25], v17 offset:32768\n" \
    "ds_read_b64_tr_b16 a[26:27], v17 offset:33792\n" \
    "ds_read_b64_tr_b16 a[28:29], v17 offset:40960\n" \
    "ds_read_b64_tr_b16 a[30:31], v17 offset:41984\n" \
    "ds_read_b64_tr_b16 a[88:89], v18 offset:32768\n" \
    "ds_read_b64_tr_b16 a[90:91], v18 offset:33792\n" \
    "ds_read_b64_tr_b16 a[92:93], v18 offset:40960\n" \
    "ds_read_b64_tr_b16 a[94:95], v18 offset:41984\n" \
    "ds_read_b64_tr_b16 a[152:153], v19 offset:32768\n" \
    "ds_read_b64_tr_b16 a[154:155], v19 offset:33792\n" \
    "ds_read_b64_tr_b16 a[156:157], v19 offset:40960\n" \
    "ds_read_b64_tr_b16 a[158:159], v19 offset:41984\n" \
    "ds_read_b64_tr_b16 a[216:217], v20 offset:32768\n" \
    "ds_read_b64_tr_b16 a[218:219], v20 offset:33792\n" \
    "ds_read_b64_tr_b16 a[220:221], v20 offset:40960\n" \
    "ds_read_b64_tr_b16 a[222:223], v20 offset:41984\n" \
    "s_waitcnt lgkmcnt(0)\n" \
    "s_barrier\n" \
    "s_add_u32 m0, s67, 98304\n" \
    "s_nop 0\n" \
    "global_load_lds_dwordx4 v8, s[40:41]\n" \
    "s_add_u32 m0, s67, 99328\n" \
    "s_nop 0\n" \
    "global_load_lds_dwordx4 v9, s[40:41]\n" \
    "s_add_u32 m0, s67, 100352\n" \
    "s_nop 0\n" \
    "global_load_lds_dwordx4 v10, s[40:41]\n" \
    "s_add_u32 m0, s67, 101376\n" \
    "s_nop 0\n" \
    "global_load_lds_dwordx4 v11, s[40:41]\n" \
    "s_add_u32 s40, s40, s66\n" \
    "s_addc_u32 s41, s41, 0\n" \
    "s_add_u32 m0, s67, 114688\n" \
    "s_nop 0\n" \
    "global_load_lds_dwordx4 v8, s[42:43]\n" \
    "s_add_u32 m0, s67, 115712\n" \
    "s_nop 0\n" \
    "global_load_lds_dwordx4 v9, s[42:43]\n" \
    "s_add_u32 m0, s67, 116736\n" \
    "s_nop 0\n" \
    "global_load_lds_dwordx4 v10, s[42:43]\n" \
    "s_add_u32 m0, s67, 117760\n" \
    "s_nop 0\n" \
    "global_load_lds_dwordx4 v11, s[42:43]\n" \
    "s_add_u32 s42, s42, s66\n" \
    "s_addc_u32 s43, s43, 0\n" \
    "s_waitcnt vmcnt(24)\n" \
    "s_barrier\n" \
    "ds_read_b64_tr_b16 a[32:33], v13 offset:0\n" \
    "ds_read_b64_tr_b16 a[34:35], v13 offset:1024\n" \
    "ds_read_b64_tr_b16 a[36:37], v13 offset:8192\n" \
    "ds_read_b64_tr_b16 a[38:39], v13 offset:9216\n" \
    "ds_read_b64_tr_b16 a[96:97], v14 offset:0\n" \
    "ds_read_b64_tr_b16 a[98:99], v14 offset:1024\n" \
    "ds_read_b64_tr_b16 a[100:101], v14 offset:8192\n" \
    "ds_read_b64_tr_b16 a[102:103], v14 offset:9216\n" \
    "ds_read_b64_tr_b16 a[160:161], v15 offset:0\n" \
    "ds_read_b64_tr_b16 a[162:163], v15 offset:1024\n" \
    "ds_read_b64_tr_b16 a[164:165], v15 offset:8192\n" \
    "ds_read_b64_tr_b16 a[166:167], v15 offset:9216\n" \
    "ds_read_b64_tr_b16 a[224:225], v16 offset:0\n" \
    "ds_read_b64_tr_b16 a[226:227], v16 offset:1024\n" \
    "ds_read_b64_tr_b16 a[228:229], v16 offset:8192\n" \
    "ds_read_b64_tr_b16 a[230:231], v16 offset:9216\n" \
    "s_waitcnt lgkmcnt(0)\n" \
    "s_barrier\n" \
    "s_waitcnt vmcnt(16)\n" \
    "s_barrier\n" \
    "ds_read_b64_tr_b16 a[40:41], v13 offset:32768\n" \
    "ds_read_b64_tr_b16 a[42:43], v13 offset:33792\n" \
    "ds_read_b64_tr_b16 a[44:45], v13 offset:40960\n" \
    "ds_read_b64_tr_b16 a[46:47], v13 offset:41984\n" \
    "ds_read_b64_tr_b16 a[104:105], v14 offset:32768\n" \
    "ds_read_b64_tr_b16 a[106:107], v14 offset:33792\n" \
    "ds_read_b64_tr_b16 a[108:109], v14 offset:40960\n" \
    "ds_read_b64_tr_b16 a[110:111], v14 offset:41984\n" \
    "ds_read_b64_tr_b16 a[168:169], v15 offset:32768\n" \
    "ds_read_b64_tr_b16 a[170:171], v15 offset:33792\n" \
    "ds_read_b64_tr_b16 a[172:173], v15 offset:40960\n" \
    "ds_read_b64_tr_b16 a[174:175], v15 offset:41984\n" \
    "ds_read_b64_tr_b16 a[232:233], v16 offset:32768\n" \
    "ds_read_b64_tr_b16 a[234:235], v16 offset:33792\n" \
    "ds_read_b64_tr_b16 a[236:237], v16 offset:40960\n" \
    "ds_read_b64_tr_b16 a[238:239], v16 offset:41984\n" \
    "s_waitcnt lgkmcnt(0)\n" \
    "s_barrier\n" \
    "s_waitcnt vmcnt(8)\n" \
    "s_barrier\n" \
    "ds_read_b64_tr_b16 a[48:49], v17 offset:0\n" \
    "ds_read_b64_tr_b16 a[50:51], v17 offset:1024\n" \
    "ds_read_b64_tr_b16 a[52:53], v17 offset:8192\n" \
    "ds_read_b64_tr_b16 a[54:55], v17 offset:9216\n" \
    "ds_read_b64_tr_b16 a[112:113], v18 offset:0\n" \
    "ds_read_b64_tr_b16 a[114:115], v18 offset:1024\n" \
    "ds_read_b64_tr_b16 a[116:117], v18 offset:8192\n" \
    "ds_read_b64_tr_b16 a[118:119], v18 offset:9216\n" \
    "ds_read_b64_tr_b16 a[176:177], v19 offset:0\n" \
    "ds_read_b64_tr_b16 a[178:179], v19 offset:1024\n" \
    "ds_read_b64_tr_b16 a[180:181], v19 offset:8192\n" \
    "ds_read_b64_tr_b16 a[182:183], v19 offset:9216\n" \
    "ds_read_b64_tr_b16 a[240:241], v20 offset:0\n" \
    "ds_read_b64_tr_b16 a[242:243], v20 offset:1024\n" \
    "ds_read_b64_tr_b16 a[244:245], v20 offset:8192\n" \
    "ds_read_b64_tr_b16 a[246:247], v20 offset:9216\n" \
    "s_waitcnt lgkmcnt(0)\n" \
    "s_barrier\n" \
    "s_waitcnt vmcnt(0)\n" \
    "s_barrier\n" \
    "ds_read_b64_tr_b16 a[56:57], v17 offset:32768\n" \
    "ds_read_b64_tr_b16 a[58:59], v17 offset:33792\n" \
    "ds_read_b64_tr_b16 a[60:61], v17 offset:40960\n" \
    "ds_read_b64_tr_b16 a[62:63], v17 offset:41984\n" \
    "ds_read_b64_tr_b16 a[120:121], v18 offset:32768\n" \
    "ds_read_b64_tr_b16 a[122:123], v18 offset:33792\n" \
    "ds_read_b64_tr_b16 a[124:125], v18 offset:40960\n" \
    "ds_read_b64_tr_b16 a[126:127], v18 offset:41984\n" \
    "ds_read_b64_tr_b16 a[184:185], v19 offset:32768\n" \
    "ds_read_b64_tr_b16 a[186:187], v19 offset:33792\n" \
    "ds_read_b64_tr_b16 a[188:189], v19 offset:40960\n" \
    "ds_read_b64_tr_b16 a[190:191], v19 offset:41984\n" \
    "ds_read_b64_tr_b16 a[248:249], v20 offset:32768\n" \
    "ds_read_b64_tr_b16 a[250:251], v20 offset:33792\n" \
    "ds_read_b64_tr_b16 a[252:253], v20 offset:40960\n" \
    "ds_read_b64_tr_b16 a[254:255], v20 offset:41984\n" \
    "s_waitcnt lgkmcnt(0)\n" \
    "s_barrier\n" \
    "; ---- first task: all four chunks\n" \
    "s_mov_b32 s57, s55\n" \
    "s_lshl_b32 s57, s57, 17\n" \
    "s_lshl_b32 s58, s53, 15\n" \
    "s_add_u32 s57, s57, s58\n" \
    "s_add_u32 s60, s44, s57\n" \
    "s_addc_u32 s61, s45, 0\n" \
    "s_add_u32 s62, s60, 16384\n" \
    "s_addc_u32 s63, s61, 0\n" \
    "s_add_u32 m0, s68, 0\n" \
    "s_nop 0\n" \
    "global_load_lds_dwordx4 v128, s[60:61]\n" \
    "s_add_u32 m0, s68, 1024\n" \
    "s_nop 0\n" \
    "global_load_lds_dwordx4 v129, s[60:61]\n" \
    "s_add_u32 m0, s68, 2048\n" \
    "s_nop 0\n" \
    "global_load_lds_dwordx4 v130, s[60:61]\n" \
    "s_add_u32 m0, s68, 3072\n" \
    "s_nop 0\n" \
    "global_load_lds_dwordx4 v131, s[60:61]\n" \
    "s_add_u32 m0, s68, 4096\n" \
    "s_nop 0\n" \
    "global_load_lds_dwordx4 v128, s[62:63]\n" \
    "s_add_u32 m0, s68, 5120\n" \
    "s_nop 0\n" \
    "global_load_lds_dwordx4 v129, s[62:63]\n" \
    "s_add_u32 m0, s68, 6144\n" \
    "s_nop 0\n" \
    "global_load_lds_dwordx4 v130, s[62:63]\n" \
    "s_add_u32 m0, s68, 7168\n" \
    "s_nop 0\n" \
    "global_load_lds_dwordx4 v131, s[62:63]\n" \
    "s_add_u32 s60, s60, 256\n" \
    "s_addc_u32 s61, s61, 0\n" \
    "s_add_u32 s62, s62, 256\n" \
    "s_addc_u32 s63, s63, 0\n" \
    "s_add_u32 m0, s68, 32768\n" \
    "s_nop 0\n" \
    "global_load_lds_dwordx4 v128, s[60:61]\n" \
    "s_add_u32 m0, s68, 33792\n" \
    "s_nop 0\n" \
    "global_load_lds_dwordx4 v129, s[60:61]\n" \
    "s_add_u32 m0, s68, 34816\n" \
    "s_nop 0\n" \
    "global_load_lds_dwordx4 v130, s[60:61]\n" \
    "s_add_u32 m0, s68, 35840\n" \
    "s_nop 0\n" \
    "global_load_lds_dwordx4 v131, s[60:61]\n" \
    "s_add_u32 m0, s68, 36864\n" \
    "s_nop 0\n" \
    "global_load_lds_dwordx4 v128, s[62:63]\n" \
    "s_add_u32 m0, s68, 37888\n" \
    "s_nop 0\n" \
    "global_load_lds_dwordx4 v129, s[62:63]\n" \
    "s_add_u32 m0, s68, 38912\n" \
    "s_nop 0\n" \
    "global_load_lds_dwordx4 v130, s[62:63]\n" \
    "s_add_u32 m0, s68, 39936\n" \
    "s_nop 0\n" \
    "global_load_lds_dwordx4 v131, s[62:63]\n" \
    "s_add_u32 s60, s60, 256\n" \
    "s_addc_u32 s61, s61, 0\n" \
    "s_add_u32 s62, s62, 256\n" \
    "s_addc_u32 s63, s63, 0\n" \
    "s_add_u32 m0, s68, 65536\n" \
    "s_nop 0\n" \
    "global_load_lds_dwordx4 v128, s[60:61]\n" \
    "s_add_u32 m0, s68, 66560\n" \
    "s_nop 0\n" \
    "global_load_lds_dwordx4 v129, s[60:61]\n" \
    "s_add_u32 m0, s68, 67584\n" \
    "s_nop 0\n" \
    "global_load_lds_dwordx4 v130, s[60:61]\n" \
    "s_add_u32 m0, s68, 68608\n" \
    "s_nop 0\n" \
    "global_load_lds_dwordx4 v131, s[60:61]\n" \
    "s_add_u32 m0, s68, 69632\n" \
    "s_nop 0\n" \
    "global_load_lds_dwordx4 v128, s[62:63]\n" \
    "s_add_u32 m0, s68, 70656\n" \
    "s_nop 0\n" \
    "global_load_lds_dwordx4 v129, s[62:63]\n" \
    "s_add_u32 m0, s68, 71680\n" \
    "s_nop 0\n" \
    "global_load_lds_dwordx4 v130, s[62:63]\n" \
    "s_add_u32 m0, s68, 72704\n" \
    "s_nop 0\n" \
    "global_load_lds_dwordx4 v131, s[62:63]\n" \
    "s_add_u32 s60, s60, 256\n" \
    "s_addc_u32 s61, s61, 0\n" \
    "s_add_u32 s62, s62, 256\n" \
    "s_addc_u32 s63, s63, 0\n" \
    "s_add_u32 m0, s68, 98304\n" \
    "s_nop 0\n" \
    "global_load_lds_dwordx4 v128, s[60:61]\n" \
    "s_add_u32 m0, s68, 99328\n" \
    "s_nop 0\n" \
    "global_load_lds_dwordx4 v129, s[60:61]\n" \
    "s_add_u32 m0, s68, 100352\n" \
    "s_nop 0\n" \
    "global_load_lds_dwordx4 v130, s[60:61]\n" \
    "s_add_u32 m0, s68, 101376\n" \
    "s_nop 0\n" \
    "global_load_lds_dwordx4 v131, s[60:61]\n" \
    "s_add_u32 m0, s68, 102400\n" \
    "s_nop 0\n" \
    "global_load_lds_dwordx4 v128, s[62:63]\n" \
    "s_add_u32 m0, s68, 103424\n" \
    "s_nop 0\n" \
    "global_load_lds_dwordx4 v129, s[62:63]\n" \
    "s_add_u32 m0, s68, 104448\n" \
    "s_nop 0\n" \
    "global_load_lds_dwordx4 v130, s[62:63]\n" \
    "s_add_u32 m0, s68, 105472\n" \
    "s_nop 0\n" \
    "global_load_lds_dwordx4 v131, s[62:63]\n" \
    "s_add_u32 s60, s60, 256\n" \
    "s_addc_u32 s61, s61, 0\n" \
    "s_add_u32 s62, s62, 256\n" \
    "s_addc_u32 s63, s63, 0\n" \
    "s_add_u32 s57, s55, 2\n" \
    "s_min_u32 s57, s57, 15\n" \
    "s_lshl_b32 s57, s57, 17\n" \
    "s_lshl_b32 s58, s53, 15\n" \
    "s_add_u32 s57, s57, s58\n" \
    "s_add_u32 s60, s44, s57\n" \
    "s_addc_u32 s61, s45, 0\n" \
    "s_add_u32 s62, s60, 16384\n" \
    "s_addc_u32 s63, s61, 0\n" \
    "s_waitcnt vmcnt(0)\n" \
    "s_barrier\n" \
    "ds_read_b128 v[160:163], v132 offset:0\n" \
    "ds_read_b128 v[164:167], v132 offset:4096\n" \
    "ds_read_b128 v[168:171], v132 offset:8192\n" \
    "ds_read_b128 v[172:175], v132 offset:12288\n" \
    "ds_read_b128 v[176:179], v132 offset:16384\n" \
    "ds_read_b128 v[180:183], v132 offset:20480\n" \
    "ds_read_b128 v[184:187], v132 offset:24576\n" \
    "ds_read_b128 v[188:191], v132 offset:28672\n" \
    "TASK_%=:\n" \
    "s_cmp_eq_u32 s52, 0\n" \
    "s_cbranch_scc0 ML0_%=\n" \
    "s_cmp_ge_u32 s53, 2\n" \
    "s_cbranch_scc1 ML1_%=\n" \
    "ML0_%=:\n" \
    "s_waitcnt lgkmcnt(7)\n" \
    "v_mfma_f32_16x16x32_bf16 v[0:3], a[0:3], v[160:163], 0\n" \
    "ds_read_b128 v[192:195], v133 offset:0\n" \
    "v_mfma_f32_16x16x32_bf16 v[32:35], a[64:67], v[160:163], 0\n" \
    "v_mfma_f32_16x16x32_bf16 v[64:67], a[128:131], v[160:163], 0\n" \
    "v_mfma_f32_16x16x32_bf16 v[96:99], a[192:195], v[160:163], 0\n" \
    "s_waitcnt lgkmcnt(7)\n" \
    "v_mfma_f32_16x16x32_bf16 v[4:7], a[0:3], v[164:167], 0\n" \
    "ds_read_b128 v[196:199], v133 offset:4096\n" \
    "v_mfma_f32_16x16x32_bf16 v[36:39], a[64:67], v[164:167], 0\n" \
    "v_mfma_f32_16x16x32_bf16 v[68:71], a[128:131], v[164:167], 0\n" \
    "v_mfma_f32_16x16x32_bf16 v[100:103], a[192:195], v[164:167], 0\n" \
    "s_waitcnt lgkmcnt(7)\n" \
    "v_mfma_f32_16x16x32_bf16 v[8:11], a[0:3], v[168:171], 0\n" \
    "ds_read_b128 v[200:203], v133 offset:8192\n" \
    "v_mfma_f32_16x16x32_bf16 v[40:43], a[64:67], v[168:171], 0\n" \
    "v_mfma_f32_16x16x32_bf16 v[72:75], a[128:131], v[168:171], 0\n" \
    "v_mfma_f32_16x16x32_bf16 v[104:107], a[192:195], v[168:171], 0\n" \
    "s_waitcnt lgkmcnt(7)\n" \
    "v_mfma_f32_16x16x32_bf16 v[12:15], a[0:3], v[172:175], 0\n" \
    "ds_read_b128 v[204:207], v133 offset:12288\n" \
    "v_mfma_f32_16x16x32_bf16 v[44:47], a[64:67], v[172:175], 0\n" \
    "v_mfma_f32_16x16x32_bf16 v[76:79], a[128:131], v[172:175], 0\n" \
    "v_mfma_f32_16x16x32_bf16 v[108:111], a[192:195], v[172:175], 0\n" \
    "s_waitcnt lgkmcnt(7)\n" \
    "v_mfma_f32_16x16x32_bf16 v[16:19], a[0:3], v[176:179], 0\n" \
    "ds_read_b128 v[208:211], v133 offset:16384\n" \
    "v_mfma_f32_16x16x32_bf16 v[48:51], a[64:67], v[176:179], 0\n" \
    "v_mfma_f32_16x16x32_bf16 v[80:83], a[128:131], v[176:179], 0\n" \
    "v_mfma_f32_16x16x32_bf16 v[112:115], a[192:195], v[176:179], 0\n" \
    "s_waitcnt lgkmcnt(7)\n" \
    "v_mfma_f32_16x16x32_bf16 v[20:23], a[0:3], v[180:183], 0\n" \
    "ds_read_b128 v[212:215], v133 offset:20480\n" \
    "v_mfma_f32_16x16x32_bf16 v[52:55], a[64:67], v[180:183], 0\n" \
    "v_mfma_f32_16x16x32_bf16 v[84:87], a[128:131], v[180:183], 0\n" \
    "v_mfma_f32_16x16x32_bf16 v[116:119], a[192:195], v[180:183], 0\n" \
    "s_waitcnt lgkmcnt(7)\n" \
    "v_mfma_f32_16x16x32_bf16 v[24:27], a[0:3], v[184:187], 0\n" \
    "ds_read_b128 v[216:219], v133 offset:24576\n" \
    "v_mfma_f32_16x16x32_bf16 v[56:59], a[64:67], v[184:187], 0\n" \
    "v_mfma_f32_16x16x32_bf16 v[88:91], a[128:131], v[184:187], 0\n" \
    "v_mfma_f32_16x16x32_bf16 v[120:123], a[192:195], v[184:187], 0\n" \
    "s_waitcnt lgkmcnt(7)\n" \
    "v_mfma_f32_16x16x32_bf16 v[28:31], a[0:3], v[188:191], 0\n" \
    "ds_read_b128 v[220:223], v133 offset:28672\n" \
    "v_mfma_f32_16x16x32_bf16 v[60:63], a[64:67], v[188:191], 0\n" \
    "v_mfma_f32_16x16x32_bf16 v[92:95], a[128:131], v[188:191], 0\n" \
    "v_mfma_f32_16x16x32_bf16 v[124:127], a[192:195], v[188:191], 0\n" \
    "s_waitcnt lgkmcnt(7)\n" \
    "v_mfma_f32_16x16x32_bf16 v[0:3], a[4:7], v[192:195], v[0:3]\n" \
    "ds_read_b128 v[160:163], v134 offset:0\n" \
    "v_mfma_f32_16x16x32_bf16 v[32:35], a[68:71], v[192:195], v[32:35]\n" \
    "v_mfma_f32_16x16x32_bf16 v[64:67], a[132:135], v[192:195], v[64:67]\n" \
    "v_mfma_f32_16x16x32_bf16 v[96:99], a[196:199], v[192:195], v[96:99]\n" \
    "s_waitcnt lgkmcnt(7)\n" \
    "v_mfma_f32_16x16x32_bf16 v[4:7], a[4:7], v[196:199], v[4:7]\n" \
    "ds_read_b128 v[164:167], v134 offset:4096\n" \
    "v_mfma_f32_16x16x32_bf16 v[36:39], a[68:71], v[196:199], v[36:39]\n" \
    "v_mfma_f32_16x16x32_bf16 v[68:71], a[132:135], v[196:199], v[68:71]\n" \
    "v_mfma_f32_16x16x32_bf16 v[100:103], a[196:199], v[196:199], v[100:103]\n" \
    "s_waitcnt lgkmcnt(7)\n" \
    "v_mfma_f32_16x16x32_bf16 v[8:11], a[4:7], v[200:203], v[8:11]\n" \
    "ds_read_b128 v[168:171], v134 offset:8192\n" \
    "v_mfma_f32_16x16x32_bf16 v[40:43], a[68:71], v[200:203], v[40:43]\n" \
    "v_mfma_f32_16x16x32_bf16 v[72:75], a[132:135], v[200:203], v[72:75]\n" \
    "v_mfma_f32_16x16x32_bf16 v[104:107], a[196:199], v[200:203], v[104:107]\n" \
    "s_waitcnt lgkmcnt(7)\n" \
    "v_mfma_f32_16x16x32_bf16 v[12:15], a[4:7], v[204:207], v[12:15]\n" \
    "ds_read_b128 v[172:175], v134 offset:12288\n" \
    "v_mfma_f32_16x16x32_bf16 v[44:47], a[68:71], v[204:207], v[44:47]\n" \
    "v_mfma_f32_16x16x32_bf16 v[76:79], a[132:135], v[204:207], v[76:79]\n" \
    "v_mfma_f32_16x16x32_bf16 v[108:111], a[196:199], v[204:207], v[108:111]\n" \
    "s_waitcnt lgkmcnt(7)\n" \
    "v_mfma_f32_16x16x32_bf16 v[16:19], a[4:7], v[208:211], v[16:19]\n" \
    "ds_read_b128 v[176:179], v134 offset:16384\n" \
    "v_mfma_f32_16x16x32_bf16 v[48:51], a[68:71], v[208:211], v[48:51]\n" \
    "v_mfma_f32_16x16x32_bf16 v[80:83], a[132:135], v[208:211], v[80:83]\n" \
    "v_mfma_f32_16x16x32_bf16 v[112:115], a[196:199], v[208:211], v[112:115]\n" \
    "s_waitcnt lgkmcnt(7)\n" \
    "v_mfma_f32_16x16x32_bf16 v[20:23], a[4:7], v[212:215], v[20:23]\n" \
    "ds_read_b128 v[180:183], v134 offset:20480\n" \
    "v_mfma_f32_16x16x32_bf16 v[52:55], a[68:71], v[212:215], v[52:55]\n" \
    "v_mfma_f32_16x16x32_bf16 v[84:87], a[132:135], v[212:215], v[84:87]\n" \
    "v_mfma_f32_16x16x32_bf16 v[116:119], a[196:199], v[212:215], v[116:119]\n" \
    "s_waitcnt lgkmcnt(7)\n" \
    "v_mfma_f32_16x16x32_bf16 v[24:27], a[4:7], v[216:219], v[24:27]\n" \
    "ds_read_b128 v[184:187], v134 offset:24576\n" \
    "v_mfma_f32_16x16x32_bf16 v[56:59], a[68:71], v[216:219], v[56:59]\n" \
    "v_mfma_f32_16x16x32_bf16 v[88:91], a[132:135], v[216:219], v[88:91]\n" \
    "v_mfma_f32_16x16x32_bf16 v[120:123], a[196:199], v[216:219], v[120:123]\n" \
    "s_waitcnt lgkmcnt(7)\n" \
    "v_mfma_f32_16x16x32_bf16 v[28:31], a[4:7], v[220:223], v[28:31]\n" \
    "ds_read_b128 v[188:191], v134 offset:28672\n" \
    "v_mfma_f32_16x16x32_bf16 v[60:63], a[68:71], v[220:223], v[60:63]\n" \
    "v_mfma_f32_16x16x32_bf16 v[92:95], a[132:135], v[220:223], v[92:95]\n" \
    "v_mfma_f32_16x16x32_bf16 v[124:127], a[196:199], v[220:223], v[124:127]\n" \
    "s_waitcnt lgkmcnt(7)\n" \
    "v_mfma_f32_16x16x32_bf16 v[0:3], a[8:11], v[160:163], v[0:3]\n" \
    "ds_read_b128 v[192:195], v135 offset:0\n" \
    "v_mfma_f32_16x16x32_bf16 v[32:35], a[72:75], v[160:163], v[32:35]\n" \
    "v_mfma_f32_16x16x32_bf16 v[64:67], a[136:139], v[160:163], v[64:67]\n" \
    "v_mfma_f32_16x16x32_bf16 v[96:99], a[200:203], v[160:163], v[96:99]\n" \
    "s_waitcnt lgkmcnt(7)\n" \
    "v_mfma_f32_16x16x32_bf16 v[4:7], a[8:11], v[164:167], v[4:7]\n" \
    "ds_read_b128 v[196:199], v135 offset:4096\n" \
    "v_mfma_f32_16x16x32_bf16 v[36:39], a[72:75], v[164:167], v[36:39]\n" \
    "v_mfma_f32_16x16x32_bf16 v[68:71], a[136:139], v[164:167], v[68:71]\n" \
    "v_mfma_f32_16x16x32_bf16 v[100:103], a[200:203], v[164:167], v[100:103]\n" \
    "s_waitcnt lgkmcnt(7)\n" \
    "v_mfma_f32_16x16x32_bf16 v[8:11], a[8:11], v[168:171], v[8:11]\n" \
    "ds_read_b128 v[200:203], v135 offset:8192\n" \
    "v_mfma_f32_16x16x32_bf16 v[40:43], a[72:75], v[168:171], v[40:43]\n" \
    "v_mfma_f32_16x16x32_bf16 v[72:75], a[136:139], v[168:171], v[72:75]\n" \
    "v_mfma_f32_16x16x32_bf16 v[104:107], a[200:203], v[168:171], v[104:107]\n" \
    "s_waitcnt lgkmcnt(7)\n" \
    "v_mfma_f32_16x16x32_bf16 v[12:15], a[8:11], v[172:175], v[12:15]\n" \
    "ds_read_b128 v[204:207], v135 offset:12288\n" \
    "v_mfma_f32_16x16x32_bf16 v[44:47], a[72:75], v[172:175], v[44:47]\n" \
    "v_mfma_f32_16x16x32_bf16 v[76:79], a[136:139], v[172:175], v[76:79]\n" \
    "v_mfma_f32_16x16x32_bf16 v[108:111], a[200:203], v[172:175], v[108:111]\n" \
    "s_waitcnt lgkmcnt(7)\n" \
    "v_mfma_f32_16x16x32_bf16 v[16:19], a[8:11], v[176:179], v[16:19]\n" \
    "ds_read_b128 v[208:211], v135 offset:16384\n" \
    "v_mfma_f32_16x16x32_bf16 v[48:51], a[72:75], v[176:179], v[48:51]\n" \
    "v_mfma_f32_16x16x32_bf16 v[80:83], a[136:139], v[176:179], v[80:83]\n" \
    "v_mfma_f32_16x16x32_bf16 v[112:115], a[200:203], v[176:179], v[112:115]\n" \
    "s_waitcnt lgkmcnt(7)\n" \
    "v_mfma_f32_16x16x32_bf16 v[20:23], a[8:11], v[180:183], v[20:23]\n" \
    "ds_read_b128 v[212:215], v135 offset:20480\n" \
    "v_mfma_f32_16x16x32_bf16 v[52:55], a[72:75], v[180:183], v[52:55]\n" \
    "v_mfma_f32_16x16x32_bf16 v[84:87], a[136:139], v[180:183], v[84:87]\n" \
    "v_mfma_f32_16x16x32_bf16 v[116:119], a[200:203], v[180:183], v[116:119]\n" \
    "s_waitcnt lgkmcnt(7)\n" \
    "v_mfma_f32_16x16x32_bf16 v[24:27], a[8:11], v[184:187], v[24:27]\n" \
    "ds_read_b128 v[216:219], v135 offset:24576\n" \
    "v_mfma_f32_16x16x32_bf16 v[56:59], a[72:75], v[184:187], v[56:59]\n" \
    "v_mfma_f32_16x16x32_bf16 v[88:91], a[136:139], v[184:187], v[88:91]\n" \
    "v_mfma_f32_16x16x32_bf16 v[120:123], a[200:203], v[184:187], v[120:123]\n" \
    "s_waitcnt lgkmcnt(7)\n" \
    "v_mfma_f32_16x16x32_bf16 v[28:31], a[8:11], v[188:191], v[28:31]\n" \
    "ds_read_b128 v[220:223], v135 offset:28672\n" \
    "v_mfma_f32_16x16x32_bf16 v[60:63], a[72:75], v[188:191], v[60:63]\n" \
    "v_mfma_f32_16x16x32_bf16 v[92:95], a[136:139], v[188:191], v[92:95]\n" \
    "v_mfma_f32_16x16x32_bf16 v[124:127], a[200:203], v[188:191], v[124:127]\n" \
    "s_waitcnt vmcnt(32) lgkmcnt(0)\n" \
    "s_barrier\n" \
    "v_mfma_f32_16x16x32_bf16 v[0:3], a[12:15], v[192:195], v[0:3]\n" \
    "ds_read_b128 v[160:163], v132 offset:32768\n" \
    "v_mfma_f32_16x16x32_bf16 v[32:35], a[76:79], v[192:195], v[32:35]\n" \
    "s_add_u32 m0, s68, 0\n" \
    "v_mfma_f32_16x16x32_bf16 v[64:67], a[140:143], v[192:195], v[64:67]\n" \
    "global_load_lds_dwordx4 v128, s[60:61]\n" \
    "v_mfma_f32_16x16x32_bf16 v[96:99], a[204:207], v[192:195], v[96:99]\n" \
    "s_add_u32 m0, s68, 1024\n" \
    "v_mfma_f32_16x16x32_bf16 v[4:7], a[12:15], v[196:199], v[4:7]\n" \
    "ds_read_b128 v[164:167], v132 offset:36864\n" \
    "v_mfma_f32_16x16x32_bf16 v[36:39], a[76:79], v[196:199], v[36:39]\n" \
    "global_load_lds_dwordx4 v129, s[60:61]\n" \
    "v_mfma_f32_16x16x32_bf16 v[68:71], a[140:143], v[196:199], v[68:71]\n" \
    "s_add_u32 m0, s68, 2048\n" \
    "v_mfma_f32_16x16x32_bf16 v[100:103], a[204:207], v[196:199], v[100:103]\n" \
    "global_load_lds_dwordx4 v130, s[60:61]\n" \
    "v_mfma_f32_16x16x32_bf16 v[8:11], a[12:15], v[200:203], v[8:11]\n" \
    "ds_read_b128 v[168:171], v132 offset:40960\n" \
    "v_mfma_f32_16x16x32_bf16 v[40:43], a[76:79], v[200:203], v[40:43]\n" \
    "s_add_u32 m0, s68, 3072\n" \
    "v_mfma_f32_16x16x32_bf16 v[72:75], a[140:143], v[200:203], v[72:75]\n" \
    "global_load_lds_dwordx4 v131, s[60:61]\n" \
    "v_mfma_f32_16x16x32_bf16 v[104:107], a[204:207], v[200:203], v[104:107]\n" \
    "s_add_u32 m0, s68, 4096\n" \
    "v_mfma_f32_16x16x32_bf16 v[12:15], a[12:15], v[204:207], v[12:15]\n" \
    "ds_read_b128 v[172:175], v132 offset:45056\n" \
    "v_mfma_f32_16x16x32_bf16 v[44:47], a[76:79], v[204:207], v[44:47]\n" \
    "global_load_lds_dwordx4 v128, s[62:63]\n" \
    "v_mfma_f32_16x16x32_bf16 v[76:79], a[140:143], v[204:207], v[76:79]\n" \
    "s_add_u32 m0, s68, 5120\n" \
    "v_mfma_f32_16x16x32_bf16 v[108:111], a[204:207], v[204:207], v[108:111]\n" \
    "global_load_lds_dwordx4 v129, s[62:63]\n" \
    "v_mfma_f32_16x16x32_bf16 v[16:19], a[12:15], v[208:211], v[16:19]\n" \
    "ds_read_b128 v[176:179], v132 offset:49152\n" \
    "v_mfma_f32_16x16x32_bf16 v[48:51], a[76:79], v[208:211], v[48:51]\n" \
    "s_add_u32 m0, s68, 6144\n" \
    "v_mfma_f32_16x16x32_bf16 v[80:83], a[140:143], v[208:211], v[80:83]\n" \
    "global_load_lds_dwordx4 v130, s[62:63]\n" \
    "v_mfma_f32_16x16x32_bf16 v[112:115], a[204:207], v[208:211], v[112:115]\n" \
    "s_add_u32 m0, s68, 7168\n" \
    "v_mfma_f32_16x16x32_bf16 v[20:23], a[12:15], v[212:215], v[20:23]\n" \
    "ds_read_b128 v[180:183], v132 offset:53248\n" \
    "v_mfma_f32_16x16x32_bf16 v[52:55], a[76:79], v[212:215], v[52:55]\n" \
    "global_load_lds_dwordx4 v131, s[62:63]\n" \
    "v_mfma_f32_16x16x32_bf16 v[84:87], a[140:143], v[212:215], v[84:87]\n" \
    "s_add_u32 s60, s60, 256\n" \
    "v_mfma_f32_16x16x32_bf16 v[116:119], a[204:207], v[212:215], v[116:119]\n" \
    "s_addc_u32 s61, s61, 0\n" \
    "v_mfma_f32_16x16x32_bf16 v[24:27], a[12:15], v[216:219], v[24:27]\n" \
    "ds_read_b128 v[184:187], v132 offset:57344\n" \
    "v_mfma_f32_16x16x32_bf16 v[56:59], a[76:79], v[216:219], v[56:59]\n" \
    "s_add_u32 s62, s62, 256\n" \
    "v_mfma_f32_16x16x32_bf16 v[88:91], a[140:143], v[216:219], v[88:91]\n" \
    "s_addc_u32 s63, s63, 0\n" \
    "v_mfma_f32_16x16x32_bf16 v[120:123], a[204:207], v[216:219], v[120:123]\n" \
    "v_mfma_f32_16x16x32_bf16 v[28:31], a[12:15], v[220:223], v[28:31]\n" \
    "ds_read_b128 v[188:191], v132 offset:61440\n" \
    "v_mfma_f32_16x16x32_bf16 v[60:63], a[76:79], v[220:223], v[60:63]\n" \
    "v_mfma_f32_16x16x32_bf16 v[92:95], a[140:143], v[220:223], v[92:95]\n" \
    "v_mfma_f32_16x16x32_bf16 v[124:127], a[204:207], v[220:223], v[124:127]\n" \
    "s_waitcnt lgkmcnt(7)\n" \
    "v_mfma_f32_16x16x32_bf16 v[0:3], a[16:19], v[160:163], v[0:3]\n" \
    "ds_read_b128 v[192:195], v133 offset:32768\n" \
    "v_mfma_f32_16x16x32_bf16 v[32:35], a[80:83], v[160:163], v[32:35]\n" \
    "v_mfma_f32_16x16x32_bf16 v[64:67], a[144:147], v[160:163], v[64:67]\n" \
    "v_mfma_f32_16x16x32_bf16 v[96:99], a[208:211], v[160:163], v[96:99]\n" \
    "s_waitcnt lgkmcnt(7)\n" \
    "v_mfma_f32_16x16x32_bf16 v[4:7], a[16:19], v[164:167], v[4:7]\n" \
    "ds_read_b128 v[196:199], v133 offset:36864\n" \
    "v_mfma_f32_16x16x32_bf16 v[36:39], a[80:83], v[164:167], v[36:39]\n" \
    "v_mfma_f32_16x16x32_bf16 v[68:71], a[144:147], v[164:167], v[68:71]\n" \
    "v_mfma_f32_16x16x32_bf16 v[100:103], a[208:211], v[164:167], v[100:103]\n" \
    "s_waitcnt lgkmcnt(7)\n" \
    "v_mfma_f32_16x16x32_bf16 v[8:11], a[16:19], v[168:171], v[8:11]\n" \
    "ds_read_b128 v[200:203], v133 offset:40960\n" \
    "v_mfma_f32_16x16x32_bf16 v[40:43], a[80:83], v[168:171], v[40:43]\n" \
    "v_mfma_f32_16x16x32_bf16 v[72:75], a[144:147], v[168:171], v[72:75]\n" \
    "v_mfma_f32_16x16x32_bf16 v[104:107], a[208:211], v[168:171], v[104:107]\n" \
    "s_waitcnt lgkmcnt(7)\n" \
    "v_mfma_f32_16x16x32_bf16 v[12:15], a[16:19], v[172:175], v[12:15]\n" \
    "ds_read_b128 v[204:207], v133 offset:45056\n" \
    "v_mfma_f32_16x16x32_bf16 v[44:47], a[80:83], v[172:175], v[44:47]\n" \
    "v_mfma_f32_16x16x32_bf16 v[76:79], a[144:147], v[172:175], v[76:79]\n" \
    "v_mfma_f32_16x16x32_bf16 v[108:111], a[208:211], v[172:175], v[108:111]\n" \
    "s_waitcnt lgkmcnt(7)\n" \
    "v_mfma_f32_16x16x32_bf16 v[16:19], a[16:19], v[176:179], v[16:19]\n" \
    "ds_read_b128 v[208:211], v133 offset:49152\n" \
    "v_mfma_f32_16x16x32_bf16 v[48:51], a[80:83], v[176:179], v[48:51]\n" \
    "v_mfma_f32_16x16x32_bf16 v[80:83], a[144:147], v[176:179], v[80:83]\n" \
    "v_mfma_f32_16x16x32_bf16 v[112:115], a[208:211], v[176:179], v[112:115]\n" \
    "s_waitcnt lgkmcnt(7)\n" \
    "v_mfma_f32_16x16x32_bf16 v[20:23], a[16:19], v[180:183], v[20:23]\n" \
    "ds_read_b128 v[212:215], v133 offset:53248\n" \
    "v_mfma_f32_16x16x32_bf16 v[52:55], a[80:83], v[180:183], v[52:55]\n" \
    "v_mfma_f32_16x16x32_bf16 v[84:87], a[144:147], v[180:183], v[84:87]\n" \
    "v_mfma_f32_16x16x32_bf16 v[116:119], a[208:211], v[180:183], v[116:119]\n" \
    "s_waitcnt lgkmcnt(7)\n" \
    "v_mfma_f32_16x16x32_bf16 v[24:27], a[16:19], v[184:187], v[24:27]\n" \
    "ds_read_b128 v[216:219], v133 offset:57344\n" \
    "v_mfma_f32_16x16x32_bf16 v[56:59], a[80:83], v[184:187], v[56:59]\n" \
    "v_mfma_f32_16x16x32_bf16 v[88:91], a[144:147], v[184:187], v[88:91]\n" \
    "v_mfma_f32_16x16x32_bf16 v[120:123], a[208:211], v[184:187], v[120:123]\n" \
    "s_waitcnt lgkmcnt(7)\n" \
    "v_mfma_f32_16x16x32_bf16 v[28:31], a[16:19], v[188:191], v[28:31]\n" \
    "ds_read_b128 v[220:223], v133 offset:61440\n" \
    "v_mfma_f32_16x16x32_bf16 v[60:63], a[80:83], v[188:191], v[60:63]\n" \
    "v_mfma_f32_16x16x32_bf16 v[92:95], a[144:147], v[188:191], v[92:95]\n" \
    "v_mfma_f32_16x16x32_bf16 v[124:127], a[208:211], v[188:191], v[124:127]\n" \
    "s_waitcnt lgkmcnt(7)\n" \
    "v_mfma_f32_16x16x32_bf16 v[0:3], a[20:23], v[192:195], v[0:3]\n" \
    "ds_read_b128 v[160:163], v134 offset:32768\n" \
    "v_mfma_f32_16x16x32_bf16 v[32:35], a[84:87], v[192:195], v[32:35]\n" \
    "v_mfma_f32_16x16x32_bf16 v[64:67], a[148:151], v[192:195], v[64:67]\n" \
    "v_mfma_f32_16x16x32_bf16 v[96:99], a[212:215], v[192:195], v[96:99]\n" \
    "s_waitcnt lgkmcnt(7)\n" \
    "v_mfma_f32_16x16x32_bf16 v[4:7], a[20:23], v[196:199], v[4:7]\n" \
    "ds_read_b128 v[164:167], v134 offset:36864\n" \
    "v_mfma_f32_16x16x32_bf16 v[36:39], a[84:87], v[196:199], v[36:39]\n" \
    "v_mfma_f32_16x16x32_bf16 v[68:71], a[148:151], v[196:199], v[68:71]\n" \
    "v_mfma_f32_16x16x32_bf16 v[100:103], a[212:215], v[196:199], v[100:103]\n" \
    "s_waitcnt lgkmcnt(7)\n" \
    "v_mfma_f32_16x16x32_bf16 v[8:11], a[20:23], v[200:203], v[8:11]\n" \
    "ds_read_b128 v[168:171], v134 offset:40960\n" \
    "v_mfma_f32_16x16x32_bf16 v[40:43], a[84:87], v[200:203], v[40:43]\n" \
    "v_mfma_f32_16x16x32_bf16 v[72:75], a[148:151], v[200:203], v[72:75]\n" \
    "v_mfma_f32_16x16x32_bf16 v[104:107], a[212:215], v[200:203], v[104:107]\n" \
    "s_waitcnt lgkmcnt(7)\n" \
    "v_mfma_f32_16x16x32_bf16 v[12:15], a[20:23], v[204:207], v[12:15]\n" \
    "ds_read_b128 v[172:175], v134 offset:45056\n" \
    "v_mfma_f32_16x16x32_bf16 v[44:47], a[84:87], v[204:207], v[44:47]\n" \
    "v_mfma_f32_16x16x32_bf16 v[76:79], a[148:151], v[204:207], v[76:79]\n" \
    "v_mfma_f32_16x16x32_bf16 v[108:111], a[212:215], v[204:207], v[108:111]\n" \
    "s_waitcnt lgkmcnt(7)\n" \
    "v_mfma_f32_16x16x32_bf16 v[16:19], a[20:23], v[208:211], v[16:19]\n" \
    "ds_read_b128 v[176:179], v134 offset:49152\n" \
    "v_mfma_f32_16x16x32_bf16 v[48:51], a[84:87], v[208:211], v[48:51]\n" \
    "v_mfma_f32_16x16x32_bf16 v[80:83], a[148:151], v[208:211], v[80:83]\n" \
    "v_mfma_f32_16x16x32_bf16 v[112:115], a[212:215], v[208:211], v[112:115]\n" \
    "s_waitcnt lgkmcnt(7)\n" \
    "v_mfma_f32_16x16x32_bf16 v[20:23], a[20:23], v[212:215], v[20:23]\n" \
    "ds_read_b128 v[180:183], v134 offset:53248\n" \
    "v_mfma_f32_16x16x32_bf16 v[52:55], a[84:87], v[212:215], v[52:55]\n" \
    "v_mfma_f32_16x16x32_bf16 v[84:87], a[148:151], v[212:215], v[84:87]\n" \
    "v_mfma_f32_16x16x32_bf16 v[116:119], a[212:215], v[212:215], v[116:119]\n" \
    "s_waitcnt lgkmcnt(7)\n" \
    "v_mfma_f32_16x16x32_bf16 v[24:27], a[20:23], v[216:219], v[24:27]\n" \
    "ds_read_b128 v[184:187], v134 offset:57344\n" \
    "v_mfma_f32_16x16x32_bf16 v[56:59], a[84:87], v[216:219], v[56:59]\n" \
    "v_mfma_f32_16x16x32_bf16 v[88:91], a[148:151], v[216:219], v[88:91]\n" \
    "v_mfma_f32_16x16x32_bf16 v[120:123], a[212:215], v[216:219], v[120:123]\n" \
    "s_waitcnt lgkmcnt(7)\n" \
    "v_mfma_f32_16x16x32_bf16 v[28:31], a[20:23], v[220:223], v[28:31]\n" \
    "ds_read_b128 v[188:191], v134 offset:61440\n" \
    "v_mfma_f32_16x16x32_bf16 v[60:63], a[84:87], v[220:223], v[60:63]\n" \
    "v_mfma_f32_16x16x32_bf16 v[92:95], a[148:151], v[220:223], v[92:95]\n" \
    "v_mfma_f32_16x16x32_bf16 v[124:127], a[212:215], v[220:223], v[124:127]\n" \
    "s_waitcnt lgkmcnt(7)\n" \
    "v_mfma_f32_16x16x32_bf16 v[0:3], a[24:27], v[160:163], v[0:3]\n" \
    "ds_read_b128 v[192:195], v135 offset:32768\n" \
    "v_mfma_f32_16x16x32_bf16 v[32:35], a[88:91], v[160:163], v[32:35]\n" \
    "v_mfma_f32_16x16x32_bf16 v[64:67], a[152:155], v[160:163], v[64:67]\n" \
    "v_mfma_f32_16x16x32_bf16 v[96:99], a[216:219], v[160:163], v[96:99]\n" \
    "s_waitcnt lgkmcnt(7)\n" \
    "v_mfma_f32_16x16x32_bf16 v[4:7], a[24:27], v[164:167], v[4:7]\n" \
    "ds_read_b128 v[196:199], v135 offset:36864\n" \
    "v_mfma_f32_16x16x32_bf16 v[36:39], a[88:91], v[164:167], v[36:39]\n" \
    "v_mfma_f32_16x16x32_bf16 v[68:71], a[152:155], v[164:167], v[68:71]\n" \
    "v_mfma_f32_16x16x32_bf16 v[100:103], a[216:219], v[164:167], v[100:103]\n" \
    "s_waitcnt lgkmcnt(7)\n" \
    "v_mfma_f32_16x16x32_bf16 v[8:11], a[24:27], v[168:171], v[8:11]\n" \
    "ds_read_b128 v[200:203], v135 offset:40960\n" \
    "v_mfma_f32_16x16x32_bf16 v[40:43], a[88:91], v[168:171], v[40:43]\n" \
    "v_mfma_f32_16x16x32_bf16 v[72:75], a[152:155], v[168:171], v[72:75]\n" \
    "v_mfma_f32_16x16x32_bf16 v[104:107], a[216:219], v[168:171], v[104:107]\n" \
    "s_waitcnt lgkmcnt(7)\n" \
    "v_mfma_f32_16x16x32_bf16 v[12:15], a[24:27], v[172:175], v[12:15]\n" \
    "ds_read_b128 v[204:207], v135 offset:45056\n" \
    "v_mfma_f32_16x16x32_bf16 v[44:47], a[88:91], v[172:175], v[44:47]\n" \
    "v_mfma_f32_16x16x32_bf16 v[76:79], a[152:155], v[172:175], v[76:79]\n" \
    "v_mfma_f32_16x16x32_bf16 v[108:111], a[216:219], v[172:175], v[108:111]\n" \
    "s_waitcnt lgkmcnt(7)\n" \
    "v_mfma_f32_16x16x32_bf16 v[16:19], a[24:27], v[176:179], v[16:19]\n" \
    "ds_read_b128 v[208:211], v135 offset:49152\n" \
    "v_mfma_f32_16x16x32_bf16 v[48:51], a[88:91], v[176:179], v[48:51]\n" \
    "v_mfma_f32_16x16x32_bf16 v[80:83], a[152:155], v[176:179], v[80:83]\n" \
    "v_mfma_f32_16x16x32_bf16 v[112:115], a[216:219], v[176:179], v[112:115]\n" \
    "s_waitcnt lgkmcnt(7)\n" \
    "v_mfma_f32_16x16x32_bf16 v[20:23], a[24:27], v[180:183], v[20:23]\n" \
    "ds_read_b128 v[212:215], v135 offset:53248\n" \
    "v_mfma_f32_16x16x32_bf16 v[52:55], a[88:91], v[180:183], v[52:55]\n" \
    "v_mfma_f32_16x16x32_bf16 v[84:87], a[152:155], v[180:183], v[84:87]\n" \
    "v_mfma_f32_16x16x32_bf16 v[116:119], a[216:219], v[180:183], v[116:119]\n" \
    "s_waitcnt lgkmcnt(7)\n" \
    "v_mfma_f32_16x16x32_bf16 v[24:27], a[24:27], v[184:187], v[24:27]\n" \
    "ds_read_b128 v[216:219], v135 offset:57344\n" \
    "v_mfma_f32_16x16x32_bf16 v[56:59], a[88:91], v[184:187], v[56:59]\n" \
    "v_mfma_f32_16x16x32_bf16 v[88:91], a[152:155], v[184:187], v[88:91]\n" \
    "v_mfma_f32_16x16x32_bf16 v[120:123], a[216:219], v[184:187], v[120:123]\n" \
    "s_waitcnt lgkmcnt(7)\n" \
    "v_mfma_f32_16x16x32_bf16 v[28:31], a[24:27], v[188:191], v[28:31]\n" \
    "ds_read_b128 v[220:223], v135 offset:61440\n" \
    "v_mfma_f32_16x16x32_bf16 v[60:63], a[88:91], v[188:191], v[60:63]\n" \
    "v_mfma_f32_16x16x32_bf16 v[92:95], a[152:155], v[188:191], v[92:95]\n" \
    "v_mfma_f32_16x16x32_bf16 v[124:127], a[216:219], v[188:191], v[124:127]\n" \
    "s_waitcnt vmcnt(8) lgkmcnt(0)\n" \
    "s_barrier\n" \
    "s_cmp_eq_u32 s77, 0\n" \
    "s_cbranch_scc1 NF0_%=\n" \
    "v_mov_b32 v141, 0\n" \
    "s_mov_b64 exec, 1\n" \
    "global_store_dword v141, v141, s[72:73] sc1\n" \
    "s_mov_b64 exec, -1\n" \
    "NF0_%=:\n" \
    "v_mfma_f32_16x16x32_bf16 v[0:3], a[28:31], v[192:195], v[0:3]\n" \
    "ds_read_b128 v[160:163], v136 offset:0\n" \
    "v_mfma_f32_16x16x32_bf16 v[32:35], a[92:95], v[192:195], v[32:35]\n" \
    "s_add_u32 m0, s68, 32768\n" \
    "v_mfma_f32_16x16x32_bf16 v[64:67], a[156:159], v[192:195], v[64:67]\n" \
    "global_load_lds_dwordx4 v128, s[60:61]\n" \
    "v_mfma_f32_16x16x32_bf16 v[96:99], a[220:223], v[192:195], v[96:99]\n" \
    "s_add_u32 m0, s68, 33792\n" \
    "v_mfma_f32_16x16x32_bf16 v[4:7], a[28:31], v[196:199], v[4:7]\n" \
    "ds_read_b128 v[164:167], v136 offset:4096\n" \
    "v_mfma_f32_16x16x32_bf16 v[36:39], a[92:95], v[196:199], v[36:39]\n" \
    "global_load_lds_dwordx4 v129, s[60:61]\n" \
    "v_mfma_f32_16x16x32_bf16 v[68:71], a[156:159], v[196:199], v[68:71]\n" \
    "s_add_u32 m0, s68, 34816\n" \
    "v_mfma_f32_16x16x32_bf16 v[100:103], a[220:223], v[196:199], v[100:103]\n" \
    "global_load_lds_dwordx4 v130, s[60:61]\n" \
    "v_mfma_f32_16x16x32_bf16 v[8:11], a[28:31], v[200:203], v[8:11]\n" \
    "ds_read_b128 v[168:171], v136 offset:8192\n" \
    "v_mfma_f32_16x16x32_bf16 v[40:43], a[92:95], v[200:203], v[40:43]\n" \
    "s_add_u32 m0, s68, 35840\n" \
    "v_mfma_f32_16x16x32_bf16 v[72:75], a[156:159], v[200:203], v[72:75]\n" \
    "global_load_lds_dwordx4 v131, s[60:61]\n" \
    "v_mfma_f32_16x16x32_bf16 v[104:107], a[220:223], v[200:203], v[104:107]\n" \
    "s_add_u32 m0, s68, 36864\n" \
    "v_mfma_f32_16x16x32_bf16 v[12:15], a[28:31], v[204:207], v[12:15]\n" \
    "ds_read_b128 v[172:175], v136 offset:12288\n" \
    "v_mfma_f32_16x16x32_bf16 v[44:47], a[92:95], v[204:207], v[44:47]\n" \
    "global_load_lds_dwordx4 v128, s[62:63]\n" \
    "v_mfma_f32_16x16x32_bf16 v[76:79], a[156:159], v[204:207], v[76:79]\n" \
    "s_add_u32 m0, s68, 37888\n" \
    "v_mfma_f32_16x16x32_bf16 v[108:111], a[220:223], v[204:207], v[108:111]\n" \
    "global_load_lds_dwordx4 v129, s[62:63]\n" \
    "v_mfma_f32_16x16x32_bf16 v[16:19], a[28:31], v[208:211], v[16:19]\n" \
    "ds_read_b128 v[176:179], v136 offset:16384\n" \
    "v_mfma_f32_16x16x32_bf16 v[48:51], a[92:95], v[208:211], v[48:51]\n" \
    "s_add_u32 m0, s68, 38912\n" \
    "v_mfma_f32_16x16x32_bf16 v[80:83], a[156:159], v[208:211], v[80:83]\n" \
    "global_load_lds_dwordx4 v130, s[62:63]\n" \
    "v_mfma_f32_16x16x32_bf16 v[112:115], a[220:223], v[208:211], v[112:115]\n" \
    "s_add_u32 m0, s68, 39936\n" \
    "v_mfma_f32_16x16x32_bf16 v[20:23], a[28:31], v[212:215], v[20:23]\n" \
    "ds_read_b128 v[180:183], v136 offset:20480\n" \
    "v_mfma_f32_16x16x32_bf16 v[52:55], a[92:95], v[212:215], v[52:55]\n" \
    "global_load_lds_dwordx4 v131, s[62:63]\n" \
    "v_mfma_f32_16x16x32_bf16 v[84:87], a[156:159], v[212:215], v[84:87]\n" \
    "s_add_u32 s60, s60, 256\n" \
    "v_mfma_f32_16x16x32_bf16 v[116:119], a[220:223], v[212:215], v[116:119]\n" \
    "s_addc_u32 s61, s61, 0\n" \
    "v_mfma_f32_16x16x32_bf16 v[24:27], a[28:31], v[216:219], v[24:27]\n" \
    "ds_read_b128 v[184:187], v136 offset:24576\n" \
    "v_mfma_f32_16x16x32_bf16 v[56:59], a[92:95], v[216:219], v[56:59]\n" \
    "s_add_u32 s62, s62, 256\n" \
    "v_mfma_f32_16x16x32_bf16 v[88:91], a[156:159], v[216:219], v[88:91]\n" \
    "s_addc_u32 s63, s63, 0\n" \
    "v_mfma_f32_16x16x32_bf16 v[120:123], a[220:223], v[216:219], v[120:123]\n" \
    "v_mfma_f32_16x16x32_bf16 v[28:31], a[28:31], v[220:223], v[28:31]\n" \
    "ds_read_b128 v[188:191], v136 offset:28672\n" \
    "v_mfma_f32_16x16x32_bf16 v[60:63], a[92:95], v[220:223], v[60:63]\n" \
    "v_mfma_f32_16x16x32_bf16 v[92:95], a[156:159], v[220:223], v[92:95]\n" \
    "v_mfma_f32_16x16x32_bf16 v[124:127], a[220:223], v[220:223], v[124:127]\n" \
    "s_waitcnt lgkmcnt(7)\n" \
    "v_mfma_f32_16x16x32_bf16 v[0:3], a[32:35], v[160:163], v[0:3]\n" \
    "ds_read_b128 v[192:195], v137 offset:0\n" \
    "v_mfma_f32_16x16x32_bf16 v[32:35], a[96:99], v[160:163], v[32:35]\n" \
    "v_mfma_f32_16x16x32_bf16 v[64:67], a[160:163], v[160:163], v[64:67]\n" \
    "v_mfma_f32_16x16x32_bf16 v[96:99], a[224:227], v[160:163], v[96:99]\n" \
    "s_waitcnt lgkmcnt(7)\n" \
    "v_mfma_f32_16x16x32_bf16 v[4:7], a[32:35], v[164:167], v[4:7]\n" \
    "ds_read_b128 v[196:199], v137 offset:4096\n" \
    "v_mfma_f32_16x16x32_bf16 v[36:39], a[96:99], v[164:167], v[36:39]\n" \
    "v_mfma_f32_16x16x32_bf16 v[68:71], a[160:163], v[164:167], v[68:71]\n" \
    "v_mfma_f32_16x16x32_bf16 v[100:103], a[224:227], v[164:167], v[100:103]\n" \
    "s_waitcnt lgkmcnt(7)\n" \
    "v_mfma_f32_16x16x32_bf16 v[8:11], a[32:35], v[168:171], v[8:11]\n" \
    "ds_read_b128 v[200:203], v137 offset:8192\n" \
    "v_mfma_f32_16x16x32_bf16 v[40:43], a[96:99], v[168:171], v[40:43]\n" \
    "v_mfma_f32_16x16x32_bf16 v[72:75], a[160:163], v[168:171], v[72:75]\n" \
    "v_mfma_f32_16x16x32_bf16 v[104:107], a[224:227], v[168:171], v[104:107]\n" \
    "s_waitcnt lgkmcnt(7)\n" \
    "v_mfma_f32_16x16x32_bf16 v[12:15], a[32:35], v[172:175], v[12:15]\n" \
    "ds_read_b128 v[204:207], v137 offset:12288\n" \
    "v_mfma_f32_16x16x32_bf16 v[44:47], a[96:99], v[172:175], v[44:47]\n" \
    "v_mfma_f32_16x16x32_bf16 v[76:79], a[160:163], v[172:175], v[76:79]\n" \
    "v_mfma_f32_16x16x32_bf16 v[108:111], a[224:227], v[172:175], v[108:111]\n" \
    "s_waitcnt lgkmcnt(7)\n" \
    "v_mfma_f32_16x16x32_bf16 v[16:19], a[32:35], v[176:179], v[16:19]\n" \
    "ds_read_b128 v[208:211], v137 offset:16384\n" \
    "v_mfma_f32_16x16x32_bf16 v[48:51], a[96:99], v[176:179], v[48:51]\n" \
    "v_mfma_f32_16x16x32_bf16 v[80:83], a[160:163], v[176:179], v[80:83]\n" \
    "v_mfma_f32_16x16x32_bf16 v[112:115], a[224:227], v[176:179], v[112:115]\n" \
    "s_waitcnt lgkmcnt(7)\n" \
    "v_mfma_f32_16x16x32_bf16 v[20:23], a[32:35], v[180:183], v[20:23]\n" \
    "ds_read_b128 v[212:215], v137 offset:20480\n" \
    "v_mfma_f32_16x16x32_bf16 v[52:55], a[96:99], v[180:183], v[52:55]\n" \
    "v_mfma_f32_16x16x32_bf16 v[84:87], a[160:163], v[180:183], v[84:87]\n" \
    "v_mfma_f32_16x16x32_bf16 v[116:119], a[224:227], v[180:183], v[116:119]\n" \
    "s_waitcnt lgkmcnt(7)\n" \
    "v_mfma_f32_16x16x32_bf16 v[24:27], a[32:35], v[184:187], v[24:27]\n" \
    "ds_read_b128 v[216:219], v137 offset:24576\n" \
    "v_mfma_f32_16x16x32_bf16 v[56:59], a[96:99], v[184:187], v[56:59]\n" \
    "v_mfma_f32_16x16x32_bf16 v[88:91], a[160:163], v[184:187], v[88:91]\n" \
    "v_mfma_f32_16x16x32_bf16 v[120:123], a[224:227], v[184:187], v[120:123]\n" \
    "s_waitcnt lgkmcnt(7)\n" \
    "v_mfma_f32_16x16x32_bf16 v[28:31], a[32:35], v[188:191], v[28:31]\n" \
    "ds_read_b128 v[220:223], v137 offset:28672\n" \
    "v_mfma_f32_16x16x32_bf16 v[60:63], a[96:99], v[188:191], v[60:63]\n" \
    "v_mfma_f32_16x16x32_bf16 v[92:95], a[160:163], v[188:191], v[92:95]\n" \
    "v_mfma_f32_16x16x32_bf16 v[124:127], a[224:227], v[188:191], v[124:127]\n" \
    "s_waitcnt lgkmcnt(7)\n" \
    "v_mfma_f32_16x16x32_bf16 v[0:3], a[36:39], v[192:195], v[0:3]\n" \
    "ds_read_b128 v[160:163], v138 offset:0\n" \
    "v_mfma_f32_16x16x32_bf16 v[32:35], a[100:103], v[192:195], v[32:35]\n" \
    "v_mfma_f32_16x16x32_bf16 v[64:67], a[164:167], v[192:195], v[64:67]\n" \
    "v_mfma_f32_16x16x32_bf16 v[96:99], a[228:231], v[192:195], v[96:99]\n" \
    "s_waitcnt lgkmcnt(7)\n" \
    "v_mfma_f32_16x16x32_bf16 v[4:7], a[36:39], v[196:199], v[4:7]\n" \
    "ds_read_b128 v[164:167], v138 offset:4096\n" \
    "v_mfma_f32_16x16x32_bf16 v[36:39], a[100:103], v[196:199], v[36:39]\n" \
    "v_mfma_f32_16x16x32_bf16 v[68:71], a[164:167], v[196:199], v[68:71]\n" \
    "v_mfma_f32_16x16x32_bf16 v[100:103], a[228:231], v[196:199], v[100:103]\n" \
    "s_waitcnt lgkmcnt(7)\n" \
    "v_mfma_f32_16x16x32_bf16 v[8:11], a[36:39], v[200:203], v[8:11]\n" \
    "ds_read_b128 v[168:171], v138 offset:8192\n" \
    "v_mfma_f32_16x16x32_bf16 v[40:43], a[100:103], v[200:203], v[40:43]\n" \
    "v_mfma_f32_16x16x32_bf16 v[72:75], a[164:167], v[200:203], v[72:75]\n" \
    "v_mfma_f32_16x16x32_bf16 v[104:107], a[228:231], v[200:203], v[104:107]\n" \
    "s_waitcnt lgkmcnt(7)\n" \
    "v_mfma_f32_16x16x32_bf16 v[12:15], a[36:39], v[204:207], v[12:15]\n" \
    "ds_read_b128 v[172:175], v138 offset:12288\n" \
    "v_mfma_f32_16x16x32_bf16 v[44:47], a[100:103], v[204:207], v[44:47]\n" \
    "v_mfma_f32_16x16x32_bf16 v[76:79], a[164:167], v[204:207], v[76:79]\n" \
    "v_mfma_f32_16x16x32_bf16 v[108:111], a[228:231], v[204:207], v[108:111]\n" \
    "s_waitcnt lgkmcnt(7)\n" \
    "v_mfma_f32_16x16x32_bf16 v[16:19], a[36:39], v[208:211], v[16:19]\n" \
    "ds_read_b128 v[176:179], v138 offset:16384\n" \
    "v_mfma_f32_16x16x32_bf16 v[48:51], a[100:103], v[208:211], v[48:51]\n" \
    "v_mfma_f32_16x16x32_bf16 v[80:83], a[164:167], v[208:211], v[80:83]\n" \
    "v_mfma_f32_16x16x32_bf16 v[112:115], a[228:231], v[208:211], v[112:115]\n" \
    "s_waitcnt lgkmcnt(7)\n" \
    "v_mfma_f32_16x16x32_bf16 v[20:23], a[36:39], v[212:215], v[20:23]\n" \
    "ds_read_b128 v[180:183], v138 offset:20480\n" \
    "v_mfma_f32_16x16x32_bf16 v[52:55], a[100:103], v[212:215], v[52:55]\n" \
    "v_mfma_f32_16x16x32_bf16 v[84:87], a[164:167], v[212:215], v[84:87]\n" \
    "v_mfma_f32_16x16x32_bf16 v[116:119], a[228:231], v[212:215], v[116:119]\n" \
    "s_waitcnt lgkmcnt(7)\n" \
    "v_mfma_f32_16x16x32_bf16 v[24:27], a[36:39], v[216:219], v[24:27]\n" \
    "ds_read_b128 v[184:187], v138 offset:24576\n" \
    "v_mfma_f32_16x16x32_bf16 v[56:59], a[100:103], v[216:219], v[56:59]\n" \
    "v_mfma_f32_16x16x32_bf16 v[88:91], a[164:167], v[216:219], v[88:91]\n" \
    "v_mfma_f32_16x16x32_bf16 v[120:123], a[228:231], v[216:219], v[120:123]\n" \
    "s_waitcnt lgkmcnt(7)\n" \
    "v_mfma_f32_16x16x32_bf16 v[28:31], a[36:39], v[220:223], v[28:31]\n" \
    "ds_read_b128 v[188:191], v138 offset:28672\n" \
    "v_mfma_f32_16x16x32_bf16 v[60:63], a[100:103], v[220:223], v[60:63]\n" \
    "v_mfma_f32_16x16x32_bf16 v[92:95], a[164:167], v[220:223], v[92:95]\n" \
    "v_mfma_f32_16x16x32_bf16 v[124:127], a[228:231], v[220:223], v[124:127]\n" \
    "s_waitcnt lgkmcnt(7)\n" \
    "v_mfma_f32_16x16x32_bf16 v[0:3], a[40:43], v[160:163], v[0:3]\n" \
    "ds_read_b128 v[192:195], v139 offset:0\n" \
    "v_mfma_f32_16x16x32_bf16 v[32:35], a[104:107], v[160:163], v[32:35]\n" \
    "v_mfma_f32_16x16x32_bf16 v[64:67], a[168:171], v[160:163], v[64:67]\n" \
    "v_mfma_f32_16x16x32_bf16 v[96:99], a[232:235], v[160:163], v[96:99]\n" \
    "s_waitcnt lgkmcnt(7)\n" \
    "v_mfma_f32_16x16x32_bf16 v[4:7], a[40:43], v[164:167], v[4:7]\n" \
    "ds_read_b128 v[196:199], v139 offset:4096\n" \
    "v_mfma_f32_16x16x32_bf16 v[36:39], a[104:107], v[164:167], v[36:39]\n" \
    "v_mfma_f32_16x16x32_bf16 v[68:71], a[168:171], v[164:167], v[68:71]\n" \
    "v_mfma_f32_16x16x32_bf16 v[100:103], a[232:235], v[164:167], v[100:103]\n" \
    "s_waitcnt lgkmcnt(7)\n" \
    "v_mfma_f32_16x16x32_bf16 v[8:11], a[40:43], v[168:171], v[8:11]\n" \
    "ds_read_b128 v[200:203], v139 offset:8192\n" \
    "v_mfma_f32_16x16x32_bf16 v[40:43], a[104:107], v[168:171], v[40:43]\n" \
    "v_mfma_f32_16x16x32_bf16 v[72:75], a[168:171], v[168:171], v[72:75]\n" \
    "v_mfma_f32_16x16x32_bf16 v[104:107], a[232:235], v[168:171], v[104:107]\n" \
    "s_waitcnt lgkmcnt(7)\n" \
    "v_mfma_f32_16x16x32_bf16 v[12:15], a[40:43], v[172:175], v[12:15]\n" \
    "ds_read_b128 v[204:207], v139 offset:12288\n" \
    "v_mfma_f32_16x16x32_bf16 v[44:47], a[104:107], v[172:175], v[44:47]\n" \
    "v_mfma_f32_16x16x32_bf16 v[76:79], a[168:171], v[172:175], v[76:79]\n" \
    "v_mfma_f32_16x16x32_bf16 v[108:111], a[232:235], v[172:175], v[108:111]\n" \
    "s_waitcnt lgkmcnt(7)\n" \
    "v_mfma_f32_16x16x32_bf16 v[16:19], a[40:43], v[176:179], v[16:19]\n" \
    "ds_read_b128 v[208:211], v139 offset:16384\n" \
    "v_mfma_f32_16x16x32_bf16 v[48:51], a[104:107], v[176:179], v[48:51]\n" \
    "v_mfma_f32_16x16x32_bf16 v[80:83], a[168:171], v[176:179], v[80:83]\n" \
    "v_mfma_f32_16x16x32_bf16 v[112:115], a[232:235], v[176:179], v[112:115]\n" \
    "s_waitcnt lgkmcnt(7)\n" \
    "v_mfma_f32_16x16x32_bf16 v[20:23], a[40:43], v[180:183], v[20:23]\n" \
    "ds_read_b128 v[212:215], v139 offset:20480\n" \
    "v_mfma_f32_16x16x32_bf16 v[52:55], a[104:107], v[180:183], v[52:55]\n" \
    "v_mfma_f32_16x16x32_bf16 v[84:87], a[168:171], v[180:183], v[84:87]\n" \
    "v_mfma_f32_16x16x32_bf16 v[116:119], a[232:235], v[180:183], v[116:119]\n" \
    "s_waitcnt lgkmcnt(7)\n" \
    "v_mfma_f32_16x16x32_bf16 v[24:27], a[40:43], v[184:187], v[24:27]\n" \
    "ds_read_b128 v[216:219], v139 offset:24576\n" \
    "v_mfma_f32_16x16x32_bf16 v[56:59], a[104:107], v[184:187], v[56:59]\n" \
    "v_mfma_f32_16x16x32_bf16 v[88:91], a[168:171], v[184:187], v[88:91]\n" \
    "v_mfma_f32_16x16x32_bf16 v[120:123], a[232:235], v[184:187], v[120:123]\n" \
    "s_waitcnt lgkmcnt(7)\n" \
    "v_mfma_f32_16x16x32_bf16 v[28:31], a[40:43], v[188:191], v[28:31]\n" \
    "ds_read_b128 v[220:223], v139 offset:28672\n" \
    "v_mfma_f32_16x16x32_bf16 v[60:63], a[104:107], v[188:191], v[60:63]\n" \
    "v_mfma_f32_16x16x32_bf16 v[92:95], a[168:171], v[188:191], v[92:95]\n" \
    "v_mfma_f32_16x16x32_bf16 v[124:127], a[232:235], v[188:191], v[124:127]\n" \
    "s_waitcnt vmcnt(16) lgkmcnt(0)\n" \
    "s_barrier\n" \
    "v_mfma_f32_16x16x32_bf16 v[0:3], a[44:47], v[192:195], v[0:3]\n" \
    "ds_read_b128 v[160:163], v136 offset:32768\n" \
    "v_mfma_f32_16x16x32_bf16 v[32:35], a[108:111], v[192:195], v[32:35]\n" \
    "s_add_u32 m0, s68, 65536\n" \
    "v_mfma_f32_16x16x32_bf16 v[64:67], a[172:175], v[192:195], v[64:67]\n" \
    "global_load_lds_dwordx4 v128, s[60:61]\n" \
    "v_mfma_f32_16x16x32_bf16 v[96:99], a[236:239], v[192:195], v[96:99]\n" \
    "s_add_u32 m0, s68, 66560\n" \
    "v_mfma_f32_16x16x32_bf16 v[4:7], a[44:47], v[196:199], v[4:7]\n" \
    "ds_read_b128 v[164:167], v136 offset:36864\n" \
    "v_mfma_f32_16x16x32_bf16 v[36:39], a[108:111], v[196:199], v[36:39]\n" \
    "global_load_lds_dwordx4 v129, s[60:61]\n" \
    "v_mfma_f32_16x16x32_bf16 v[68:71], a[172:175], v[196:199], v[68:71]\n" \
    "s_add_u32 m0, s68, 67584\n" \
    "v_mfma_f32_16x16x32_bf16 v[100:103], a[236:239], v[196:199], v[100:103]\n" \
    "global_load_lds_dwordx4 v130, s[60:61]\n" \
    "v_mfma_f32_16x16x32_bf16 v[8:11], a[44:47], v[200:203], v[8:11]\n" \
    "ds_read_b128 v[168:171], v136 offset:40960\n" \
    "v_mfma_f32_16x16x32_bf16 v[40:43], a[108:111], v[200:203], v[40:43]\n" \
    "s_add_u32 m0, s68, 68608\n" \
    "v_mfma_f32_16x16x32_bf16 v[72:75], a[172:175], v[200:203], v[72:75]\n" \
    "global_load_lds_dwordx4 v131, s[60:61]\n" \
    "v_mfma_f32_16x16x32_bf16 v[104:107], a[236:239], v[200:203], v[104:107]\n" \
    "s_add_u32 m0, s68, 69632\n" \
    "v_mfma_f32_16x16x32_bf16 v[12:15], a[44:47], v[204:207], v[12:15]\n" \
    "ds_read_b128 v[172:175], v136 offset:45056\n" \
    "v_mfma_f32_16x16x32_bf16 v[44:47], a[108:111], v[204:207], v[44:47]\n" \
    "global_load_lds_dwordx4 v128, s[62:63]\n" \
    "v_mfma_f32_16x16x32_bf16 v[76:79], a[172:175], v[204:207], v[76:79]\n" \
    "s_add_u32 m0, s68, 70656\n" \
    "v_mfma_f32_16x16x32_bf16 v[108:111], a[236:239], v[204:207], v[108:111]\n" \
    "global_load_lds_dwordx4 v129, s[62:63]\n" \
    "v_mfma_f32_16x16x32_bf16 v[16:19], a[44:47], v[208:211], v[16:19]\n" \
    "ds_read_b128 v[176:179], v136 offset:49152\n" \
    "v_mfma_f32_16x16x32_bf16 v[48:51], a[108:111], v[208:211], v[48:51]\n" \
    "s_add_u32 m0, s68, 71680\n" \
    "v_mfma_f32_16x16x32_bf16 v[80:83], a[172:175], v[208:211], v[80:83]\n" \
    "global_load_lds_dwordx4 v130, s[62:63]\n" \
    "v_mfma_f32_16x16x32_bf16 v[112:115], a[236:239], v[208:211], v[112:115]\n" \
    "s_add_u32 m0, s68, 72704\n" \
    "v_mfma_f32_16x16x32_bf16 v[20:23], a[44:47], v[212:215], v[20:23]\n" \
    "ds_read_b128 v[180:183], v136 offset:53248\n" \
    "v_mfma_f32_16x16x32_bf16 v[52:55], a[108:111], v[212:215], v[52:55]\n" \
    "global_load_lds_dwordx4 v131, s[62:63]\n" \
    "v_mfma_f32_16x16x32_bf16 v[84:87], a[172:175], v[212:215], v[84:87]\n" \
    "s_add_u32 s60, s60, 256\n" \
    "v_mfma_f32_16x16x32_bf16 v[116:119], a[236:239], v[212:215], v[116:119]\n" \
    "s_addc_u32 s61, s61, 0\n" \
    "v_mfma_f32_16x16x32_bf16 v[24:27], a[44:47], v[216:219], v[24:27]\n" \
    "ds_read_b128 v[184:187], v136 offset:57344\n" \
    "v_mfma_f32_16x16x32_bf16 v[56:59], a[108:111], v[216:219], v[56:59]\n" \
    "s_add_u32 s62, s62, 256\n" \
    "v_mfma_f32_16x16x32_bf16 v[88:91], a[172:175], v[216:219], v[88:91]\n" \
    "s_addc_u32 s63, s63, 0\n" \
    "v_mfma_f32_16x16x32_bf16 v[120:123], a[236:239], v[216:219], v[120:123]\n" \
    "v_mfma_f32_16x16x32_bf16 v[28:31], a[44:47], v[220:223], v[28:31]\n" \
    "ds_read_b128 v[188:191], v136 offset:61440\n" \
    "v_mfma_f32_16x16x32_bf16 v[60:63], a[108:111], v[220:223], v[60:63]\n" \
    "v_mfma_f32_16x16x32_bf16 v[92:95], a[172:175], v[220:223], v[92:95]\n" \
    "v_mfma_f32_16x16x32_bf16 v[124:127], a[236:239], v[220:223], v[124:127]\n" \
    "s_waitcnt lgkmcnt(7)\n" \
    "v_mfma_f32_16x16x32_bf16 v[0:3], a[48:51], v[160:163], v[0:3]\n" \
    "ds_read_b128 v[192:195], v137 offset:32768\n" \
    "v_mfma_f32_16x16x32_bf16 v[32:35], a[112:115], v[160:163], v[32:35]\n" \
    "v_mfma_f32_16x16x32_bf16 v[64:67], a[176:179], v[160:163], v[64:67]\n" \
    "v_mfma_f32_16x16x32_bf16 v[96:99], a[240:243], v[160:163], v[96:99]\n" \
    "s_waitcnt lgkmcnt(7)\n" \
    "v_mfma_f32_16x16x32_bf16 v[4:7], a[48:51], v[164:167], v[4:7]\n" \
    "ds_read_b128 v[196:199], v137 offset:36864\n" \
    "v_mfma_f32_16x16x32_bf16 v[36:39], a[112:115], v[164:167], v[36:39]\n" \
    "v_mfma_f32_16x16x32_bf16 v[68:71], a[176:179], v[164:167], v[68:71]\n" \
    "v_mfma_f32_16x16x32_bf16 v[100:103], a[240:243], v[164:167], v[100:103]\n" \
    "s_waitcnt lgkmcnt(7)\n" \
    "v_mfma_f32_16x16x32_bf16 v[8:11], a[48:51], v[168:171], v[8:11]\n" \
    "ds_read_b128 v[200:203], v137 offset:40960\n" \
    "v_mfma_f32_16x16x32_bf16 v[40:43], a[112:115], v[168:171], v[40:43]\n" \
    "v_mfma_f32_16x16x32_bf16 v[72:75], a[176:179], v[168:171], v[72:75]\n" \
    "v_mfma_f32_16x16x32_bf16 v[104:107], a[240:243], v[168:171], v[104:107]\n" \
    "s_waitcnt lgkmcnt(7)\n" \
    "v_mfma_f32_16x16x32_bf16 v[12:15], a[48:51], v[172:175], v[12:15]\n" \
    "ds_read_b128 v[204:207], v137 offset:45056\n" \
    "v_mfma_f32_16x16x32_bf16 v[44:47], a[112:115], v[172:175], v[44:47]\n" \
    "v_mfma_f32_16x16x32_bf16 v[76:79], a[176:179], v[172:175], v[76:79]\n" \
    "v_mfma_f32_16x16x32_bf16 v[108:111], a[240:243], v[172:175], v[108:111]\n" \
    "s_waitcnt lgkmcnt(7)\n" \
    "v_mfma_f32_16x16x32_bf16 v[16:19], a[48:51], v[176:179], v[16:19]\n" \
    "ds_read_b128 v[208:211], v137 offset:49152\n" \
    "v_mfma_f32_16x16x32_bf16 v[48:51], a[112:115], v[176:179], v[48:51]\n" \
    "v_mfma_f32_16x16x32_bf16 v[80:83], a[176:179], v[176:179], v[80:83]\n" \
    "v_mfma_f32_16x16x32_bf16 v[112:115], a[240:243], v[176:179], v[112:115]\n" \
    "s_waitcnt lgkmcnt(7)\n" \
    "v_mfma_f32_16x16x32_bf16 v[20:23], a[48:51], v[180:183], v[20:23]\n" \
    "ds_read_b128 v[212:215], v137 offset:53248\n" \
    "v_mfma_f32_16x16x32_bf16 v[52:55], a[112:115], v[180:183], v[52:55]\n" \
    "v_mfma_f32_16x16x32_bf16 v[84:87], a[176:179], v[180:183], v[84:87]\n" \
    "v_mfma_f32_16x16x32_bf16 v[116:119], a[240:243], v[180:183], v[116:119]\n" \
    "s_waitcnt lgkmcnt(7)\n" \
    "v_mfma_f32_16x16x32_bf16 v[24:27], a[48:51], v[184:187], v[24:27]\n" \
    "ds_read_b128 v[216:219], v137 offset:57344\n" \
    "v_mfma_f32_16x16x32_bf16 v[56:59], a[112:115], v[184:187], v[56:59]\n" \
    "v_mfma_f32_16x16x32_bf16 v[88:91], a[176:179], v[184:187], v[88:91]\n" \
    "v_mfma_f32_16x16x32_bf16 v[120:123], a[240:243], v[184:187], v[120:123]\n" \
    "s_waitcnt lgkmcnt(7)\n" \
    "v_mfma_f32_16x16x32_bf16 v[28:31], a[48:51], v[188:191], v[28:31]\n" \
    "ds_read_b128 v[220:223], v137 offset:61440\n" \
    "v_mfma_f32_16x16x32_bf16 v[60:63], a[112:115], v[188:191], v[60:63]\n" \
    "v_mfma_f32_16x16x32_bf16 v[92:95], a[176:179], v[188:191], v[92:95]\n" \
    "v_mfma_f32_16x16x32_bf16 v[124:127], a[240:243], v[188:191], v[124:127]\n" \
    "s_waitcnt lgkmcnt(7)\n" \
    "v_mfma_f32_16x16x32_bf16 v[0:3], a[52:55], v[192:195], v[0:3]\n" \
    "ds_read_b128 v[160:163], v138 offset:32768\n" \
    "v_mfma_f32_16x16x32_bf16 v[32:35], a[116:119], v[192:195], v[32:35]\n" \
    "v_mfma_f32_16x16x32_bf16 v[64:67], a[180:183], v[192:195], v[64:67]\n" \
    "v_mfma_f32_16x16x32_bf16 v[96:99], a[244:247], v[192:195], v[96:99]\n" \
    "s_waitcnt lgkmcnt(7)\n" \
    "v_mfma_f32_16x16x32_bf16 v[4:7], a[52:55], v[196:199], v[4:7]\n" \
    "ds_read_b128 v[164:167], v138 offset:36864\n" \
    "v_mfma_f32_16x16x32_bf16 v[36:39], a[116:119], v[196:199], v[36:39]\n" \
    "v_mfma_f32_16x16x32_bf16 v[68:71], a[180:183], v[196:199], v[68:71]\n" \
    "v_mfma_f32_16x16x32_bf16 v[100:103], a[244:247], v[196:199], v[100:103]\n" \
    "s_waitcnt lgkmcnt(7)\n" \
    "v_mfma_f32_16x16x32_bf16 v[8:11], a[52:55], v[200:203], v[8:11]\n" \
    "ds_read_b128 v[168:171], v138 offset:40960\n" \
    "v_mfma_f32_16x16x32_bf16 v[40:43], a[116:119], v[200:203], v[40:43]\n" \
    "v_mfma_f32_16x16x32_bf16 v[72:75], a[180:183], v[200:203], v[72:75]\n" \
    "v_mfma_f32_16x16x32_bf16 v[104:107], a[244:247], v[200:203], v[104:107]\n" \
    "s_waitcnt lgkmcnt(7)\n" \
    "v_mfma_f32_16x16x32_bf16 v[12:15], a[52:55], v[204:207], v[12:15]\n" \
    "ds_read_b128 v[172:175], v138 offset:45056\n" \
    "v_mfma_f32_16x16x32_bf16 v[44:47], a[116:119], v[204:207], v[44:47]\n" \
    "v_mfma_f32_16x16x32_bf16 v[76:79], a[180:183], v[204:207], v[76:79]\n" \
    "v_mfma_f32_16x16x32_bf16 v[108:111], a[244:247], v[204:207], v[108:111]\n" \
    "s_waitcnt lgkmcnt(7)\n" \
    "v_mfma_f32_16x16x32_bf16 v[16:19], a[52:55], v[208:211], v[16:19]\n" \
    "ds_read_b128 v[176:179], v138 offset:49152\n" \
    "v_mfma_f32_16x16x32_bf16 v[48:51], a[116:119], v[208:211], v[48:51]\n" \
    "v_mfma_f32_16x16x32_bf16 v[80:83], a[180:183], v[208:211], v[80:83]\n" \
    "v_mfma_f32_16x16x32_bf16 v[112:115], a[244:247], v[208:211], v[112:115]\n" \
    "s_waitcnt lgkmcnt(7)\n" \
    "v_mfma_f32_16x16x32_bf16 v[20:23], a[52:55], v[212:215], v[20:23]\n" \
    "ds_read_b128 v[180:183], v138 offset:53248\n" \
    "v_mfma_f32_16x16x32_bf16 v[52:55], a[116:119], v[212:215], v[52:55]\n" \
    "v_mfma_f32_16x16x32_bf16 v[84:87], a[180:183], v[212:215], v[84:87]\n" \
    "v_mfma_f32_16x16x32_bf16 v[116:119], a[244:247], v[212:215], v[116:119]\n" \
    "s_waitcnt lgkmcnt(7)\n" \
    "v_mfma_f32_16x16x32_bf16 v[24:27], a[52:55], v[216:219], v[24:27]\n" \
    "ds_read_b128 v[184:187], v138 offset:57344\n" \
    "v_mfma_f32_16x16x32_bf16 v[56:59], a[116:119], v[216:219], v[56:59]\n" \
    "v_mfma_f32_16x16x32_bf16 v[88:91], a[180:183], v[216:219], v[88:91]\n" \
    "v_mfma_f32_16x16x32_bf16 v[120:123], a[244:247], v[216:219], v[120:123]\n" \
    "s_waitcnt lgkmcnt(7)\n" \
    "v_mfma_f32_16x16x32_bf16 v[28:31], a[52:55], v[220:223], v[28:31]\n" \
    "ds_read_b128 v[188:191], v138 offset:61440\n" \
    "v_mfma_f32_16x16x32_bf16 v[60:63], a[116:119], v[220:223], v[60:63]\n" \
    "v_mfma_f32_16x16x32_bf16 v[92:95], a[180:183], v[220:223], v[92:95]\n" \
    "v_mfma_f32_16x16x32_bf16 v[124:127], a[244:247], v[220:223], v[124:127]\n" \
    "s_waitcnt lgkmcnt(7)\n" \
    "v_mfma_f32_16x16x32_bf16 v[0:3], a[56:59], v[160:163], v[0:3]\n" \
    "ds_read_b128 v[192:195], v139 offset:32768\n" \
    "v_mfma_f32_16x16x32_bf16 v[32:35], a[120:123], v[160:163], v[32:35]\n" \
    "v_mfma_f32_16x16x32_bf16 v[64:67], a[184:187], v[160:163], v[64:67]\n" \
    "v_mfma_f32_16x16x32_bf16 v[96:99], a[248:251], v[160:163], v[96:99]\n" \
    "s_waitcnt lgkmcnt(7)\n" \
    "v_mfma_f32_16x16x32_bf16 v[4:7], a[56:59], v[164:167], v[4:7]\n" \
    "ds_read_b128 v[196:199], v139 offset:36864\n" \
    "v_mfma_f32_16x16x32_bf16 v[36:39], a[120:123], v[164:167], v[36:39]\n" \
    "v_mfma_f32_16x16x32_bf16 v[68:71], a[184:187], v[164:167], v[68:71]\n" \
    "v_mfma_f32_16x16x32_bf16 v[100:103], a[248:251], v[164:167], v[100:103]\n" \
    "s_waitcnt lgkmcnt(7)\n" \
    "v_mfma_f32_16x16x32_bf16 v[8:11], a[56:59], v[168:171], v[8:11]\n" \
    "ds_read_b128 v[200:203], v139 offset:40960\n" \
    "v_mfma_f32_16x16x32_bf16 v[40:43], a[120:123], v[168:171], v[40:43]\n" \
    "v_mfma_f32_16x16x32_bf16 v[72:75], a[184:187], v[168:171], v[72:75]\n" \
    "v_mfma_f32_16x16x32_bf16 v[104:107], a[248:251], v[168:171], v[104:107]\n" \
    "s_waitcnt lgkmcnt(7)\n" \
    "v_mfma_f32_16x16x32_bf16 v[12:15], a[56:59], v[172:175], v[12:15]\n" \
    "ds_read_b128 v[204:207], v139 offset:45056\n" \
    "v_mfma_f32_16x16x32_bf16 v[44:47], a[120:123], v[172:175], v[44:47]\n" \
    "v_mfma_f32_16x16x32_bf16 v[76:79], a[184:187], v[172:175], v[76:79]\n" \
    "v_mfma_f32_16x16x32_bf16 v[108:111], a[248:251], v[172:175], v[108:111]\n" \
    "s_waitcnt lgkmcnt(7)\n" \
    "v_mfma_f32_16x16x32_bf16 v[16:19], a[56:59], v[176:179], v[16:19]\n" \
    "ds_read_b128 v[208:211], v139 offset:49152\n" \
    "v_mfma_f32_16x16x32_bf16 v[48:51], a[120:123], v[176:179], v[48:51]\n" \
    "v_mfma_f32_16x16x32_bf16 v[80:83], a[184:187], v[176:179], v[80:83]\n" \
    "v_mfma_f32_16x16x32_bf16 v[112:115], a[248:251], v[176:179], v[112:115]\n" \
    "s_waitcnt lgkmcnt(7)\n" \
    "v_mfma_f32_16x16x32_bf16 v[20:23], a[56:59], v[180:183], v[20:23]\n" \
    "ds_read_b128 v[212:215], v139 offset:53248\n" \
    "v_mfma_f32_16x16x32_bf16 v[52:55], a[120:123], v[180:183], v[52:55]\n" \
    "v_mfma_f32_16x16x32_bf16 v[84:87], a[184:187], v[180:183], v[84:87]\n" \
    "v_mfma_f32_16x16x32_bf16 v[116:119], a[248:251], v[180:183], v[116:119]\n" \
    "s_waitcnt lgkmcnt(7)\n" \
    "v_mfma_f32_16x16x32_bf16 v[24:27], a[56:59], v[184:187], v[24:27]\n" \
    "ds_read_b128 v[216:219], v139 offset:57344\n" \
    "v_mfma_f32_16x16x32_bf16 v[56:59], a[120:123], v[184:187], v[56:59]\n" \
    "v_mfma_f32_16x16x32_bf16 v[88:91], a[184:187], v[184:187], v[88:91]\n" \
    "v_mfma_f32_16x16x32_bf16 v[120:123], a[248:251], v[184:187], v[120:123]\n" \
    "s_waitcnt lgkmcnt(7)\n" \
    "v_mfma_f32_16x16x32_bf16 v[28:31], a[56:59], v[188:191], v[28:31]\n" \
    "ds_read_b128 v[220:223], v139 offset:61440\n" \
    "v_mfma_f32_16x16x32_bf16 v[60:63], a[120:123], v[188:191], v[60:63]\n" \
    "v_mfma_f32_16x16x32_bf16 v[92:95], a[184:187], v[188:191], v[92:95]\n" \
    "v_mfma_f32_16x16x32_bf16 v[124:127], a[248:251], v[188:191], v[124:127]\n" \
    "s_waitcnt vmcnt(16) lgkmcnt(0)\n" \
    "s_barrier\n" \
    "v_mfma_f32_16x16x32_bf16 v[0:3], a[60:63], v[192:195], v[0:3]\n" \
    "ds_read_b128 v[160:163], v132 offset:0\n" \
    "v_mfma_f32_16x16x32_bf16 v[32:35], a[124:127], v[192:195], v[32:35]\n" \
    "s_add_u32 m0, s68, 98304\n" \
    "v_mfma_f32_16x16x32_bf16 v[64:67], a[188:191], v[192:195], v[64:67]\n" \
    "global_load_lds_dwordx4 v128, s[60:61]\n" \
    "v_mfma_f32_16x16x32_bf16 v[96:99], a[252:255], v[192:195], v[96:99]\n" \
    "s_add_u32 m0, s68, 99328\n" \
    "v_mfma_f32_16x16x32_bf16 v[4:7], a[60:63], v[196:199], v[4:7]\n" \
    "ds_read_b128 v[164:167], v132 offset:4096\n" \
    "v_mfma_f32_16x16x32_bf16 v[36:39], a[124:127], v[196:199], v[36:39]\n" \
    "global_load_lds_dwordx4 v129, s[60:61]\n" \
    "v_mfma_f32_16x16x32_bf16 v[68:71], a[188:191], v[196:199], v[68:71]\n" \
    "s_add_u32 m0, s68, 100352\n" \
    "v_mfma_f32_16x16x32_bf16 v[100:103], a[252:255], v[196:199], v[100:103]\n" \
    "global_load_lds_dwordx4 v130, s[60:61]\n" \
    "v_mfma_f32_16x16x32_bf16 v[8:11], a[60:63], v[200:203], v[8:11]\n" \
    "ds_read_b128 v[168:171], v132 offset:8192\n" \
    "v_mfma_f32_16x16x32_bf16 v[40:43], a[124:127], v[200:203], v[40:43]\n" \
    "s_add_u32 m0, s68, 101376\n" \
    "v_mfma_f32_16x16x32_bf16 v[72:75], a[188:191], v[200:203], v[72:75]\n" \
    "global_load_lds_dwordx4 v131, s[60:61]\n" \
    "v_mfma_f32_16x16x32_bf16 v[104:107], a[252:255], v[200:203], v[104:107]\n" \
    "s_add_u32 m0, s68, 102400\n" \
    "v_mfma_f32_16x16x32_bf16 v[12:15], a[60:63], v[204:207], v[12:15]\n" \
    "ds_read_b128 v[172:175], v132 offset:12288\n" \
    "v_mfma_f32_16x16x32_bf16 v[44:47], a[124:127], v[204:207], v[44:47]\n" \
    "global_load_lds_dwordx4 v128, s[62:63]\n" \
    "v_mfma_f32_16x16x32_bf16 v[76:79], a[188:191], v[204:207], v[76:79]\n" \
    "s_add_u32 m0, s68, 103424\n" \
    "v_mfma_f32_16x16x32_bf16 v[108:111], a[252:255], v[204:207], v[108:111]\n" \
    "global_load_lds_dwordx4 v129, s[62:63]\n" \
    "v_mfma_f32_16x16x32_bf16 v[16:19], a[60:63], v[208:211], v[16:19]\n" \
    "ds_read_b128 v[176:179], v132 offset:16384\n" \
    "v_mfma_f32_16x16x32_bf16 v[48:51], a[124:127], v[208:211], v[48:51]\n" \
    "s_add_u32 m0, s68, 104448\n" \
    "v_mfma_f32_16x16x32_bf16 v[80:83], a[188:191], v[208:211], v[80:83]\n" \
    "global_load_lds_dwordx4 v130, s[62:63]\n" \
    "v_mfma_f32_16x16x32_bf16 v[112:115], a[252:255], v[208:211], v[112:115]\n" \
    "s_add_u32 m0, s68, 105472\n" \
    "v_mfma_f32_16x16x32_bf16 v[20:23], a[60:63], v[212:215], v[20:23]\n" \
    "ds_read_b128 v[180:183], v132 offset:20480\n" \
    "v_mfma_f32_16x16x32_bf16 v[52:55], a[124:127], v[212:215], v[52:55]\n" \
    "global_load_lds_dwordx4 v131, s[62:63]\n" \
    "v_mfma_f32_16x16x32_bf16 v[84:87], a[188:191], v[212:215], v[84:87]\n" \
    "s_add_u32 s60, s60, 256\n" \
    "v_mfma_f32_16x16x32_bf16 v[116:119], a[252:255], v[212:215], v[116:119]\n" \
    "s_addc_u32 s61, s61, 0\n" \
    "v_mfma_f32_16x16x32_bf16 v[24:27], a[60:63], v[216:219], v[24:27]\n" \
    "ds_read_b128 v[184:187], v132 offset:24576\n" \
    "v_mfma_f32_16x16x32_bf16 v[56:59], a[124:127], v[216:219], v[56:59]\n" \
    "s_add_u32 s62, s62, 256\n" \
    "v_mfma_f32_16x16x32_bf16 v[88:91], a[188:191], v[216:219], v[88:91]\n" \
    "s_addc_u32 s63, s63, 0\n" \
    "v_mfma_f32_16x16x32_bf16 v[120:123], a[252:255], v[216:219], v[120:123]\n" \
    "v_mfma_f32_16x16x32_bf16 v[28:31], a[60:63], v[220:223], v[28:31]\n" \
    "ds_read_b128 v[188:191], v132 offset:28672\n" \
    "v_mfma_f32_16x16x32_bf16 v[60:63], a[124:127], v[220:223], v[60:63]\n" \
    "v_mfma_f32_16x16x32_bf16 v[92:95], a[188:191], v[220:223], v[92:95]\n" \
    "v_mfma_f32_16x16x32_bf16 v[124:127], a[252:255], v[220:223], v[124:127]\n" \
    "s_branch EPI_%=\n" \
    "ML1_%=:\n" \
    "s_waitcnt lgkmcnt(7)\n" \
    "v_mfma_f32_16x16x32_bf16 v[0:3], v[160:163], a[0:3], 0\n" \
    "ds_read_b128 v[192:195], v133 offset:0\n" \
    "v_mfma_f32_16x16x32_bf16 v[32:35], v[160:163], a[64:67], 0\n" \
    "v_mfma_f32_16x16x32_bf16 v[64:67], v[160:163], a[128:131], 0\n" \
    "v_mfma_f32_16x16x32_bf16 v[96:99], v[160:163], a[192:195], 0\n" \
    "s_waitcnt lgkmcnt(7)\n" \
    "v_mfma_f32_16x16x32_bf16 v[4:7], v[164:167], a[0:3], 0\n" \
    "ds_read_b128 v[196:199], v133 offset:4096\n" \
    "v_mfma_f32_16x16x32_bf16 v[36:39], v[164:167], a[64:67], 0\n" \
    "v_mfma_f32_16x16x32_bf16 v[68:71], v[164:167], a[128:131], 0\n" \
    "v_mfma_f32_16x16x32_bf16 v[100:103], v[164:167], a[192:195], 0\n" \
    "s_waitcnt lgkmcnt(7)\n" \
    "v_mfma_f32_16x16x32_bf16 v[8:11], v[168:171], a[0:3], 0\n" \
    "ds_read_b128 v[200:203], v133 offset:8192\n" \
    "v_mfma_f32_16x16x32_bf16 v[40:43], v[168:171], a[64:67], 0\n" \
    "v_mfma_f32_16x16x32_bf16 v[72:75], v[168:171], a[128:131], 0\n" \
    "v_mfma_f32_16x16x32_bf16 v[104:107], v[168:171], a[192:195], 0\n" \
    "s_waitcnt lgkmcnt(7)\n" \
    "v_mfma_f32_16x16x32_bf16 v[12:15], v[172:175], a[0:3], 0\n" \
    "ds_read_b128 v[204:207], v133 offset:12288\n" \
    "v_mfma_f32_16x16x32_bf16 v[44:47], v[172:175], a[64:67], 0\n" \
    "v_mfma_f32_16x16x32_bf16 v[76:79], v[172:175], a[128:131], 0\n" \
    "v_mfma_f32_16x16x32_bf16 v[108:111], v[172:175], a[192:195], 0\n" \
    "s_waitcnt lgkmcnt(7)\n" \
    "v_mfma_f32_16x16x32_bf16 v[16:19], v[176:179], a[0:3], 0\n" \
    "ds_read_b128 v[208:211], v133 offset:16384\n" \
    "v_mfma_f32_16x16x32_bf16 v[48:51], v[176:179], a[64:67], 0\n" \
    "v_mfma_f32_16x16x32_bf16 v[80:83], v[176:179], a[128:131], 0\n" \
    "v_mfma_f32_16x16x32_bf16 v[112:115], v[176:179], a[192:195], 0\n" \
    "s_waitcnt lgkmcnt(7)\n" \
    "v_mfma_f32_16x16x32_bf16 v[20:23], v[180:183], a[0:3], 0\n" \
    "ds_read_b128 v[212:215], v133 offset:20480\n" \
    "v_mfma_f32_16x16x32_bf16 v[52:55], v[180:183], a[64:67], 0\n" \
    "v_mfma_f32_16x16x32_bf16 v[84:87], v[180:183], a[128:131], 0\n" \
    "v_mfma_f32_16x16x32_bf16 v[116:119], v[180:183], a[192:195], 0\n" \
    "s_waitcnt lgkmcnt(7)\n" \
    "v_mfma_f32_16x16x32_bf16 v[24:27], v[184:187], a[0:3], 0\n" \
    "ds_read_b128 v[216:219], v133 offset:24576\n" \
    "v_mfma_f32_16x16x32_bf16 v[56:59], v[184:187], a[64:67], 0\n" \
    "v_mfma_f32_16x16x32_bf16 v[88:91], v[184:187], a[128:131], 0\n" \
    "v_mfma_f32_16x16x32_bf16 v[120:123], v[184:187], a[192:195], 0\n" \
    "s_waitcnt lgkmcnt(7)\n" \
    "v_mfma_f32_16x16x32_bf16 v[28:31], v[188:191], a[0:3], 0\n" \
    "ds_read_b128 v[220:223], v133 offset:28672\n" \
    "v_mfma_f32_16x16x32_bf16 v[60:63], v[188:191], a[64:67], 0\n" \
    "v_mfma_f32_16x16x32_bf16 v[92:95], v[188:191], a[128:131], 0\n" \
    "v_mfma_f32_16x16x32_bf16 v[124:127], v[188:191], a[192:195], 0\n" \
    "s_waitcnt lgkmcnt(7)\n" \
    "v_mfma_f32_16x16x32_bf16 v[0:3], v[192:195], a[4:7], v[0:3]\n" \
    "ds_read_b128 v[160:163], v134 offset:0\n" \
    "v_mfma_f32_16x16x32_bf16 v[32:35], v[192:195], a[68:71], v[32:35]\n" \
    "v_mfma_f32_16x16x32_bf16 v[64:67], v[192:195], a[132:135], v[64:67]\n" \
    "v_mfma_f32_16x16x32_bf16 v[96:99], v[192:195], a[196:199], v[96:99]\n" \
    "s_waitcnt lgkmcnt(7)\n" \
    "v_mfma_f32_16x16x32_bf16 v[4:7], v[196:199], a[4:7], v[4:7]\n" \
    "ds_read_b128 v[164:167], v134 offset:4096\n" \
    "v_mfma_f32_16x16x32_bf16 v[36:39], v[196:199], a[68:71], v[36:39]\n" \
    "v_mfma_f32_16x16x32_bf16 v[68:71], v[196:199], a[132:135], v[68:71]\n" \
    "v_mfma_f32_16x16x32_bf16 v[100:103], v[196:199], a[196:199], v[100:103]\n" \
    "s_waitcnt lgkmcnt(7)\n" \
    "v_mfma_f32_16x16x32_bf16 v[8:11], v[200:203], a[4:7], v[8:11]\n" \
    "ds_read_b128 v[168:171], v134 offset:8192\n" \
    "v_mfma_f32_16x16x32_bf16 v[40:43], v[200:203], a[68:71], v[40:43]\n" \
    "v_mfma_f32_16x16x32_bf16 v[72:75], v[200:203], a[132:135], v[72:75]\n" \
    "v_mfma_f32_16x16x32_bf16 v[104:107], v[200:203], a[196:199], v[104:107]\n" \
    "s_waitcnt lgkmcnt(7)\n" \
    "v_mfma_f32_16x16x32_bf16 v[12:15], v[204:207], a[4:7], v[12:15]\n" \
    "ds_read_b128 v[172:175], v134 offset:12288\n" \
    "v_mfma_f32_16x16x32_bf16 v[44:47], v[204:207], a[68:71], v[44:47]\n" \
    "v_mfma_f32_16x16x32_bf16 v[76:79], v[204:207], a[132:135], v[76:79]\n" \
    "v_mfma_f32_16x16x32_bf16 v[108:111], v[204:207], a[196:199], v[108:111]\n" \
    "s_waitcnt lgkmcnt(7)\n" \
    "v_mfma_f32_16x16x32_bf16 v[16:19], v[208:211], a[4:7], v[16:19]\n" \
    "ds_read_b128 v[176:179], v134 offset:16384\n" \
    "v_mfma_f32_16x16x32_bf16 v[48:51], v[208:211], a[68:71], v[48:51]\n" \
    "v_mfma_f32_16x16x32_bf16 v[80:83], v[208:211], a[132:135], v[80:83]\n" \
    "v_mfma_f32_16x16x32_bf16 v[112:115], v[208:211], a[196:199], v[112:115]\n" \
    "s_waitcnt lgkmcnt(7)\n" \
    "v_mfma_f32_16x16x32_bf16 v[20:23], v[212:215], a[4:7], v[20:23]\n" \
    "ds_read_b128 v[180:183], v134 offset:20480\n" \
    "v_mfma_f32_16x16x32_bf16 v[52:55], v[212:215], a[68:71], v[52:55]\n" \
    "v_mfma_f32_16x16x32_bf16 v[84:87], v[212:215], a[132:135], v[84:87]\n" \
    "v_mfma_f32_16x16x32_bf16 v[116:119], v[212:215], a[196:199], v[116:119]\n" \
    "s_waitcnt lgkmcnt(7)\n" \
    "v_mfma_f32_16x16x32_bf16 v[24:27], v[216:219], a[4:7], v[24:27]\n" \
    "ds_read_b128 v[184:187], v134 offset:24576\n" \
    "v_mfma_f32_16x16x32_bf16 v[56:59], v[216:219], a[68:71], v[56:59]\n" \
    "v_mfma_f32_16x16x32_bf16 v[88:91], v[216:219], a[132:135], v[88:91]\n" \
    "v_mfma_f32_16x16x32_bf16 v[120:123], v[216:219], a[196:199], v[120:123]\n" \
    "s_waitcnt lgkmcnt(7)\n" \
    "v_mfma_f32_16x16x32_bf16 v[28:31], v[220:223], a[4:7], v[28:31]\n" \
    "ds_read_b128 v[188:191], v134 offset:28672\n" \
    "v_mfma_f32_16x16x32_bf16 v[60:63], v[220:223], a[68:71], v[60:63]\n" \
    "v_mfma_f32_16x16x32_bf16 v[92:95], v[220:223], a[132:135], v[92:95]\n" \
    "v_mfma_f32_16x16x32_bf16 v[124:127], v[220:223], a[196:199], v[124:127]\n" \
    "s_waitcnt lgkmcnt(7)\n" \
    "v_mfma_f32_16x16x32_bf16 v[0:3], v[160:163], a[8:11], v[0:3]\n" \
    "ds_read_b128 v[192:195], v135 offset:0\n" \
    "v_mfma_f32_16x16x32_bf16 v[32:35], v[160:163], a[72:75], v[32:35]\n" \
    "v_mfma_f32_16x16x32_bf16 v[64:67], v[160:163], a[136:139], v[64:67]\n" \
    "v_mfma_f32_16x16x32_bf16 v[96:99], v[160:163], a[200:203], v[96:99]\n" \
    "s_waitcnt lgkmcnt(7)\n" \
    "v_mfma_f32_16x16x32_bf16 v[4:7], v[164:167], a[8:11], v[4:7]\n" \
    "ds_read_b128 v[196:199], v135 offset:4096\n" \
    "v_mfma_f32_16x16x32_bf16 v[36:39], v[164:167], a[72:75], v[36:39]\n" \
    "v_mfma_f32_16x16x32_bf16 v[68:71], v[164:167], a[136:139], v[68:71]\n" \
    "v_mfma_f32_16x16x32_bf16 v[100:103], v[164:167], a[200:203], v[100:103]\n" \
    "s_waitcnt lgkmcnt(7)\n" \
    "v_mfma_f32_16x16x32_bf16 v[8:11], v[168:171], a[8:11], v[8:11]\n" \
    "ds_read_b128 v[200:203], v135 offset:8192\n" \
    "v_mfma_f32_16x16x32_bf16 v[40:43], v[168:171], a[72:75], v[40:43]\n" \
    "v_mfma_f32_16x16x32_bf16 v[72:75], v[168:171], a[136:139], v[72:75]\n" \
    "v_mfma_f32_16x16x32_bf16 v[104:107], v[168:171], a[200:203], v[104:107]\n" \
    "s_waitcnt lgkmcnt(7)\n" \
    "v_mfma_f32_16x16x32_bf16 v[12:15], v[172:175], a[8:11], v[12:15]\n" \
    "ds_read_b128 v[204:207], v135 offset:12288\n" \
    "v_mfma_f32_16x16x32_bf16 v[44:47], v[172:175], a[72:75], v[44:47]\n" \
    "v_mfma_f32_16x16x32_bf16 v[76:79], v[172:175], a[136:139], v[76:79]\n" \
    "v_mfma_f32_16x16x32_bf16 v[108:111], v[172:175], a[200:203], v[108:111]\n" \
    "s_waitcnt lgkmcnt(7)\n" \
    "v_mfma_f32_16x16x32_bf16 v[16:19], v[176:179], a[8:11], v[16:19]\n" \
    "ds_read_b128 v[208:211], v135 offset:16384\n" \
    "v_mfma_f32_16x16x32_bf16 v[48:51], v[176:179], a[72:75], v[48:51]\n" \
    "v_mfma_f32_16x16x32_bf16 v[80:83], v[176:179], a[136:139], v[80:83]\n" \
    "v_mfma_f32_16x16x32_bf16 v[112:115], v[176:179], a[200:203], v[112:115]\n" \
    "s_waitcnt lgkmcnt(7)\n" \
    "v_mfma_f32_16x16x32_bf16 v[20:23], v[180:183], a[8:11], v[20:23]\n" \
    "ds_read_b128 v[212:215], v135 offset:20480\n" \
    "v_mfma_f32_16x16x32_bf16 v[52:55], v[180:183], a[72:75], v[52:55]\n" \
    "v_mfma_f32_16x16x32_bf16 v[84:87], v[180:183], a[136:139], v[84:87]\n" \
    "v_mfma_f32_16x16x32_bf16 v[116:119], v[180:183], a[200:203], v[116:119]\n" \
    "s_waitcnt lgkmcnt(7)\n" \
    "v_mfma_f32_16x16x32_bf16 v[24:27], v[184:187], a[8:11], v[24:27]\n" \
    "ds_read_b128 v[216:219], v135 offset:24576\n" \
    "v_mfma_f32_16x16x32_bf16 v[56:59], v[184:187], a[72:75], v[56:59]\n" \
    "v_mfma_f32_16x16x32_bf16 v[88:91], v[184:187], a[136:139], v[88:91]\n" \
    "v_mfma_f32_16x16x32_bf16 v[120:123], v[184:187], a[200:203], v[120:123]\n" \
    "s_waitcnt lgkmcnt(7)\n" \
    "v_mfma_f32_16x16x32_bf16 v[28:31], v[188:191], a[8:11], v[28:31]\n" \
    "ds_read_b128 v[220:223], v135 offset:28672\n" \
    "v_mfma_f32_16x16x32_bf16 v[60:63], v[188:191], a[72:75], v[60:63]\n" \
    "v_mfma_f32_16x16x32_bf16 v[92:95], v[188:191], a[136:139], v[92:95]\n" \
    "v_mfma_f32_16x16x32_bf16 v[124:127], v[188:191], a[200:203], v[124:127]\n" \
    "s_waitcnt vmcnt(32) lgkmcnt(0)\n" \
    "s_barrier\n" \
    "v_mfma_f32_16x16x32_bf16 v[0:3], v[192:195], a[12:15], v[0:3]\n" \
    "ds_read_b128 v[160:163], v132 offset:32768\n" \
    "v_mfma_f32_16x16x32_bf16 v[32:35], v[192:195], a[76:79], v[32:35]\n" \
    "s_add_u32 m0, s68, 0\n" \
    "v_mfma_f32_16x16x32_bf16 v[64:67], v[192:195], a[140:143], v[64:67]\n" \
    "global_load_lds_dwordx4 v128, s[60:61]\n" \
    "v_mfma_f32_16x16x32_bf16 v[96:99], v[192:195], a[204:207], v[96:99]\n" \
    "s_add_u32 m0, s68, 1024\n" \
    "v_mfma_f32_16x16x32_bf16 v[4:7], v[196:199], a[12:15], v[4:7]\n" \
    "ds_read_b128 v[164:167], v132 offset:36864\n" \
    "v_mfma_f32_16x16x32_bf16 v[36:39], v[196:199], a[76:79], v[36:39]\n" \
    "global_load_lds_dwordx4 v129, s[60:61]\n" \
    "v_mfma_f32_16x16x32_bf16 v[68:71], v[196:199], a[140:143], v[68:71]\n" \
    "s_add_u32 m0, s68, 2048\n" \
    "v_mfma_f32_16x16x32_bf16 v[100:103], v[196:199], a[204:207], v[100:103]\n" \
    "global_load_lds_dwordx4 v130, s[60:61]\n" \
    "v_mfma_f32_16x16x32_bf16 v[8:11], v[200:203], a[12:15], v[8:11]\n" \
    "ds_read_b128 v[168:171], v132 offset:40960\n" \
    "v_mfma_f32_16x16x32_bf16 v[40:43], v[200:203], a[76:79], v[40:43]\n" \
    "s_add_u32 m0, s68, 3072\n" \
    "v_mfma_f32_16x16x32_bf16 v[72:75], v[200:203], a[140:143], v[72:75]\n" \
    "global_load_lds_dwordx4 v131, s[60:61]\n" \
    "v_mfma_f32_16x16x32_bf16 v[104:107], v[200:203], a[204:207], v[104:107]\n" \
    "s_add_u32 m0, s68, 4096\n" \
    "v_mfma_f32_16x16x32_bf16 v[12:15], v[204:207], a[12:15], v[12:15]\n" \
    "ds_read_b128 v[172:175], v132 offset:45056\n" \
    "v_mfma_f32_16x16x32_bf16 v[44:47], v[204:207], a[76:79], v[44:47]\n" \
    "global_load_lds_dwordx4 v128, s[62:63]\n" \
    "v_mfma_f32_16x16x32_bf16 v[76:79], v[204:207], a[140:143], v[76:79]\n" \
    "s_add_u32 m0, s68, 5120\n" \
    "v_mfma_f32_16x16x32_bf16 v[108:111], v[204:207], a[204:207], v[108:111]\n" \
    "global_load_lds_dwordx4 v129, s[62:63]\n" \
    "v_mfma_f32_16x16x32_bf16 v[16:19], v[208:211], a[12:15], v[16:19]\n" \
    "ds_read_b128 v[176:179], v132 offset:49152\n" \
    "v_mfma_f32_16x16x32_bf16 v[48:51], v[208:211], a[76:79], v[48:51]\n" \
    "s_add_u32 m0, s68, 6144\n" \
    "v_mfma_f32_16x16x32_bf16 v[80:83], v[208:211], a[140:143], v[80:83]\n" \
    "global_load_lds_dwordx4 v130, s[62:63]\n" \
    "v_mfma_f32_16x16x32_bf16 v[112:115], v[208:211], a[204:207], v[112:115]\n" \
    "s_add_u32 m0, s68, 7168\n" \
    "v_mfma_f32_16x16x32_bf16 v[20:23], v[212:215], a[12:15], v[20:23]\n" \
    "ds_read_b128 v[180:183], v132 offset:53248\n" \
    "v_mfma_f32_16x16x32_bf16 v[52:55], v[212:215], a[76:79], v[52:55]\n" \
    "global_load_lds_dwordx4 v131, s[62:63]\n" \
    "v_mfma_f32_16x16x32_bf16 v[84:87], v[212:215], a[140:143], v[84:87]\n" \
    "s_add_u32 s60, s60, 256\n" \
    "v_mfma_f32_16x16x32_bf16 v[116:119], v[212:215], a[204:207], v[116:119]\n" \
    "s_addc_u32 s61, s61, 0\n" \
    "v_mfma_f32_16x16x32_bf16 v[24:27], v[216:219], a[12:15], v[24:27]\n" \
    "ds_read_b128 v[184:187], v132 offset:57344\n" \
    "v_mfma_f32_16x16x32_bf16 v[56:59], v[216:219], a[76:79], v[56:59]\n" \
    "s_add_u32 s62, s62, 256\n" \
    "v_mfma_f32_16x16x32_bf16 v[88:91], v[216:219], a[140:143], v[88:91]\n" \
    "s_addc_u32 s63, s63, 0\n" \
    "v_mfma_f32_16x16x32_bf16 v[120:123], v[216:219], a[204:207], v[120:123]\n" \
    "v_mfma_f32_16x16x32_bf16 v[28:31], v[220:223], a[12:15], v[28:31]\n" \
    "ds_read_b128 v[188:191], v132 offset:61440\n" \
    "v_mfma_f32_16x16x32_bf16 v[60:63], v[220:223], a[76:79], v[60:63]\n" \
    "v_mfma_f32_16x16x32_bf16 v[92:95], v[220:223], a[140:143], v[92:95]\n" \
    "v_mfma_f32_16x16x32_bf16 v[124:127], v[220:223], a[204:207], v[124:127]\n" \
    "s_waitcnt lgkmcnt(7)\n" \
    "v_mfma_f32_16x16x32_bf16 v[0:3], v[160:163], a[16:19], v[0:3]\n" \
    "ds_read_b128 v[192:195], v133 offset:32768\n" \
    "v_mfma_f32_16x16x32_bf16 v[32:35], v[160:163], a[80:83], v[32:35]\n" \
    "v_mfma_f32_16x16x32_bf16 v[64:67], v[160:163], a[144:147], v[64:67]\n" \
    "v_mfma_f32_16x16x32_bf16 v[96:99], v[160:163], a[208:211], v[96:99]\n" \
    "s_waitcnt lgkmcnt(7)\n" \
    "v_mfma_f32_16x16x32_bf16 v[4:7], v[164:167], a[16:19], v[4:7]\n" \
    "ds_read_b128 v[196:199], v133 offset:36864\n" \
    "v_mfma_f32_16x16x32_bf16 v[36:39], v[164:167], a[80:83], v[36:39]\n" \
    "v_mfma_f32_16x16x32_bf16 v[68:71], v[164:167], a[144:147], v[68:71]\n" \
    "v_mfma_f32_16x16x32_bf16 v[100:103], v[164:167], a[208:211], v[100:103]\n" \
    "s_waitcnt lgkmcnt(7)\n" \
    "v_mfma_f32_16x16x32_bf16 v[8:11], v[168:171], a[16:19], v[8:11]\n" \
    "ds_read_b128 v[200:203], v133 offset:40960\n" \
    "v_mfma_f32_16x16x32_bf16 v[40:43], v[168:171], a[80:83], v[40:43]\n" \
    "v_mfma_f32_16x16x32_bf16 v[72:75], v[168:171], a[144:147], v[72:75]\n" \
    "v_mfma_f32_16x16x32_bf16 v[104:107], v[168:171], a[208:211], v[104:107]\n" \
    "s_waitcnt lgkmcnt(7)\n" \
    "v_mfma_f32_16x16x32_bf16 v[12:15], v[172:175], a[16:19], v[12:15]\n" \
    "ds_read_b128 v[204:207], v133 offset:45056\n" \
    "v_mfma_f32_16x16x32_bf16 v[44:47], v[172:175], a[80:83], v[44:47]\n" \
    "v_mfma_f32_16x16x32_bf16 v[76:79], v[172:175], a[144:147], v[76:79]\n" \
    "v_mfma_f32_16x16x32_bf16 v[108:111], v[172:175], a[208:211], v[108:111]\n" \
    "s_waitcnt lgkmcnt(7)\n" \
    "v_mfma_f32_16x16x32_bf16 v[16:19], v[176:179], a[16:19], v[16:19]\n" \
    "ds_read_b128 v[208:211], v133 offset:49152\n" \
    "v_mfma_f32_16x16x32_bf16 v[48:51], v[176:179], a[80:83], v[48:51]\n" \
    "v_mfma_f32_16x16x32_bf16 v[80:83], v[176:179], a[144:147], v[80:83]\n" \
    "v_mfma_f32_16x16x32_bf16 v[112:115], v[176:179], a[208:211], v[112:115]\n" \
    "s_waitcnt lgkmcnt(7)\n" \
    "v_mfma_f32_16x16x32_bf16 v[20:23], v[180:183], a[16:19], v[20:23]\n" \
    "ds_read_b128 v[212:215], v133 offset:53248\n" \
    "v_mfma_f32_16x16x32_bf16 v[52:55], v[180:183], a[80:83], v[52:55]\n" \
    "v_mfma_f32_16x16x32_bf16 v[84:87], v[180:183], a[144:147], v[84:87]\n" \
    "v_mfma_f32_16x16x32_bf16 v[116:119], v[180:183], a[208:211], v[116:119]\n" \
    "s_waitcnt lgkmcnt(7)\n" \
    "v_mfma_f32_16x16x32_bf16 v[24:27], v[184:187], a[16:19], v[24:27]\n" \
    "ds_read_b128 v[216:219], v133 offset:57344\n" \
    "v_mfma_f32_16x16x32_bf16 v[56:59], v[184:187], a[80:83], v[56:59]\n" \
    "v_mfma_f32_16x16x32_bf16 v[88:91], v[184:187], a[144:147], v[88:91]\n" \
    "v_mfma_f32_16x16x32_bf16 v[120:123], v[184:187], a[208:211], v[120:123]\n" \
    "s_waitcnt lgkmcnt(7)\n" \
    "v_mfma_f32_16x16x32_bf16 v[28:31], v[188:191], a[16:19], v[28:31]\n" \
    "ds_read_b128 v[220:223], v133 offset:61440\n" \
    "v_mfma_f32_16x16x32_bf16 v[60:63], v[188:191], a[80:83], v[60:63]\n" \
    "v_mfma_f32_16x16x32_bf16 v[92:95], v[188:191], a[144:147], v[92:95]\n" \
    "v_mfma_f32_16x16x32_bf16 v[124:127], v[188:191], a[208:211], v[124:127]\n" \
    "s_waitcnt lgkmcnt(7)\n" \
    "v_mfma_f32_16x16x32_bf16 v[0:3], v[192:195], a[20:23], v[0:3]\n" \
    "ds_read_b128 v[160:163], v134 offset:32768\n" \
    "v_mfma_f32_16x16x32_bf16 v[32:35], v[192:195], a[84:87], v[32:35]\n" \
    "v_mfma_f32_16x16x32_bf16 v[64:67], v[192:195], a[148:151], v[64:67]\n" \
    "v_mfma_f32_16x16x32_bf16 v[96:99], v[192:195], a[212:215], v[96:99]\n" \
    "s_waitcnt lgkmcnt(7)\n" \
    "v_mfma_f32_16x16x32_bf16 v[4:7], v[196:199], a[20:23], v[4:7]\n" \
    "ds_read_b128 v[164:167], v134 offset:36864\n" \
    "v_mfma_f32_16x16x32_bf16 v[36:39], v[196:199], a[84:87], v[36:39]\n" \
    "v_mfma_f32_16x16x32_bf16 v[68:71], v[196:199], a[148:151], v[68:71]\n" \
    "v_mfma_f32_16x16x32_bf16 v[100:103], v[196:199], a[212:215], v[100:103]\n" \
    "s_waitcnt lgkmcnt(7)\n" \
    "v_mfma_f32_16x16x32_bf16 v[8:11], v[200:203], a[20:23], v[8:11]\n" \
    "ds_read_b128 v[168:171], v134 offset:40960\n" \
    "v_mfma_f32_16x16x32_bf16 v[40:43], v[200:203], a[84:87], v[40:43]\n" \
    "v_mfma_f32_16x16x32_bf16 v[72:75], v[200:203], a[148:151], v[72:75]\n" \
    "v_mfma_f32_16x16x32_bf16 v[104:107], v[200:203], a[212:215], v[104:107]\n" \
    "s_waitcnt lgkmcnt(7)\n" \
    "v_mfma_f32_16x16x32_bf16 v[12:15], v[204:207], a[20:23], v[12:15]\n" \
    "ds_read_b128 v[172:175], v134 offset:45056\n" \
    "v_mfma_f32_16x16x32_bf16 v[44:47], v[204:207], a[84:87], v[44:47]\n" \
    "v_mfma_f32_16x16x32_bf16 v[76:79], v[204:207], a[148:151], v[76:79]\n" \
    "v_mfma_f32_16x16x32_bf16 v[108:111], v[204:207], a[212:215], v[108:111]\n" \
    "s_waitcnt lgkmcnt(7)\n" \
    "v_mfma_f32_16x16x32_bf16 v[16:19], v[208:211], a[20:23], v[16:19]\n" \
    "ds_read_b128 v[176:179], v134 offset:49152\n" \
    "v_mfma_f32_16x16x32_bf16 v[48:51], v[208:211], a[84:87], v[48:51]\n" \
    "v_mfma_f32_16x16x32_bf16 v[80:83], v[208:211], a[148:151], v[80:83]\n" \
    "v_mfma_f32_16x16x32_bf16 v[112:115], v[208:211], a[212:215], v[112:115]\n" \
    "s_waitcnt lgkmcnt(7)\n" \
    "v_mfma_f32_16x16x32_bf16 v[20:23], v[212:215], a[20:23], v[20:23]\n" \
    "ds_read_b128 v[180:183], v134 offset:53248\n" \
    "v_mfma_f32_16x16x32_bf16 v[52:55], v[212:215], a[84:87], v[52:55]\n" \
    "v_mfma_f32_16x16x32_bf16 v[84:87], v[212:215], a[148:151], v[84:87]\n" \
    "v_mfma_f32_16x16x32_bf16 v[116:119], v[212:215], a[212:215], v[116:119]\n" \
    "s_waitcnt lgkmcnt(7)\n" \
    "v_mfma_f32_16x16x32_bf16 v[24:27], v[216:219], a[20:23], v[24:27]\n" \
    "ds_read_b128 v[184:187], v134 offset:57344\n" \
    "v_mfma_f32_16x16x32_bf16 v[56:59], v[216:219], a[84:87], v[56:59]\n" \
    "v_mfma_f32_16x16x32_bf16 v[88:91], v[216:219], a[148:151], v[88:91]\n" \
    "v_mfma_f32_16x16x32_bf16 v[120:123], v[216:219], a[212:215], v[120:123]\n" \
    "s_waitcnt lgkmcnt(7)\n" \
    "v_mfma_f32_16x16x32_bf16 v[28:31], v[220:223], a[20:23], v[28:31]\n" \
    "ds_read_b128 v[188:191], v134 offset:61440\n" \
    "v_mfma_f32_16x16x32_bf16 v[60:63], v[220:223], a[84:87], v[60:63]\n" \
    "v_mfma_f32_16x16x32_bf16 v[92:95], v[220:223], a[148:151], v[92:95]\n" \
    "v_mfma_f32_16x16x32_bf16 v[124:127], v[220:223], a[212:215], v[124:127]\n" \
    "s_waitcnt lgkmcnt(7)\n" \
    "v_mfma_f32_16x16x32_bf16 v[0:3], v[160:163], a[24:27], v[0:3]\n" \
    "ds_read_b128 v[192:195], v135 offset:32768\n" \
    "v_mfma_f32_16x16x32_bf16 v[32:35], v[160:163], a[88:91], v[32:35]\n" \
    "v_mfma_f32_16x16x32_bf16 v[64:67], v[160:163], a[152:155], v[64:67]\n" \
    "v_mfma_f32_16x16x32_bf16 v[96:99], v[160:163], a[216:219], v[96:99]\n" \
    "s_waitcnt lgkmcnt(7)\n" \
    "v_mfma_f32_16x16x32_bf16 v[4:7], v[164:167], a[24:27], v[4:7]\n" \
    "ds_read_b128 v[196:199], v135 offset:36864\n" \
    "v_mfma_f32_16x16x32_bf16 v[36:39], v[164:167], a[88:91], v[36:39]\n" \
    "v_mfma_f32_16x16x32_bf16 v[68:71], v[164:167], a[152:155], v[68:71]\n" \
    "v_mfma_f32_16x16x32_bf16 v[100:103], v[164:167], a[216:219], v[100:103]\n" \
    "s_waitcnt lgkmcnt(7)\n" \
    "v_mfma_f32_16x16x32_bf16 v[8:11], v[168:171], a[24:27], v[8:11]\n" \
    "ds_read_b128 v[200:203], v135 offset:40960\n" \
    "v_mfma_f32_16x16x32_bf16 v[40:43], v[168:171], a[88:91], v[40:43]\n" \
    "v_mfma_f32_16x16x32_bf16 v[72:75], v[168:171], a[152:155], v[72:75]\n" \
    "v_mfma_f32_16x16x32_bf16 v[104:107], v[168:171], a[216:219], v[104:107]\n" \
    "s_waitcnt lgkmcnt(7)\n" \
    "v_mfma_f32_16x16x32_bf16 v[12:15], v[172:175], a[24:27], v[12:15]\n" \
    "ds_read_b128 v[204:207], v135 offset:45056\n" \
    "v_mfma_f32_16x16x32_bf16 v[44:47], v[172:175], a[88:91], v[44:47]\n" \
    "v_mfma_f32_16x16x32_bf16 v[76:79], v[172:175], a[152:155], v[76:79]\n" \
    "v_mfma_f32_16x16x32_bf16 v[108:111], v[172:175], a[216:219], v[108:111]\n" \
    "s_waitcnt lgkmcnt(7)\n" \
    "v_mfma_f32_16x16x32_bf16 v[16:19], v[176:179], a[24:27], v[16:19]\n" \
    "ds_read_b128 v[208:211], v135 offset:49152\n" \
    "v_mfma_f32_16x16x32_bf16 v[48:51], v[176:179], a[88:91], v[48:51]\n" \
    "v_mfma_f32_16x16x32_bf16 v[80:83], v[176:179], a[152:155], v[80:83]\n" \
    "v_mfma_f32_16x16x32_bf16 v[112:115], v[176:179], a[216:219], v[112:115]\n" \
    "s_waitcnt lgkmcnt(7)\n" \
    "v_mfma_f32_16x16x32_bf16 v[20:23], v[180:183], a[24:27], v[20:23]\n" \
    "ds_read_b128 v[212:215], v135 offset:53248\n" \
    "v_mfma_f32_16x16x32_bf16 v[52:55], v[180:183], a[88:91], v[52:55]\n" \
    "v_mfma_f32_16x16x32_bf16 v[84:87], v[180:183], a[152:155], v[84:87]\n" \
    "v_mfma_f32_16x16x32_bf16 v[116:119], v[180:183], a[216:219], v[116:119]\n" \
    "s_waitcnt lgkmcnt(7)\n" \
    "v_mfma_f32_16x16x32_bf16 v[24:27], v[184:187], a[24:27], v[24:27]\n" \
    "ds_read_b128 v[216:219], v135 offset:57344\n" \
    "v_mfma_f32_16x16x32_bf16 v[56:59], v[184:187], a[88:91], v[56:59]\n" \
    "v_mfma_f32_16x16x32_bf16 v[88:91], v[184:187], a[152:155], v[88:91]\n" \
    "v_mfma_f32_16x16x32_bf16 v[120:123], v[184:187], a[216:219], v[120:123]\n" \
    "s_waitcnt lgkmcnt(7)\n" \
    "v_mfma_f32_16x16x32_bf16 v[28:31], v[188:191], a[24:27], v[28:31]\n" \
    "ds_read_b128 v[220:223], v135 offset:61440\n" \
    "v_mfma_f32_16x16x32_bf16 v[60:63], v[188:191], a[88:91], v[60:63]\n" \
    "v_mfma_f32_16x16x32_bf16 v[92:95], v[188:191], a[152:155], v[92:95]\n" \
    "v_mfma_f32_16x16x32_bf16 v[124:127], v[188:191], a[216:219], v[124:127]\n" \
    "s_waitcnt vmcnt(8) lgkmcnt(0)\n" \
    "s_barrier\n" \
    "s_cmp_eq_u32 s77, 0\n" \
    "s_cbranch_scc1 NF1_%=\n" \
    "v_mov_b32 v141, 0\n" \
    "s_mov_b64 exec, 1\n" \
    "global_store_dword v141, v141, s[72:73] sc1\n" \
    "s_mov_b64 exec, -1\n" \
    "NF1_%=:\n" \
    "v_mfma_f32_16x16x32_bf16 v[0:3], v[192:195], a[28:31], v[0:3]\n" \
    "ds_read_b128 v[160:163], v136 offset:0\n" \
    "v_mfma_f32_16x16x32_bf16 v[32:35], v[192:195], a[92:95], v[32:35]\n" \
    "s_add_u32 m0, s68, 32768\n" \
    "v_mfma_f32_16x16x32_bf16 v[64:67], v[192:195], a[156:159], v[64:67]\n" \
    "global_load_lds_dwordx4 v128, s[60:61]\n" \
    "v_mfma_f32_16x16x32_bf16 v[96:99], v[192:195], a[220:223], v[96:99]\n" \
    "s_add_u32 m0, s68, 33792\n" \
    "v_mfma_f32_16x16x32_bf16 v[4:7], v[196:199], a[28:31], v[4:7]\n" \
    "ds_read_b128 v[164:167], v136 offset:4096\n" \
    "v_mfma_f32_16x16x32_bf16 v[36:39], v[196:199], a[92:95], v[36:39]\n" \
    "global_load_lds_dwordx4 v129, s[60:61]\n" \
    "v_mfma_f32_16x16x32_bf16 v[68:71], v[196:199], a[156:159], v[68:71]\n" \
    "s_add_u32 m0, s68, 34816\n" \
    "v_mfma_f32_16x16x32_bf16 v[100:103], v[196:199], a[220:223], v[100:103]\n" \
    "global_load_lds_dwordx4 v130, s[60:61]\n" \
    "v_mfma_f32_16x16x32_bf16 v[8:11], v[200:203], a[28:31], v[8:11]\n" \
    "ds_read_b128 v[168:171], v136 offset:8192\n" \
    "v_mfma_f32_16x16x32_bf16 v[40:43], v[200:203], a[92:95], v[40:43]\n" \
    "s_add_u32 m0, s68, 35840\n" \
    "v_mfma_f32_16x16x32_bf16 v[72:75], v[200:203], a[156:159], v[72:75]\n" \
    "global_load_lds_dwordx4 v131, s[60:61]\n" \
    "v_mfma_f32_16x16x32_bf16 v[104:107], v[200:203], a[220:223], v[104:107]\n" \
    "s_add_u32 m0, s68, 36864\n" \
    "v_mfma_f32_16x16x32_bf16 v[12:15], v[204:207], a[28:31], v[12:15]\n" \
    "ds_read_b128 v[172:175], v136 offset:12288\n" \
    "v_mfma_f32_16x16x32_bf16 v[44:47], v[204:207], a[92:95], v[44:47]\n" \
    "global_load_lds_dwordx4 v128, s[62:63]\n" \
    "v_mfma_f32_16x16x32_bf16 v[76:79], v[204:207], a[156:159], v[76:79]\n" \
    "s_add_u32 m0, s68, 37888\n" \
    "v_mfma_f32_16x16x32_bf16 v[108:111], v[204:207], a[220:223], v[108:111]\n" \
    "global_load_lds_dwordx4 v129, s[62:63]\n" \
    "v_mfma_f32_16x16x32_bf16 v[16:19], v[208:211], a[28:31], v[16:19]\n" \
    "ds_read_b128 v[176:179], v136 offset:16384\n" \
    "v_mfma_f32_16x16x32_bf16 v[48:51], v[208:211], a[92:95], v[48:51]\n" \
    "s_add_u32 m0, s68, 38912\n" \
    "v_mfma_f32_16x16x32_bf16 v[80:83], v[208:211], a[156:159], v[80:83]\n" \
    "global_load_lds_dwordx4 v130, s[62:63]\n" \
    "v_mfma_f32_16x16x32_bf16 v[112:115], v[208:211], a[220:223], v[112:115]\n" \
    "s_add_u32 m0, s68, 39936\n" \
    "v_mfma_f32_16x16x32_bf16 v[20:23], v[212:215], a[28:31], v[20:23]\n" \
    "ds_read_b128 v[180:183], v136 offset:20480\n" \
    "v_mfma_f32_16x16x32_bf16 v[52:55], v[212:215], a[92:95], v[52:55]\n" \
    "global_load_lds_dwordx4 v131, s[62:63]\n" \
    "v_mfma_f32_16x16x32_bf16 v[84:87], v[212:215], a[156:159], v[84:87]\n" \
    "s_add_u32 s60, s60, 256\n" \
    "v_mfma_f32_16x16x32_bf16 v[116:119], v[212:215], a[220:223], v[116:119]\n" \
    "s_addc_u32 s61, s61, 0\n" \
    "v_mfma_f32_16x16x32_bf16 v[24:27], v[216:219], a[28:31], v[24:27]\n" \
    "ds_read_b128 v[184:187], v136 offset:24576\n" \
    "v_mfma_f32_16x16x32_bf16 v[56:59], v[216:219], a[92:95], v[56:59]\n" \
    "s_add_u32 s62, s62, 256\n" \
    "v_mfma_f32_16x16x32_bf16 v[88:91], v[216:219], a[156:159], v[88:91]\n" \
    "s_addc_u32 s63, s63, 0\n" \
    "v_mfma_f32_16x16x32_bf16 v[120:123], v[216:219], a[220:223], v[120:123]\n" \
    "v_mfma_f32_16x16x32_bf16 v[28:31], v[220:223], a[28:31], v[28:31]\n" \
    "ds_read_b128 v[188:191], v136 offset:28672\n" \
    "v_mfma_f32_16x16x32_bf16 v[60:63], v[220:223], a[92:95], v[60:63]\n" \
    "v_mfma_f32_16x16x32_bf16 v[92:95], v[220:223], a[156:159], v[92:95]\n" \
    "v_mfma_f32_16x16x32_bf16 v[124:127], v[220:223], a[220:223], v[124:127]\n" \
    "s_waitcnt lgkmcnt(7)\n" \
    "v_mfma_f32_16x16x32_bf16 v[0:3], v[160:163], a[32:35], v[0:3]\n" \
    "ds_read_b128 v[192:195], v137 offset:0\n" \
    "v_mfma_f32_16x16x32_bf16 v[32:35], v[160:163], a[96:99], v[32:35]\n" \
    "v_mfma_f32_16x16x32_bf16 v[64:67], v[160:163], a[160:163], v[64:67]\n" \
    "v_mfma_f32_16x16x32_bf16 v[96:99], v[160:163], a[224:227], v[96:99]\n" \
    "s_waitcnt lgkmcnt(7)\n" \
    "v_mfma_f32_16x16x32_bf16 v[4:7], v[164:167], a[32:35], v[4:7]\n" \
    "ds_read_b128 v[196:199], v137 offset:4096\n" \
    "v_mfma_f32_16x16x32_bf16 v[36:39], v[164:167], a[96:99], v[36:39]\n" \
    "v_mfma_f32_16x16x32_bf16 v[68:71], v[164:167], a[160:163], v[68:71]\n" \
    "v_mfma_f32_16x16x32_bf16 v[100:103], v[164:167], a[224:227], v[100:103]\n" \
    "s_waitcnt lgkmcnt(7)\n" \
    "v_mfma_f32_16x16x32_bf16 v[8:11], v[168:171], a[32:35], v[8:11]\n" \
    "ds_read_b128 v[200:203], v137 offset:8192\n" \
    "v_mfma_f32_16x16x32_bf16 v[40:43], v[168:171], a[96:99], v[40:43]\n" \
    "v_mfma_f32_16x16x32_bf16 v[72:75], v[168:171], a[160:163], v[72:75]\n" \
    "v_mfma_f32_16x16x32_bf16 v[104:107], v[168:171], a[224:227], v[104:107]\n" \
    "s_waitcnt lgkmcnt(7)\n" \
    "v_mfma_f32_16x16x32_bf16 v[12:15], v[172:175], a[32:35], v[12:15]\n" \
    "ds_read_b128 v[204:207], v137 offset:12288\n" \
    "v_mfma_f32_16x16x32_bf16 v[44:47], v[172:175], a[96:99], v[44:47]\n" \
    "v_mfma_f32_16x16x32_bf16 v[76:79], v[172:175], a[160:163], v[76:79]\n" \
    "v_mfma_f32_16x16x32_bf16 v[108:111], v[172:175], a[224:227], v[108:111]\n" \
    "s_waitcnt lgkmcnt(7)\n" \
    "v_mfma_f32_16x16x32_bf16 v[16:19], v[176:179], a[32:35], v[16:19]\n" \
    "ds_read_b128 v[208:211], v137 offset:16384\n" \
    "v_mfma_f32_16x16x32_bf16 v[48:51], v[176:179], a[96:99], v[48:51]\n" \
    "v_mfma_f32_16x16x32_bf16 v[80:83], v[176:179], a[160:163], v[80:83]\n" \
    "v_mfma_f32_16x16x32_bf16 v[112:115], v[176:179], a[224:227], v[112:115]\n" \
    "s_waitcnt lgkmcnt(7)\n" \
    "v_mfma_f32_16x16x32_bf16 v[20:23], v[180:183], a[32:35], v[20:23]\n" \
    "ds_read_b128 v[212:215], v137 offset:20480\n" \
    "v_mfma_f32_16x16x32_bf16 v[52:55], v[180:183], a[96:99], v[52:55]\n" \
    "v_mfma_f32_16x16x32_bf16 v[84:87], v[180:183], a[160:163], v[84:87]\n" \
    "v_mfma_f32_16x16x32_bf16 v[116:119], v[180:183], a[224:227], v[116:119]\n" \
    "s_waitcnt lgkmcnt(7)\n" \
    "v_mfma_f32_16x16x32_bf16 v[24:27], v[184:187], a[32:35], v[24:27]\n" \
    "ds_read_b128 v[216:219], v137 offset:24576\n" \
    "v_mfma_f32_16x16x32_bf16 v[56:59], v[184:187], a[96:99], v[56:59]\n" \
    "v_mfma_f32_16x16x32_bf16 v[88:91], v[184:187], a[160:163], v[88:91]\n" \
    "v_mfma_f32_16x16x32_bf16 v[120:123], v[184:187], a[224:227], v[120:123]\n" \
    "s_waitcnt lgkmcnt(7)\n" \
    "v_mfma_f32_16x16x32_bf16 v[28:31], v[188:191], a[32:35], v[28:31]\n" \
    "ds_read_b128 v[220:223], v137 offset:28672\n" \
    "v_mfma_f32_16x16x32_bf16 v[60:63], v[188:191], a[96:99], v[60:63]\n" \
    "v_mfma_f32_16x16x32_bf16 v[92:95], v[188:191], a[160:163], v[92:95]\n" \
    "v_mfma_f32_16x16x32_bf16 v[124:127], v[188:191], a[224:227], v[124:127]\n" \
    "s_waitcnt lgkmcnt(7)\n" \
    "v_mfma_f32_16x16x32_bf16 v[0:3], v[192:195], a[36:39], v[0:3]\n" \
    "ds_read_b128 v[160:163], v138 offset:0\n" \
    "v_mfma_f32_16x16x32_bf16 v[32:35], v[192:195], a[100:103], v[32:35]\n" \
    "v_mfma_f32_16x16x32_bf16 v[64:67], v[192:195], a[164:167], v[64:67]\n" \
    "v_mfma_f32_16x16x32_bf16 v[96:99], v[192:195], a[228:231], v[96:99]\n" \
    "s_waitcnt lgkmcnt(7)\n" \
    "v_mfma_f32_16x16x32_bf16 v[4:7], v[196:199], a[36:39], v[4:7]\n" \
    "ds_read_b128 v[164:167], v138 offset:4096\n" \
    "v_mfma_f32_16x16x32_bf16 v[36:39], v[196:199], a[100:103], v[36:39]\n" \
    "v_mfma_f32_16x16x32_bf16 v[68:71], v[196:199], a[164:167], v[68:71]\n" \
    "v_mfma_f32_16x16x32_bf16 v[100:103], v[196:199], a[228:231], v[100:103]\n" \
    "s_waitcnt lgkmcnt(7)\n" \
    "v_mfma_f32_16x16x32_bf16 v[8:11], v[200:203], a[36:39], v[8:11]\n" \
    "ds_read_b128 v[168:171], v138 offset:8192\n" \
    "v_mfma_f32_16x16x32_bf16 v[40:43], v[200:203], a[100:103], v[40:43]\n" \
    "v_mfma_f32_16x16x32_bf16 v[72:75], v[200:203], a[164:167], v[72:75]\n" \
    "v_mfma_f32_16x16x32_bf16 v[104:107], v[200:203], a[228:231], v[104:107]\n" \
    "s_waitcnt lgkmcnt(7)\n" \
    "v_mfma_f32_16x16x32_bf16 v[12:15], v[204:207], a[36:39], v[12:15]\n" \
    "ds_read_b128 v[172:175], v138 offset:12288\n" \
    "v_mfma_f32_16x16x32_bf16 v[44:47], v[204:207], a[100:103], v[44:47]\n" \
    "v_mfma_f32_16x16x32_bf16 v[76:79], v[204:207], a[164:167], v[76:79]\n" \
    "v_mfma_f32_16x16x32_bf16 v[108:111], v[204:207], a[228:231], v[108:111]\n" \
    "s_waitcnt lgkmcnt(7)\n" \
    "v_mfma_f32_16x16x32_bf16 v[16:19], v[208:211], a[36:39], v[16:19]\n" \
    "ds_read_b128 v[176:179], v138 offset:16384\n" \
    "v_mfma_f32_16x16x32_bf16 v[48:51], v[208:211], a[100:103], v[48:51]\n" \
    "v_mfma_f32_16x16x32_bf16 v[80:83], v[208:211], a[164:167], v[80:83]\n" \
    "v_mfma_f32_16x16x32_bf16 v[112:115], v[208:211], a[228:231], v[112:115]\n" \
    "s_waitcnt lgkmcnt(7)\n" \
    "v_mfma_f32_16x16x32_bf16 v[20:23], v[212:215], a[36:39], v[20:23]\n" \
    "ds_read_b128 v[180:183], v138 offset:20480\n" \
    "v_mfma_f32_16x16x32_bf16 v[52:55], v[212:215], a[100:103], v[52:55]\n" \
    "v_mfma_f32_16x16x32_bf16 v[84:87], v[212:215], a[164:167], v[84:87]\n" \
    "v_mfma_f32_16x16x32_bf16 v[116:119], v[212:215], a[228:231], v[116:119]\n" \
    "s_waitcnt lgkmcnt(7)\n" \
    "v_mfma_f32_16x16x32_bf16 v[24:27], v[216:219], a[36:39], v[24:27]\n" \
    "ds_read_b128 v[184:187], v138 offset:24576\n" \
    "v_mfma_f32_16x16x32_bf16 v[56:59], v[216:219], a[100:103], v[56:59]\n" \
    "v_mfma_f32_16x16x32_bf16 v[88:91], v[216:219], a[164:167], v[88:91]\n" \
    "v_mfma_f32_16x16x32_bf16 v[120:123], v[216:219], a[228:231], v[120:123]\n" \
    "s_waitcnt lgkmcnt(7)\n" \
    "v_mfma_f32_16x16x32_bf16 v[28:31], v[220:223], a[36:39], v[28:31]\n" \
    "ds_read_b128 v[188:191], v138 offset:28672\n" \
    "v_mfma_f32_16x16x32_bf16 v[60:63], v[220:223], a[100:103], v[60:63]\n" \
    "v_mfma_f32_16x16x32_bf16 v[92:95], v[220:223], a[164:167], v[92:95]\n" \
    "v_mfma_f32_16x16x32_bf16 v[124:127], v[220:223], a[228:231], v[124:127]\n" \
    "s_waitcnt lgkmcnt(7)\n" \
    "v_mfma_f32_16x16x32_bf16 v[0:3], v[160:163], a[40:43], v[0:3]\n" \
    "ds_read_b128 v[192:195], v139 offset:0\n" \
    "v_mfma_f32_16x16x32_bf16 v[32:35], v[160:163], a[104:107], v[32:35]\n" \
    "v_mfma_f32_16x16x32_bf16 v[64:67], v[160:163], a[168:171], v[64:67]\n" \
    "v_mfma_f32_16x16x32_bf16 v[96:99], v[160:163], a[232:235], v[96:99]\n" \
    "s_waitcnt lgkmcnt(7)\n" \
    "v_mfma_f32_16x16x32_bf16 v[4:7], v[164:167], a[40:43], v[4:7]\n" \
    "ds_read_b128 v[196:199], v139 offset:4096\n" \
    "v_mfma_f32_16x16x32_bf16 v[36:39], v[164:167], a[104:107], v[36:39]\n" \
    "v_mfma_f32_16x16x32_bf16 v[68:71], v[164:167], a[168:171], v[68:71]\n" \
    "v_mfma_f32_16x16x32_bf16 v[100:103], v[164:167], a[232:235], v[100:103]\n" \
    "s_waitcnt lgkmcnt(7)\n" \
    "v_mfma_f32_16x16x32_bf16 v[8:11], v[168:171], a[40:43], v[8:11]\n" \
    "ds_read_b128 v[200:203], v139 offset:8192\n" \
    "v_mfma_f32_16x16x32_bf16 v[40:43], v[168:171], a[104:107], v[40:43]\n" \
    "v_mfma_f32_16x16x32_bf16 v[72:75], v[168:171], a[168:171], v[72:75]\n" \
    "v_mfma_f32_16x16x32_bf16 v[104:107], v[168:171], a[232:235], v[104:107]\n" \
    "s_waitcnt lgkmcnt(7)\n" \
    "v_mfma_f32_16x16x32_bf16 v[12:15], v[172:175], a[40:43], v[12:15]\n" \
    "ds_read_b128 v[204:207], v139 offset:12288\n" \
    "v_mfma_f32_16x16x32_bf16 v[44:47], v[172:175], a[104:107], v[44:47]\n" \
    "v_mfma_f32_16x16x32_bf16 v[76:79], v[172:175], a[168:171], v[76:79]\n" \
    "v_mfma_f32_16x16x32_bf16 v[108:111], v[172:175], a[232:235], v[108:111]\n" \
    "s_waitcnt lgkmcnt(7)\n" \
    "v_mfma_f32_16x16x32_bf16 v[16:19], v[176:179], a[40:43], v[16:19]\n" \
    "ds_read_b128 v[208:211], v139 offset:16384\n" \
    "v_mfma_f32_16x16x32_bf16 v[48:51], v[176:179], a[104:107], v[48:51]\n" \
    "v_mfma_f32_16x16x32_bf16 v[80:83], v[176:179], a[168:171], v[80:83]\n" \
    "v_mfma_f32_16x16x32_bf16 v[112:115], v[176:179], a[232:235], v[112:115]\n" \
    "s_waitcnt lgkmcnt(7)\n" \
    "v_mfma_f32_16x16x32_bf16 v[20:23], v[180:183], a[40:43], v[20:23]\n" \
    "ds_read_b128 v[212:215], v139 offset:20480\n" \
    "v_mfma_f32_16x16x32_bf16 v[52:55], v[180:183], a[104:107], v[52:55]\n" \
    "v_mfma_f32_16x16x32_bf16 v[84:87], v[180:183], a[168:171], v[84:87]\n" \
    "v_mfma_f32_16x16x32_bf16 v[116:119], v[180:183], a[232:235], v[116:119]\n" \
    "s_waitcnt lgkmcnt(7)\n" \
    "v_mfma_f32_16x16x32_bf16 v[24:27], v[184:187], a[40:43], v[24:27]\n" \
    "ds_read_b128 v[216:219], v139 offset:24576\n" \
    "v_mfma_f32_16x16x32_bf16 v[56:59], v[184:187], a[104:107], v[56:59]\n" \
    "v_mfma_f32_16x16x32_bf16 v[88:91], v[184:187], a[168:171], v[88:91]\n" \
    "v_mfma_f32_16x16x32_bf16 v[120:123], v[184:187], a[232:235], v[120:123]\n" \
    "s_waitcnt lgkmcnt(7)\n" \
    "v_mfma_f32_16x16x32_bf16 v[28:31], v[188:191], a[40:43], v[28:31]\n" \
    "ds_read_b128 v[220:223], v139 offset:28672\n" \
    "v_mfma_f32_16x16x32_bf16 v[60:63], v[188:191], a[104:107], v[60:63]\n" \
    "v_mfma_f32_16x16x32_bf16 v[92:95], v[188:191], a[168:171], v[92:95]\n" \
    "v_mfma_f32_16x16x32_bf16 v[124:127], v[188:191], a[232:235], v[124:127]\n" \
    "s_waitcnt vmcnt(16) lgkmcnt(0)\n" \
    "s_barrier\n" \
    "v_mfma_f32_16x16x32_bf16 v[0:3], v[192:195], a[44:47], v[0:3]\n" \
    "ds_read_b128 v[160:163], v136 offset:32768\n" \
    "v_mfma_f32_16x16x32_bf16 v[32:35], v[192:195], a[108:111], v[32:35]\n" \
    "s_add_u32 m0, s68, 65536\n" \
    "v_mfma_f32_16x16x32_bf16 v[64:67], v[192:195], a[172:175], v[64:67]\n" \
    "global_load_lds_dwordx4 v128, s[60:61]\n" \
    "v_mfma_f32_16x16x32_bf16 v[96:99], v[192:195], a[236:239], v[96:99]\n" \
    "s_add_u32 m0, s68, 66560\n" \
    "v_mfma_f32_16x16x32_bf16 v[4:7], v[196:199], a[44:47], v[4:7]\n" \
    "ds_read_b128 v[164:167], v136 offset:36864\n" \
    "v_mfma_f32_16x16x32_bf16 v[36:39], v[196:199], a[108:111], v[36:39]\n" \
    "global_load_lds_dwordx4 v129, s[60:61]\n" \
    "v_mfma_f32_16x16x32_bf16 v[68:71], v[196:199], a[172:175], v[68:71]\n" \
    "s_add_u32 m0, s68, 67584\n" \
    "v_mfma_f32_16x16x32_bf16 v[100:103], v[196:199], a[236:239], v[100:103]\n" \
    "global_load_lds_dwordx4 v130, s[60:61]\n" \
    "v_mfma_f32_16x16x32_bf16 v[8:11], v[200:203], a[44:47], v[8:11]\n" \
    "ds_read_b128 v[168:171], v136 offset:40960\n" \
    "v_mfma_f32_16x16x32_bf16 v[40:43], v[200:203], a[108:111], v[40:43]\n" \
    "s_add_u32 m0, s68, 68608\n" \
    "v_mfma_f32_16x16x32_bf16 v[72:75], v[200:203], a[172:175], v[72:75]\n" \
    "global_load_lds_dwordx4 v131, s[60:61]\n" \
    "v_mfma_f32_16x16x32_bf16 v[104:107], v[200:203], a[236:239], v[104:107]\n" \
    "s_add_u32 m0, s68, 69632\n" \
    "v_mfma_f32_16x16x32_bf16 v[12:15], v[204:207], a[44:47], v[12:15]\n" \
    "ds_read_b128 v[172:175], v136 offset:45056\n" \
    "v_mfma_f32_16x16x32_bf16 v[44:47], v[204:207], a[108:111], v[44:47]\n" \
    "global_load_lds_dwordx4 v128, s[62:63]\n" \
    "v_mfma_f32_16x16x32_bf16 v[76:79], v[204:207], a[172:175], v[76:79]\n" \
    "s_add_u32 m0, s68, 70656\n" \
    "v_mfma_f32_16x16x32_bf16 v[108:111], v[204:207], a[236:239], v[108:111]\n" \
    "global_load_lds_dwordx4 v129, s[62:63]\n" \
    "v_mfma_f32_16x16x32_bf16 v[16:19], v[208:211], a[44:47], v[16:19]\n" \
    "ds_read_b128 v[176:179], v136 offset:49152\n" \
    "v_mfma_f32_16x16x32_bf16 v[48:51], v[208:211], a[108:111], v[48:51]\n" \
    "s_add_u32 m0, s68, 71680\n" \
    "v_mfma_f32_16x16x32_bf16 v[80:83], v[208:211], a[172:175], v[80:83]\n" \
    "global_load_lds_dwordx4 v130, s[62:63]\n" \
    "v_mfma_f32_16x16x32_bf16 v[112:115], v[208:211], a[236:239], v[112:115]\n" \
    "s_add_u32 m0, s68, 72704\n" \
    "v_mfma_f32_16x16x32_bf16 v[20:23], v[212:215], a[44:47], v[20:23]\n" \
    "ds_read_b128 v[180:183], v136 offset:53248\n" \
    "v_mfma_f32_16x16x32_bf16 v[52:55], v[212:215], a[108:111], v[52:55]\n" \
    "global_load_lds_dwordx4 v131, s[62:63]\n" \
    "v_mfma_f32_16x16x32_bf16 v[84:87], v[212:215], a[172:175], v[84:87]\n" \
    "s_add_u32 s60, s60, 256\n" \
    "v_mfma_f32_16x16x32_bf16 v[116:119], v[212:215], a[236:239], v[116:119]\n" \
    "s_addc_u32 s61, s61, 0\n" \
    "v_mfma_f32_16x16x32_bf16 v[24:27], v[216:219], a[44:47], v[24:27]\n" \
    "ds_read_b128 v[184:187], v136 offset:57344\n" \
    "v_mfma_f32_16x16x32_bf16 v[56:59], v[216:219], a[108:111], v[56:59]\n" \
    "s_add_u32 s62, s62, 256\n" \
    "v_mfma_f32_16x16x32_bf16 v[88:91], v[216:219], a[172:175], v[88:91]\n" \
    "s_addc_u32 s63, s63, 0\n" \
    "v_mfma_f32_16x16x32_bf16 v[120:123], v[216:219], a[236:239], v[120:123]\n" \
    "v_mfma_f32_16x16x32_bf16 v[28:31], v[220:223], a[44:47], v[28:31]\n" \
    "ds_read_b128 v[188:191], v136 offset:61440\n" \
    "v_mfma_f32_16x16x32_bf16 v[60:63], v[220:223], a[108:111], v[60:63]\n" \
    "v_mfma_f32_16x16x32_bf16 v[92:95], v[220:223], a[172:175], v[92:95]\n" \
    "v_mfma_f32_16x16x32_bf16 v[124:127], v[220:223], a[236:239], v[124:127]\n" \
    "s_waitcnt lgkmcnt(7)\n" \
    "v_mfma_f32_16x16x32_bf16 v[0:3], v[160:163], a[48:51], v[0:3]\n" \
    "ds_read_b128 v[192:195], v137 offset:32768\n" \
    "v_mfma_f32_16x16x32_bf16 v[32:35], v[160:163], a[112:115], v[32:35]\n" \
    "v_mfma_f32_16x16x32_bf16 v[64:67], v[160:163], a[176:179], v[64:67]\n" \
    "v_mfma_f32_16x16x32_bf16 v[96:99], v[160:163], a[240:243], v[96:99]\n" \
    "s_waitcnt lgkmcnt(7)\n" \
    "v_mfma_f32_16x16x32_bf16 v[4:7], v[164:167], a[48:51], v[4:7]\n" \
    "ds_read_b128 v[196:199], v137 offset:36864\n" \
    "v_mfma_f32_16x16x32_bf16 v[36:39], v[164:167], a[112:115], v[36:39]\n" \
    "v_mfma_f32_16x16x32_bf16 v[68:71], v[164:167], a[176:179], v[68:71]\n" \
    "v_mfma_f32_16x16x32_bf16 v[100:103], v[164:167], a[240:243], v[100:103]\n" \
    "s_waitcnt lgkmcnt(7)\n" \
    "v_mfma_f32_16x16x32_bf16 v[8:11], v[168:171], a[48:51], v[8:11]\n" \
    "ds_read_b128 v[200:203], v137 offset:40960\n" \
    "v_mfma_f32_16x16x32_bf16 v[40:43], v[168:171], a[112:115], v[40:43]\n" \
    "v_mfma_f32_16x16x32_bf16 v[72:75], v[168:171], a[176:179], v[72:75]\n" \
    "v_mfma_f32_16x16x32_bf16 v[104:107], v[168:171], a[240:243], v[104:107]\n" \
    "s_waitcnt lgkmcnt(7)\n" \
    "v_mfma_f32_16x16x32_bf16 v[12:15], v[172:175], a[48:51], v[12:15]\n" \
    "ds_read_b128 v[204:207], v137 offset:45056\n" \
    "v_mfma_f32_16x16x32_bf16 v[44:47], v[172:175], a[112:115], v[44:47]\n" \
    "v_mfma_f32_16x16x32_bf16 v[76:79], v[172:175], a[176:179], v[76:79]\n" \
    "v_mfma_f32_16x16x32_bf16 v[108:111], v[172:175], a[240:243], v[108:111]\n" \
    "s_waitcnt lgkmcnt(7)\n" \
    "v_mfma_f32_16x16x32_bf16 v[16:19], v[176:179], a[48:51], v[16:19]\n" \
    "ds_read_b128 v[208:211], v137 offset:49152\n" \
    "v_mfma_f32_16x16x32_bf16 v[48:51], v[176:179], a[112:115], v[48:51]\n" \
    "v_mfma_f32_16x16x32_bf16 v[80:83], v[176:179], a[176:179], v[80:83]\n" \
    "v_mfma_f32_16x16x32_bf16 v[112:115], v[176:179], a[240:243], v[112:115]\n" \
    "s_waitcnt lgkmcnt(7)\n" \
    "v_mfma_f32_16x16x32_bf16 v[20:23], v[180:183], a[48:51], v[20:23]\n" \
    "ds_read_b128 v[212:215], v137 offset:53248\n" \
    "v_mfma_f32_16x16x32_bf16 v[52:55], v[180:183], a[112:115], v[52:55]\n" \
    "v_mfma_f32_16x16x32_bf16 v[84:87], v[180:183], a[176:179], v[84:87]\n" \
    "v_mfma_f32_16x16x32_bf16 v[116:119], v[180:183], a[240:243], v[116:119]\n" \
    "s_waitcnt lgkmcnt(7)\n" \
    "v_mfma_f32_16x16x32_bf16 v[24:27], v[184:187], a[48:51], v[24:27]\n" \
    "ds_read_b128 v[216:219], v137 offset:57344\n" \
    "v_mfma_f32_16x16x32_bf16 v[56:59], v[184:187], a[112:115], v[56:59]\n" \
    "v_mfma_f32_16x16x32_bf16 v[88:91], v[184:187], a[176:179], v[88:91]\n" \
    "v_mfma_f32_16x16x32_bf16 v[120:123], v[184:187], a[240:243], v[120:123]\n" \
    "s_waitcnt lgkmcnt(7)\n" \
    "v_mfma_f32_16x16x32_bf16 v[28:31], v[188:191], a[48:51], v[28:31]\n" \
    "ds_read_b128 v[220:223], v137 offset:61440\n" \
    "v_mfma_f32_16x16x32_bf16 v[60:63], v[188:191], a[112:115], v[60:63]\n" \
    "v_mfma_f32_16x16x32_bf16 v[92:95], v[188:191], a[176:179], v[92:95]\n" \
    "v_mfma_f32_16x16x32_bf16 v[124:127], v[188:191], a[240:243], v[124:127]\n" \
    "s_waitcnt lgkmcnt(7)\n" \
    "v_mfma_f32_16x16x32_bf16 v[0:3], v[192:195], a[52:55], v[0:3]\n" \
    "ds_read_b128 v[160:163], v138 offset:32768\n" \
    "v_mfma_f32_16x16x32_bf16 v[32:35], v[192:195], a[116:119], v[32:35]\n" \
    "v_mfma_f32_16x16x32_bf16 v[64:67], v[192:195], a[180:183], v[64:67]\n" \
    "v_mfma_f32_16x16x32_bf16 v[96:99], v[192:195], a[244:247], v[96:99]\n" \
    "s_waitcnt lgkmcnt(7)\n" \
    "v_mfma_f32_16x16x32_bf16 v[4:7], v[196:199], a[52:55], v[4:7]\n" \
    "ds_read_b128 v[164:167], v138 offset:36864\n" \
    "v_mfma_f32_16x16x32_bf16 v[36:39], v[196:199], a[116:119], v[36:39]\n" \
    "v_mfma_f32_16x16x32_bf16 v[68:71], v[196:199], a[180:183], v[68:71]\n" \
    "v_mfma_f32_16x16x32_bf16 v[100:103], v[196:199], a[244:247], v[100:103]\n" \
    "s_waitcnt lgkmcnt(7)\n" \
    "v_mfma_f32_16x16x32_bf16 v[8:11], v[200:203], a[52:55], v[8:11]\n" \
    "ds_read_b128 v[168:171], v138 offset:40960\n" \
    "v_mfma_f32_16x16x32_bf16 v[40:43], v[200:203], a[116:119], v[40:43]\n" \
    "v_mfma_f32_16x16x32_bf16 v[72:75], v[200:203], a[180:183], v[72:75]\n" \
    "v_mfma_f32_16x16x32_bf16 v[104:107], v[200:203], a[244:247], v[104:107]\n" \
    "s_waitcnt lgkmcnt(7)\n" \
    "v_mfma_f32_16x16x32_bf16 v[12:15], v[204:207], a[52:55], v[12:15]\n" \
    "ds_read_b128 v[172:175], v138 offset:45056\n" \
    "v_mfma_f32_16x16x32_bf16 v[44:47], v[204:207], a[116:119], v[44:47]\n" \
    "v_mfma_f32_16x16x32_bf16 v[76:79], v[204:207], a[180:183], v[76:79]\n" \
    "v_mfma_f32_16x16x32_bf16 v[108:111], v[204:207], a[244:247], v[108:111]\n" \
    "s_waitcnt lgkmcnt(7)\n" \
    "v_mfma_f32_16x16x32_bf16 v[16:19], v[208:211], a[52:55], v[16:19]\n" \
    "ds_read_b128 v[176:179], v138 offset:49152\n" \
    "v_mfma_f32_16x16x32_bf16 v[48:51], v[208:211], a[116:119], v[48:51]\n" \
    "v_mfma_f32_16x16x32_bf16 v[80:83], v[208:211], a[180:183], v[80:83]\n" \
    "v_mfma_f32_16x16x32_bf16 v[112:115], v[208:211], a[244:247], v[112:115]\n" \
    "s_waitcnt lgkmcnt(7)\n" \
    "v_mfma_f32_16x16x32_bf16 v[20:23], v[212:215], a[52:55], v[20:23]\n" \
    "ds_read_b128 v[180:183], v138 offset:53248\n" \
    "v_mfma_f32_16x16x32_bf16 v[52:55], v[212:215], a[116:119], v[52:55]\n" \
    "v_mfma_f32_16x16x32_bf16 v[84:87], v[212:215], a[180:183], v[84:87]\n" \
    "v_mfma_f32_16x16x32_bf16 v[116:119], v[212:215], a[244:247], v[116:119]\n" \
    "s_waitcnt lgkmcnt(7)\n" \
    "v_mfma_f32_16x16x32_bf16 v[24:27], v[216:219], a[52:55], v[24:27]\n" \
    "ds_read_b128 v[184:187], v138 offset:57344\n" \
    "v_mfma_f32_16x16x32_bf16 v[56:59], v[216:219], a[116:119], v[56:59]\n" \
    "v_mfma_f32_16x16x32_bf16 v[88:91], v[216:219], a[180:183], v[88:91]\n" \
    "v_mfma_f32_16x16x32_bf16 v[120:123], v[216:219], a[244:247], v[120:123]\n" \
    "s_waitcnt lgkmcnt(7)\n" \
    "v_mfma_f32_16x16x32_bf16 v[28:31], v[220:223], a[52:55], v[28:31]\n" \
    "ds_read_b128 v[188:191], v138 offset:61440\n" \
    "v_mfma_f32_16x16x32_bf16 v[60:63], v[220:223], a[116:119], v[60:63]\n" \
    "v_mfma_f32_16x16x32_bf16 v[92:95], v[220:223], a[180:183], v[92:95]\n" \
    "v_mfma_f32_16x16x32_bf16 v[124:127], v[220:223], a[244:247], v[124:127]\n" \
    "s_waitcnt lgkmcnt(7)\n" \
    "v_mfma_f32_16x16x32_bf16 v[0:3], v[160:163], a[56:59], v[0:3]\n" \
    "ds_read_b128 v[192:195], v139 offset:32768\n" \
    "v_mfma_f32_16x16x32_bf16 v[32:35], v[160:163], a[120:123], v[32:35]\n" \
    "v_mfma_f32_16x16x32_bf16 v[64:67], v[160:163], a[184:187], v[64:67]\n" \
    "v_mfma_f32_16x16x32_bf16 v[96:99], v[160:163], a[248:251], v[96:99]\n" \
    "s_waitcnt lgkmcnt(7)\n" \
    "v_mfma_f32_16x16x32_bf16 v[4:7], v[164:167], a[56:59], v[4:7]\n" \
    "ds_read_b128 v[196:199], v139 offset:36864\n" \
    "v_mfma_f32_16x16x32_bf16 v[36:39], v[164:167], a[120:123], v[36:39]\n" \
    "v_mfma_f32_16x16x32_bf16 v[68:71], v[164:167], a[184:187], v[68:71]\n" \
    "v_mfma_f32_16x16x32_bf16 v[100:103], v[164:167], a[248:251], v[100:103]\n" \
    "s_waitcnt lgkmcnt(7)\n" \
    "v_mfma_f32_16x16x32_bf16 v[8:11], v[168:171], a[56:59], v[8:11]\n" \
    "ds_read_b128 v[200:203], v139 offset:40960\n" \
    "v_mfma_f32_16x16x32_bf16 v[40:43], v[168:171], a[120:123], v[40:43]\n" \
    "v_mfma_f32_16x16x32_bf16 v[72:75], v[168:171], a[184:187], v[72:75]\n" \
    "v_mfma_f32_16x16x32_bf16 v[104:107], v[168:171], a[248:251], v[104:107]\n" \
    "s_waitcnt lgkmcnt(7)\n" \
    "v_mfma_f32_16x16x32_bf16 v[12:15], v[172:175], a[56:59], v[12:15]\n" \
    "ds_read_b128 v[204:207], v139 offset:45056\n" \
    "v_mfma_f32_16x16x32_bf16 v[44:47], v[172:175], a[120:123], v[44:47]\n" \
    "v_mfma_f32_16x16x32_bf16 v[76:79], v[172:175], a[184:187], v[76:79]\n" \
    "v_mfma_f32_16x16x32_bf16 v[108:111], v[172:175], a[248:251], v[108:111]\n" \
    "s_waitcnt lgkmcnt(7)\n" \
    "v_mfma_f32_16x16x32_bf16 v[16:19], v[176:179], a[56:59], v[16:19]\n" \
    "ds_read_b128 v[208:211], v139 offset:49152\n" \
    "v_mfma_f32_16x16x32_bf16 v[48:51], v[176:179], a[120:123], v[48:51]\n" \
    "v_mfma_f32_16x16x32_bf16 v[80:83], v[176:179], a[184:187], v[80:83]\n" \
    "v_mfma_f32_16x16x32_bf16 v[112:115], v[176:179], a[248:251], v[112:115]\n" \
    "s_waitcnt lgkmcnt(7)\n" \
    "v_mfma_f32_16x16x32_bf16 v[20:23], v[180:183], a[56:59], v[20:23]\n" \
    "ds_read_b128 v[212:215], v139 offset:53248\n" \
    "v_mfma_f32_16x16x32_bf16 v[52:55], v[180:183], a[120:123], v[52:55]\n" \
    "v_mfma_f32_16x16x32_bf16 v[84:87], v[180:183], a[184:187], v[84:87]\n" \
    "v_mfma_f32_16x16x32_bf16 v[116:119], v[180:183], a[248:251], v[116:119]\n" \
    "s_waitcnt lgkmcnt(7)\n" \
    "v_mfma_f32_16x16x32_bf16 v[24:27], v[184:187], a[56:59], v[24:27]\n" \
    "ds_read_b128 v[216:219], v139 offset:57344\n" \
    "v_mfma_f32_16x16x32_bf16 v[56:59], v[184:187], a[120:123], v[56:59]\n" \
    "v_mfma_f32_16x16x32_bf16 v[88:91], v[184:187], a[184:187], v[88:91]\n" \
    "v_mfma_f32_16x16x32_bf16 v[120:123], v[184:187], a[248:251], v[120:123]\n" \
    "s_waitcnt lgkmcnt(7)\n" \
    "v_mfma_f32_16x16x32_bf16 v[28:31], v[188:191], a[56:59], v[28:31]\n" \
    "ds_read_b128 v[220:223], v139 offset:61440\n" \
    "v_mfma_f32_16x16x32_bf16 v[60:63], v[188:191], a[120:123], v[60:63]\n" \
    "v_mfma_f32_16x16x32_bf16 v[92:95], v[188:191], a[184:187], v[92:95]\n" \
    "v_mfma_f32_16x16x32_bf16 v[124:127], v[188:191], a[248:251], v[124:127]\n" \
    "s_waitcnt vmcnt(16) lgkmcnt(0)\n" \
    "s_barrier\n" \
    "v_mfma_f32_16x16x32_bf16 v[0:3], v[192:195], a[60:63], v[0:3]\n" \
    "ds_read_b128 v[160:163], v132 offset:0\n" \
    "v_mfma_f32_16x16x32_bf16 v[32:35], v[192:195], a[124:127], v[32:35]\n" \
    "s_add_u32 m0, s68, 98304\n" \
    "v_mfma_f32_16x16x32_bf16 v[64:67], v[192:195], a[188:191], v[64:67]\n" \
    "global_load_lds_dwordx4 v128, s[60:61]\n" \
    "v_mfma_f32_16x16x32_bf16 v[96:99], v[192:195], a[252:255], v[96:99]\n" \
    "s_add_u32 m0, s68, 99328\n" \
    "v_mfma_f32_16x16x32_bf16 v[4:7], v[196:199], a[60:63], v[4:7]\n" \
    "ds_read_b128 v[164:167], v132 offset:4096\n" \
    "v_mfma_f32_16x16x32_bf16 v[36:39], v[196:199], a[124:127], v[36:39]\n" \
    "global_load_lds_dwordx4 v129, s[60:61]\n" \
    "v_mfma_f32_16x16x32_bf16 v[68:71], v[196:199], a[188:191], v[68:71]\n" \
    "s_add_u32 m0, s68, 100352\n" \
    "v_mfma_f32_16x16x32_bf16 v[100:103], v[196:199], a[252:255], v[100:103]\n" \
    "global_load_lds_dwordx4 v130, s[60:61]\n" \
    "v_mfma_f32_16x16x32_bf16 v[8:11], v[200:203], a[60:63], v[8:11]\n" \
    "ds_read_b128 v[168:171], v132 offset:8192\n" \
    "v_mfma_f32_16x16x32_bf16 v[40:43], v[200:203], a[124:127], v[40:43]\n" \
    "s_add_u32 m0, s68, 101376\n" \
    "v_mfma_f32_16x16x32_bf16 v[72:75], v[200:203], a[188:191], v[72:75]\n" \
    "global_load_lds_dwordx4 v131, s[60:61]\n" \
    "v_mfma_f32_16x16x32_bf16 v[104:107], v[200:203], a[252:255], v[104:107]\n" \
    "s_add_u32 m0, s68, 102400\n" \
    "v_mfma_f32_16x16x32_bf16 v[12:15], v[204:207], a[60:63], v[12:15]\n" \
    "ds_read_b128 v[172:175], v132 offset:12288\n" \
    "v_mfma_f32_16x16x32_bf16 v[44:47], v[204:207], a[124:127], v[44:47]\n" \
    "global_load_lds_dwordx4 v128, s[62:63]\n" \
    "v_mfma_f32_16x16x32_bf16 v[76:79], v[204:207], a[188:191], v[76:79]\n" \
    "s_add_u32 m0, s68, 103424\n" \
    "v_mfma_f32_16x16x32_bf16 v[108:111], v[204:207], a[252:255], v[108:111]\n" \
    "global_load_lds_dwordx4 v129, s[62:63]\n" \
    "v_mfma_f32_16x16x32_bf16 v[16:19], v[208:211], a[60:63], v[16:19]\n" \
    "ds_read_b128 v[176:179], v132 offset:16384\n" \
    "v_mfma_f32_16x16x32_bf16 v[48:51], v[208:211], a[124:127], v[48:51]\n" \
    "s_add_u32 m0, s68, 104448\n" \
    "v_mfma_f32_16x16x32_bf16 v[80:83], v[208:211], a[188:191], v[80:83]\n" \
    "global_load_lds_dwordx4 v130, s[62:63]\n" \
    "v_mfma_f32_16x16x32_bf16 v[112:115], v[208:211], a[252:255], v[112:115]\n" \
    "s_add_u32 m0, s68, 105472\n" \
    "v_mfma_f32_16x16x32_bf16 v[20:23], v[212:215], a[60:63], v[20:23]\n" \
    "ds_read_b128 v[180:183], v132 offset:20480\n" \
    "v_mfma_f32_16x16x32_bf16 v[52:55], v[212:215], a[124:127], v[52:55]\n" \
    "global_load_lds_dwordx4 v131, s[62:63]\n" \
    "v_mfma_f32_16x16x32_bf16 v[84:87], v[212:215], a[188:191], v[84:87]\n" \
    "s_add_u32 s60, s60, 256\n" \
    "v_mfma_f32_16x16x32_bf16 v[116:119], v[212:215], a[252:255], v[116:119]\n" \
    "s_addc_u32 s61, s61, 0\n" \
    "v_mfma_f32_16x16x32_bf16 v[24:27], v[216:219], a[60:63], v[24:27]\n" \
    "ds_read_b128 v[184:187], v132 offset:24576\n" \
    "v_mfma_f32_16x16x32_bf16 v[56:59], v[216:219], a[124:127], v[56:59]\n" \
    "s_add_u32 s62, s62, 256\n" \
    "v_mfma_f32_16x16x32_bf16 v[88:91], v[216:219], a[188:191], v[88:91]\n" \
    "s_addc_u32 s63, s63, 0\n" \
    "v_mfma_f32_16x16x32_bf16 v[120:123], v[216:219], a[252:255], v[120:123]\n" \
    "v_mfma_f32_16x16x32_bf16 v[28:31], v[220:223], a[60:63], v[28:31]\n" \
    "ds_read_b128 v[188:191], v132 offset:28672\n" \
    "v_mfma_f32_16x16x32_bf16 v[60:63], v[220:223], a[124:127], v[60:63]\n" \
    "v_mfma_f32_16x16x32_bf16 v[92:95], v[220:223], a[188:191], v[92:95]\n" \
    "v_mfma_f32_16x16x32_bf16 v[124:127], v[220:223], a[252:255], v[124:127]\n" \
    "EPI_%=:\n" \
    "s_nop 7\n" \
    "s_cmp_ge_u32 s53, 2\n" \
    "s_cselect_b32 s70, s48, s46\n" \
    "s_cselect_b32 s71, s49, s47\n" \
    "s_mov_b32 s58, 1048576\n" \
    "s_cmp_eq_u32 s52, 2\n" \
    "s_cselect_b32 s58, 458752, s58\n" \
    "s_mul_i32 s58, s58, s55\n" \
    "s_add_u32 s70, s70, s58\n" \
    "s_addc_u32 s71, s71, 0\n" \
    "s_cmp_eq_u32 s52, 2\n" \
    "s_cbranch_scc1 EU_%=\n" \
    "s_cmp_eq_u32 s52, 1\n" \
    "s_cbranch_scc1 EK_%=\n" \
    "s_cmp_ge_u32 s53, 2\n" \
    "s_cbranch_scc1 EV_%=\n" \
    "EK_%=:\n" \
    "v_cvt_pk_bf16_f32 v224, v0, v1\n" \
    "v_cvt_pk_bf16_f32 v225, v2, v3\n" \
    "v_cvt_pk_bf16_f32 v226, v32, v33\n" \
    "v_cvt_pk_bf16_f32 v227, v34, v35\n" \
    "global_store_dwordx4 v140, v[224:227], s[70:71] offset:0 sc1\n" \
    "v_cvt_pk_bf16_f32 v228, v64, v65\n" \
    "v_cvt_pk_bf16_f32 v229, v66, v67\n" \
    "v_cvt_pk_bf16_f32 v230, v96, v97\n" \
    "v_cvt_pk_bf16_f32 v231, v98, v99\n" \
    "global_store_dwordx4 v140, v[228:231], s[70:71] offset:1024 sc1\n" \
    "s_add_u32 s70, s70, 4096\n" \
    "s_addc_u32 s71, s71, 0\n" \
    "v_cvt_pk_bf16_f32 v232, v4, v5\n" \
    "v_cvt_pk_bf16_f32 v233, v6, v7\n" \
    "v_cvt_pk_bf16_f32 v234, v36, v37\n" \
    "v_cvt_pk_bf16_f32 v235, v38, v39\n" \
    "global_store_dwordx4 v140, v[232:235], s[70:71] offset:0 sc1\n" \
    "v_cvt_pk_bf16_f32 v236, v68, v69\n" \
    "v_cvt_pk_bf16_f32 v237, v70, v71\n" \
    "v_cvt_pk_bf16_f32 v238, v100, v101\n" \
    "v_cvt_pk_bf16_f32 v239, v102, v103\n" \
    "global_store_dwordx4 v140, v[236:239], s[70:71] offset:1024 sc1\n" \
    "s_add_u32 s70, s70, 258048\n" \
    "s_addc_u32 s71, s71, 0\n" \
    "v_cvt_pk_bf16_f32 v224, v8, v9\n" \
    "v_cvt_pk_bf16_f32 v225, v10, v11\n" \
    "v_cvt_pk_bf16_f32 v226, v40, v41\n" \
    "v_cvt_pk_bf16_f32 v227, v42, v43\n" \
    "global_store_dwordx4 v140, v[224:227], s[70:71] offset:0 sc1\n" \
    "v_cvt_pk_bf16_f32 v228, v72, v73\n" \
    "v_cvt_pk_bf16_f32 v229, v74, v75\n" \
    "v_cvt_pk_bf16_f32 v230, v104, v105\n" \
    "v_cvt_pk_bf16_f32 v231, v106, v107\n" \
    "global_store_dwordx4 v140, v[228:231], s[70:71] offset:1024 sc1\n" \
    "s_add_u32 s70, s70, 4096\n" \
    "s_addc_u32 s71, s71, 0\n" \
    "v_cvt_pk_bf16_f32 v232, v12, v13\n" \
    "v_cvt_pk_bf16_f32 v233, v14, v15\n" \
    "v_cvt_pk_bf16_f32 v234, v44, v45\n" \
    "v_cvt_pk_bf16_f32 v235, v46, v47\n" \
    "global_store_dwordx4 v140, v[232:235], s[70:71] offset:0 sc1\n" \
    "v_cvt_pk_bf16_f32 v236, v76, v77\n" \
    "v_cvt_pk_bf16_f32 v237, v78, v79\n" \
    "v_cvt_pk_bf16_f32 v238, v108, v109\n" \
    "v_cvt_pk_bf16_f32 v239, v110, v111\n" \
    "global_store_dwordx4 v140, v[236:239], s[70:71] offset:1024 sc1\n" \
    "s_add_u32 s70, s70, 258048\n" \
    "s_addc_u32 s71, s71, 0\n" \
    "v_cvt_pk_bf16_f32 v224, v16, v17\n" \
    "v_cvt_pk_bf16_f32 v225, v18, v19\n" \
    "v_cvt_pk_bf16_f32 v226, v48, v49\n" \
    "v_cvt_pk_bf16_f32 v227, v50, v51\n" \
    "global_store_dwordx4 v140, v[224:227], s[70:71] offset:0 sc1\n" \
    "v_cvt_pk_bf16_f32 v228, v80, v81\n" \
    "v_cvt_pk_bf16_f32 v229, v82, v83\n" \
    "v_cvt_pk_bf16_f32 v230, v112, v113\n" \
    "v_cvt_pk_bf16_f32 v231, v114, v115\n" \
    "global_store_dwordx4 v140, v[228:231], s[70:71] offset:1024 sc1\n" \
    "s_add_u32 s70, s70, 4096\n" \
    "s_addc_u32 s71, s71, 0\n" \
    "v_cvt_pk_bf16_f32 v232, v20, v21\n" \
    "v_cvt_pk_bf16_f32 v233, v22, v23\n" \
    "v_cvt_pk_bf16_f32 v234, v52, v53\n" \
    "v_cvt_pk_bf16_f32 v235, v54, v55\n" \
    "global_store_dwordx4 v140, v[232:235], s[70:71] offset:0 sc1\n" \
    "v_cvt_pk_bf16_f32 v236, v84, v85\n" \
    "v_cvt_pk_bf16_f32 v237, v86, v87\n" \
    "v_cvt_pk_bf16_f32 v238, v116, v117\n" \
    "v_cvt_pk_bf16_f32 v239, v118, v119\n" \
    "global_store_dwordx4 v140, v[236:239], s[70:71] offset:1024 sc1\n" \
    "s_add_u32 s70, s70, 258048\n" \
    "s_addc_u32 s71, s71, 0\n" \
    "v_cvt_pk_bf16_f32 v224, v24, v25\n" \
    "v_cvt_pk_bf16_f32 v225, v26, v27\n" \
    "v_cvt_pk_bf16_f32 v226, v56, v57\n" \
    "v_cvt_pk_bf16_f32 v227, v58, v59\n" \
    "global_store_dwordx4 v140, v[224:227], s[70:71] offset:0 sc1\n" \
    "v_cvt_pk_bf16_f32 v228, v88, v89\n" \
    "v_cvt_pk_bf16_f32 v229, v90, v91\n" \
    "v_cvt_pk_bf16_f32 v230, v120, v121\n" \
    "v_cvt_pk_bf16_f32 v231, v122, v123\n" \
    "global_store_dwordx4 v140, v[228:231], s[70:71] offset:1024 sc1\n" \
    "s_add_u32 s70, s70, 4096\n" \
    "s_addc_u32 s71, s71, 0\n" \
    "v_cvt_pk_bf16_f32 v232, v28, v29\n" \
    "v_cvt_pk_bf16_f32 v233, v30, v31\n" \
    "v_cvt_pk_bf16_f32 v234, v60, v61\n" \
    "v_cvt_pk_bf16_f32 v235, v62, v63\n" \
    "global_store_dwordx4 v140, v[232:235], s[70:71] offset:0 sc1\n" \
    "v_cvt_pk_bf16_f32 v236, v92, v93\n" \
    "v_cvt_pk_bf16_f32 v237, v94, v95\n" \
    "v_cvt_pk_bf16_f32 v238, v124, v125\n" \
    "v_cvt_pk_bf16_f32 v239, v126, v127\n" \
    "global_store_dwordx4 v140, v[236:239], s[70:71] offset:1024 sc1\n" \
    "s_branch ED_%=\n" \
    "EV_%=:\n" \
    "v_cvt_pk_bf16_f32 v224, v0, v1\n" \
    "v_cvt_pk_bf16_f32 v225, v2, v3\n" \
    "v_cvt_pk_bf16_f32 v226, v4, v5\n" \
    "v_cvt_pk_bf16_f32 v227, v6, v7\n" \
    "global_store_dwordx4 v140, v[224:227], s[70:71] offset:0 sc1\n" \
    "v_cvt_pk_bf16_f32 v228, v32, v33\n" \
    "v_cvt_pk_bf16_f32 v229, v34, v35\n" \
    "v_cvt_pk_bf16_f32 v230, v36, v37\n" \
    "v_cvt_pk_bf16_f32 v231, v38, v39\n" \
    "global_store_dwordx4 v140, v[228:231], s[70:71] offset:1024 sc1\n" \
    "v_cvt_pk_bf16_f32 v232, v64, v65\n" \
    "v_cvt_pk_bf16_f32 v233, v66, v67\n" \
    "v_cvt_pk_bf16_f32 v234, v68, v69\n" \
    "v_cvt_pk_bf16_f32 v235, v70, v71\n" \
    "global_store_dwordx4 v140, v[232:235], s[70:71] offset:2048 sc1\n" \
    "v_cvt_pk_bf16_f32 v236, v96, v97\n" \
    "v_cvt_pk_bf16_f32 v237, v98, v99\n" \
    "v_cvt_pk_bf16_f32 v238, v100, v101\n" \
    "v_cvt_pk_bf16_f32 v239, v102, v103\n" \
    "global_store_dwordx4 v140, v[236:239], s[70:71] offset:3072 sc1\n" \
    "s_add_u32 s70, s70, 262144\n" \
    "s_addc_u32 s71, s71, 0\n" \
    "v_cvt_pk_bf16_f32 v224, v8, v9\n" \
    "v_cvt_pk_bf16_f32 v225, v10, v11\n" \
    "v_cvt_pk_bf16_f32 v226, v12, v13\n" \
    "v_cvt_pk_bf16_f32 v227, v14, v15\n" \
    "global_store_dwordx4 v140, v[224:227], s[70:71] offset:0 sc1\n" \
    "v_cvt_pk_bf16_f32 v228, v40, v41\n" \
    "v_cvt_pk_bf16_f32 v229, v42, v43\n" \
    "v_cvt_pk_bf16_f32 v230, v44, v45\n" \
    "v_cvt_pk_bf16_f32 v231, v46, v47\n" \
    "global_store_dwordx4 v140, v[228:231], s[70:71] offset:1024 sc1\n" \
    "v_cvt_pk_bf16_f32 v232, v72, v73\n" \
    "v_cvt_pk_bf16_f32 v233, v74, v75\n" \
    "v_cvt_pk_bf16_f32 v234, v76, v77\n" \
    "v_cvt_pk_bf16_f32 v235, v78, v79\n" \
    "global_store_dwordx4 v140, v[232:235], s[70:71] offset:2048 sc1\n" \
    "v_cvt_pk_bf16_f32 v236, v104, v105\n" \
    "v_cvt_pk_bf16_f32 v237, v106, v107\n" \
    "v_cvt_pk_bf16_f32 v238, v108, v109\n" \
    "v_cvt_pk_bf16_f32 v239, v110, v111\n" \
    "global_store_dwordx4 v140, v[236:239], s[70:71] offset:3072 sc1\n" \
    "s_add_u32 s70, s70, 262144\n" \
    "s_addc_u32 s71, s71, 0\n" \
    "v_cvt_pk_bf16_f32 v224, v16, v17\n" \
    "v_cvt_pk_bf16_f32 v225, v18, v19\n" \
    "v_cvt_pk_bf16_f32 v226, v20, v21\n" \
    "v_cvt_pk_bf16_f32 v227, v22, v23\n" \
    "global_store_dwordx4 v140, v[224:227], s[70:71] offset:0 sc1\n" \
    "v_cvt_pk_bf16_f32 v228, v48, v49\n" \
    "v_cvt_pk_bf16_f32 v229, v50, v51\n" \
    "v_cvt_pk_bf16_f32 v230, v52, v53\n" \
    "v_cvt_pk_bf16_f32 v231, v54, v55\n" \
    "global_store_dwordx4 v140, v[228:231], s[70:71] offset:1024 sc1\n" \
    "v_cvt_pk_bf16_f32 v232, v80, v81\n" \
    "v_cvt_pk_bf16_f32 v233, v82, v83\n" \
    "v_cvt_pk_bf16_f32 v234, v84, v85\n" \
    "v_cvt_pk_bf16_f32 v235, v86, v87\n" \
    "global_store_dwordx4 v140, v[232:235], s[70:71] offset:2048 sc1\n" \
    "v_cvt_pk_bf16_f32 v236, v112, v113\n" \
    "v_cvt_pk_bf16_f32 v237, v114, v115\n" \
    "v_cvt_pk_bf16_f32 v238, v116, v117\n" \
    "v_cvt_pk_bf16_f32 v239, v118, v119\n" \
    "global_store_dwordx4 v140, v[236:239], s[70:71] offset:3072 sc1\n" \
    "s_add_u32 s70, s70, 262144\n" \
    "s_addc_u32 s71, s71, 0\n" \
    "v_cvt_pk_bf16_f32 v224, v24, v25\n" \
    "v_cvt_pk_bf16_f32 v225, v26, v27\n" \
    "v_cvt_pk_bf16_f32 v226, v28, v29\n" \
    "v_cvt_pk_bf16_f32 v227, v30, v31\n" \
    "global_store_dwordx4 v140, v[224:227], s[70:71] offset:0 sc1\n" \
    "v_cvt_pk_bf16_f32 v228, v56, v57\n" \
    "v_cvt_pk_bf16_f32 v229, v58, v59\n" \
    "v_cvt_pk_bf16_f32 v230, v60, v61\n" \
    "v_cvt_pk_bf16_f32 v231, v62, v63\n" \
    "global_store_dwordx4 v140, v[228:231], s[70:71] offset:1024 sc1\n" \
    "v_cvt_pk_bf16_f32 v232, v88, v89\n" \
    "v_cvt_pk_bf16_f32 v233, v90, v91\n" \
    "v_cvt_pk_bf16_f32 v234, v92, v93\n" \
    "v_cvt_pk_bf16_f32 v235, v94, v95\n" \
    "global_store_dwordx4 v140, v[232:235], s[70:71] offset:2048 sc1\n" \
    "v_cvt_pk_bf16_f32 v236, v120, v121\n" \
    "v_cvt_pk_bf16_f32 v237, v122, v123\n" \
    "v_cvt_pk_bf16_f32 v238, v124, v125\n" \
    "v_cvt_pk_bf16_f32 v239, v126, v127\n" \
    "global_store_dwordx4 v140, v[236:239], s[70:71] offset:3072 sc1\n" \
    "s_branch ED_%=\n" \
    "EU_%=:\n" \
    "v_mul_f32 v232, s76, v0\n" \
    "v_mul_f32 v233, s76, v1\n" \
    "v_mul_f32 v234, s76, v2\n" \
    "v_mul_f32 v235, s76, v3\n" \
    "v_exp_f32 v232, v232\n" \
    "v_exp_f32 v233, v233\n" \
    "v_exp_f32 v234, v234\n" \
    "v_exp_f32 v235, v235\n" \
    "v_mul_f32 v64, v64, v0\n" \
    "v_mul_f32 v65, v65, v1\n" \
    "v_mul_f32 v66, v66, v2\n" \
    "v_mul_f32 v67, v67, v3\n" \
    "v_add_f32 v232, 1.0, v232\n" \
    "v_add_f32 v233, 1.0, v233\n" \
    "v_add_f32 v234, 1.0, v234\n" \
    "v_add_f32 v235, 1.0, v235\n" \
    "v_rcp_f32 v232, v232\n" \
    "v_rcp_f32 v233, v233\n" \
    "v_rcp_f32 v234, v234\n" \
    "v_rcp_f32 v235, v235\n" \
    "s_nop 0\n" \
    "v_mul_f32 v64, v64, v232\n" \
    "v_mul_f32 v65, v65, v233\n" \
    "v_mul_f32 v66, v66, v234\n" \
    "v_mul_f32 v67, v67, v235\n" \
    "v_cvt_pk_bf16_f32 v224, v64, v65\n" \
    "v_cvt_pk_bf16_f32 v225, v66, v67\n" \
    "v_mul_f32 v232, s76, v32\n" \
    "v_mul_f32 v233, s76, v33\n" \
    "v_mul_f32 v234, s76, v34\n" \
    "v_mul_f32 v235, s76, v35\n" \
    "v_exp_f32 v232, v232\n" \
    "v_exp_f32 v233, v233\n" \
    "v_exp_f32 v234, v234\n" \
    "v_exp_f32 v235, v235\n" \
    "v_mul_f32 v96, v96, v32\n" \
    "v_mul_f32 v97, v97, v33\n" \
    "v_mul_f32 v98, v98, v34\n" \
    "v_mul_f32 v99, v99, v35\n" \
    "v_add_f32 v232, 1.0, v232\n" \
    "v_add_f32 v233, 1.0, v233\n" \
    "v_add_f32 v234, 1.0, v234\n" \
    "v_add_f32 v235, 1.0, v235\n" \
    "v_rcp_f32 v232, v232\n" \
    "v_rcp_f32 v233, v233\n" \
    "v_rcp_f32 v234, v234\n" \
    "v_rcp_f32 v235, v235\n" \
    "s_nop 0\n" \
    "v_mul_f32 v96, v96, v232\n" \
    "v_mul_f32 v97, v97, v233\n" \
    "v_mul_f32 v98, v98, v234\n" \
    "v_mul_f32 v99, v99, v235\n" \
    "v_cvt_pk_bf16_f32 v226, v96, v97\n" \
    "v_cvt_pk_bf16_f32 v227, v98, v99\n" \
    "global_store_dwordx4 v140, v[224:227], s[70:71] sc1\n" \
    "s_add_u32 s70, s70, 57344\n" \
    "s_addc_u32 s71, s71, 0\n" \
    "v_mul_f32 v232, s76, v4\n" \
    "v_mul_f32 v233, s76, v5\n" \
    "v_mul_f32 v234, s76, v6\n" \
    "v_mul_f32 v235, s76, v7\n" \
    "v_exp_f32 v232, v232\n" \
    "v_exp_f32 v233, v233\n" \
    "v_exp_f32 v234, v234\n" \
    "v_exp_f32 v235, v235\n" \
    "v_mul_f32 v68, v68, v4\n" \
    "v_mul_f32 v69, v69, v5\n" \
    "v_mul_f32 v70, v70, v6\n" \
    "v_mul_f32 v71, v71, v7\n" \
    "v_add_f32 v232, 1.0, v232\n" \
    "v_add_f32 v233, 1.0, v233\n" \
    "v_add_f32 v234, 1.0, v234\n" \
    "v_add_f32 v235, 1.0, v235\n" \
    "v_rcp_f32 v232, v232\n" \
    "v_rcp_f32 v233, v233\n" \
    "v_rcp_f32 v234, v234\n" \
    "v_rcp_f32 v235, v235\n" \
    "s_nop 0\n" \
    "v_mul_f32 v68, v68, v232\n" \
    "v_mul_f32 v69, v69, v233\n" \
    "v_mul_f32 v70, v70, v234\n" \
    "v_mul_f32 v71, v71, v235\n" \
    "v_cvt_pk_bf16_f32 v228, v68, v69\n" \
    "v_cvt_pk_bf16_f32 v229, v70, v71\n" \
    "v_mul_f32 v232, s76, v36\n" \
    "v_mul_f32 v233, s76, v37\n" \
    "v_mul_f32 v234, s76, v38\n" \
    "v_mul_f32 v235, s76, v39\n" \
    "v_exp_f32 v232, v232\n" \
    "v_exp_f32 v233, v233\n" \
    "v_exp_f32 v234, v234\n" \
    "v_exp_f32 v235, v235\n" \
    "v_mul_f32 v100, v100, v36\n" \
    "v_mul_f32 v101, v101, v37\n" \
    "v_mul_f32 v102, v102, v38\n" \
    "v_mul_f32 v103, v103, v39\n" \
    "v_add_f32 v232, 1.0, v232\n" \
    "v_add_f32 v233, 1.0, v233\n" \
    "v_add_f32 v234, 1.0, v234\n" \
    "v_add_f32 v235, 1.0, v235\n" \
    "v_rcp_f32 v232, v232\n" \
    "v_rcp_f32 v233, v233\n" \
    "v_rcp_f32 v234, v234\n" \
    "v_rcp_f32 v235, v235\n" \
    "s_nop 0\n" \
    "v_mul_f32 v100, v100, v232\n" \
    "v_mul_f32 v101, v101, v233\n" \
    "v_mul_f32 v102, v102, v234\n" \
    "v_mul_f32 v103, v103, v235\n" \
    "v_cvt_pk_bf16_f32 v230, v100, v101\n" \
    "v_cvt_pk_bf16_f32 v231, v102, v103\n" \
    "global_store_dwordx4 v140, v[228:231], s[70:71] sc1\n" \
    "s_add_u32 s70, s70, 57344\n" \
    "s_addc_u32 s71, s71, 0\n" \
    "v_mul_f32 v232, s76, v8\n" \
    "v_mul_f32 v233, s76, v9\n" \
    "v_mul_f32 v234, s76, v10\n" \
    "v_mul_f32 v235, s76, v11\n" \
    "v_exp_f32 v232, v232\n" \
    "v_exp_f32 v233, v233\n" \
    "v_exp_f32 v234, v234\n" \
    "v_exp_f32 v235, v235\n" \
    "v_mul_f32 v72, v72, v8\n" \
    "v_mul_f32 v73, v73, v9\n" \
    "v_mul_f32 v74, v74, v10\n" \
    "v_mul_f32 v75, v75, v11\n" \
    "v_add_f32 v232, 1.0, v232\n" \
    "v_add_f32 v233, 1.0, v233\n" \
    "v_add_f32 v234, 1.0, v234\n" \
    "v_add_f32 v235, 1.0, v235\n" \
    "v_rcp_f32 v232, v232\n" \
    "v_rcp_f32 v233, v233\n" \
    "v_rcp_f32 v234, v234\n" \
    "v_rcp_f32 v235, v235\n" \
    "s_nop 0\n" \
    "v_mul_f32 v72, v72, v232\n" \
    "v_mul_f32 v73, v73, v233\n" \
    "v_mul_f32 v74, v74, v234\n" \
    "v_mul_f32 v75, v75, v235\n" \
    "v_cvt_pk_bf16_f32 v224, v72, v73\n" \
    "v_cvt_pk_bf16_f32 v225, v74, v75\n" \
    "v_mul_f32 v232, s76, v40\n" \
    "v_mul_f32 v233, s76, v41\n" \
    "v_mul_f32 v234, s76, v42\n" \
    "v_mul_f32 v235, s76, v43\n" \
    "v_exp_f32 v232, v232\n" \
    "v_exp_f32 v233, v233\n" \
    "v_exp_f32 v234, v234\n" \
    "v_exp_f32 v235, v235\n" \
    "v_mul_f32 v104, v104, v40\n" \
    "v_mul_f32 v105, v105, v41\n" \
    "v_mul_f32 v106, v106, v42\n" \
    "v_mul_f32 v107, v107, v43\n" \
    "v_add_f32 v232, 1.0, v232\n" \
    "v_add_f32 v233, 1.0, v233\n" \
    "v_add_f32 v234, 1.0, v234\n" \
    "v_add_f32 v235, 1.0, v235\n" \
    "v_rcp_f32 v232, v232\n" \
    "v_rcp_f32 v233, v233\n" \
    "v_rcp_f32 v234, v234\n" \
    "v_rcp_f32 v235, v235\n" \
    "s_nop 0\n" \
    "v_mul_f32 v104, v104, v232\n" \
    "v_mul_f32 v105, v105, v233\n" \
    "v_mul_f32 v106, v106, v234\n" \
    "v_mul_f32 v107, v107, v235\n" \
    "v_cvt_pk_bf16_f32 v226, v104, v105\n" \
    "v_cvt_pk_bf16_f32 v227, v106, v107\n" \
    "global_store_dwordx4 v140, v[224:227], s[70:71] sc1\n" \
    "s_add_u32 s70, s70, 57344\n" \
    "s_addc_u32 s71, s71, 0\n" \
    "v_mul_f32 v232, s76, v12\n" \
    "v_mul_f32 v233, s76, v13\n" \
    "v_mul_f32 v234, s76, v14\n" \
    "v_mul_f32 v235, s76, v15\n" \
    "v_exp_f32 v232, v232\n" \
    "v_exp_f32 v233, v233\n" \
    "v_exp_f32 v234, v234\n" \
    "v_exp_f32 v235, v235\n" \
    "v_mul_f32 v76, v76, v12\n" \
    "v_mul_f32 v77, v77, v13\n" \
    "v_mul_f32 v78, v78, v14\n" \
    "v_mul_f32 v79, v79, v15\n" \
    "v_add_f32 v232, 1.0, v232\n" \
    "v_add_f32 v233, 1.0, v233\n" \
    "v_add_f32 v234, 1.0, v234\n" \
    "v_add_f32 v235, 1.0, v235\n" \
    "v_rcp_f32 v232, v232\n" \
    "v_rcp_f32 v233, v233\n" \
    "v_rcp_f32 v234, v234\n" \
    "v_rcp_f32 v235, v235\n" \
    "s_nop 0\n" \
    "v_mul_f32 v76, v76, v232\n" \
    "v_mul_f32 v77, v77, v233\n" \
    "v_mul_f32 v78, v78, v234\n" \
    "v_mul_f32 v79, v79, v235\n" \
    "v_cvt_pk_bf16_f32 v228, v76, v77\n" \
    "v_cvt_pk_bf16_f32 v229, v78, v79\n" \
    "v_mul_f32 v232, s76, v44\n" \
    "v_mul_f32 v233, s76, v45\n" \
    "v_mul_f32 v234, s76, v46\n" \
    "v_mul_f32 v235, s76, v47\n" \
    "v_exp_f32 v232, v232\n" \
    "v_exp_f32 v233, v233\n" \
    "v_exp_f32 v234, v234\n" \
    "v_exp_f32 v235, v235\n" \
    "v_mul_f32 v108, v108, v44\n" \
    "v_mul_f32 v109, v109, v45\n" \
    "v_mul_f32 v110, v110, v46\n" \
    "v_mul_f32 v111, v111, v47\n" \
    "v_add_f32 v232, 1.0, v232\n" \
    "v_add_f32 v233, 1.0, v233\n" \
    "v_add_f32 v234, 1.0, v234\n" \
    "v_add_f32 v235, 1.0, v235\n" \
    "v_rcp_f32 v232, v232\n" \
    "v_rcp_f32 v233, v233\n" \
    "v_rcp_f32 v234, v234\n" \
    "v_rcp_f32 v235, v235\n" \
    "s_nop 0\n" \
    "v_mul_f32 v108, v108, v232\n" \
    "v_mul_f32 v109, v109, v233\n" \
    "v_mul_f32 v110, v110, v234\n" \
    "v_mul_f32 v111, v111, v235\n" \
    "v_cvt_pk_bf16_f32 v230, v108, v109\n" \
    "v_cvt_pk_bf16_f32 v231, v110, v111\n" \
    "global_store_dwordx4 v140, v[228:231], s[70:71] sc1\n" \
    "s_add_u32 s70, s70, 57344\n" \
    "s_addc_u32 s71, s71, 0\n" \
    "v_mul_f32 v232, s76, v16\n" \
    "v_mul_f32 v233, s76, v17\n" \
    "v_mul_f32 v234, s76, v18\n" \
    "v_mul_f32 v235, s76, v19\n" \
    "v_exp_f32 v232, v232\n" \
    "v_exp_f32 v233, v233\n" \
    "v_exp_f32 v234, v234\n" \
    "v_exp_f32 v235, v235\n" \
    "v_mul_f32 v80, v80, v16\n" \
    "v_mul_f32 v81, v81, v17\n" \
    "v_mul_f32 v82, v82, v18\n" \
    "v_mul_f32 v83, v83, v19\n" \
    "v_add_f32 v232, 1.0, v232\n" \
    "v_add_f32 v233, 1.0, v233\n" \
    "v_add_f32 v234, 1.0, v234\n" \
    "v_add_f32 v235, 1.0, v235\n" \
    "v_rcp_f32 v232, v232\n" \
    "v_rcp_f32 v233, v233\n" \
    "v_rcp_f32 v234, v234\n" \
    "v_rcp_f32 v235, v235\n" \
    "s_nop 0\n" \
    "v_mul_f32 v80, v80, v232\n" \
    "v_mul_f32 v81, v81, v233\n" \
    "v_mul_f32 v82, v82, v234\n" \
    "v_mul_f32 v83, v83, v235\n" \
    "v_cvt_pk_bf16_f32 v224, v80, v81\n" \
    "v_cvt_pk_bf16_f32 v225, v82, v83\n" \
    "v_mul_f32 v232, s76, v48\n" \
    "v_mul_f32 v233, s76, v49\n" \
    "v_mul_f32 v234, s76, v50\n" \
    "v_mul_f32 v235, s76, v51\n" \
    "v_exp_f32 v232, v232\n" \
    "v_exp_f32 v233, v233\n" \
    "v_exp_f32 v234, v234\n" \
    "v_exp_f32 v235, v235\n" \
    "v_mul_f32 v112, v112, v48\n" \
    "v_mul_f32 v113, v113, v49\n" \
    "v_mul_f32 v114, v114, v50\n" \
    "v_mul_f32 v115, v115, v51\n" \
    "v_add_f32 v232, 1.0, v232\n" \
    "v_add_f32 v233, 1.0, v233\n" \
    "v_add_f32 v234, 1.0, v234\n" \
    "v_add_f32 v235, 1.0, v235\n" \
    "v_rcp_f32 v232, v232\n" \
    "v_rcp_f32 v233, v233\n" \
    "v_rcp_f32 v234, v234\n" \
    "v_rcp_f32 v235, v235\n" \
    "s_nop 0\n" \
    "v_mul_f32 v112, v112, v232\n" \
    "v_mul_f32 v113, v113, v233\n" \
    "v_mul_f32 v114, v114, v234\n" \
    "v_mul_f32 v115, v115, v235\n" \
    "v_cvt_pk_bf16_f32 v226, v112, v113\n" \
    "v_cvt_pk_bf16_f32 v227, v114, v115\n" \
    "global_store_dwordx4 v140, v[224:227], s[70:71] sc1\n" \
    "s_add_u32 s70, s70, 57344\n" \
    "s_addc_u32 s71, s71, 0\n" \
    "v_mul_f32 v232, s76, v20\n" \
    "v_mul_f32 v233, s76, v21\n" \
    "v_mul_f32 v234, s76, v22\n" \
    "v_mul_f32 v235, s76, v23\n" \
    "v_exp_f32 v232, v232\n" \
    "v_exp_f32 v233, v233\n" \
    "v_exp_f32 v234, v234\n" \
    "v_exp_f32 v235, v235\n" \
    "v_mul_f32 v84, v84, v20\n" \
    "v_mul_f32 v85, v85, v21\n" \
    "v_mul_f32 v86, v86, v22\n" \
    "v_mul_f32 v87, v87, v23\n" \
    "v_add_f32 v232, 1.0, v232\n" \
    "v_add_f32 v233, 1.0, v233\n" \
    "v_add_f32 v234, 1.0, v234\n" \
    "v_add_f32 v235, 1.0, v235\n" \
    "v_rcp_f32 v232, v232\n" \
    "v_rcp_f32 v233, v233\n" \
    "v_rcp_f32 v234, v234\n" \
    "v_rcp_f32 v235, v235\n" \
    "s_nop 0\n" \
    "v_mul_f32 v84, v84, v232\n" \
    "v_mul_f32 v85, v85, v233\n" \
    "v_mul_f32 v86, v86, v234\n" \
    "v_mul_f32 v87, v87, v235\n" \
    "v_cvt_pk_bf16_f32 v228, v84, v85\n" \
    "v_cvt_pk_bf16_f32 v229, v86, v87\n" \
    "v_mul_f32 v232, s76, v52\n" \
    "v_mul_f32 v233, s76, v53\n" \
    "v_mul_f32 v234, s76, v54\n" \
    "v_mul_f32 v235, s76, v55\n" \
    "v_exp_f32 v232, v232\n" \
    "v_exp_f32 v233, v233\n" \
    "v_exp_f32 v234, v234\n" \
    "v_exp_f32 v235, v235\n" \
    "v_mul_f32 v116, v116, v52\n" \
    "v_mul_f32 v117, v117, v53\n" \
    "v_mul_f32 v118, v118, v54\n" \
    "v_mul_f32 v119, v119, v55\n" \
    "v_add_f32 v232, 1.0, v232\n" \
    "v_add_f32 v233, 1.0, v233\n" \
    "v_add_f32 v234, 1.0, v234\n" \
    "v_add_f32 v235, 1.0, v235\n" \
    "v_rcp_f32 v232, v232\n" \
    "v_rcp_f32 v233, v233\n" \
    "v_rcp_f32 v234, v234\n" \
    "v_rcp_f32 v235, v235\n" \
    "s_nop 0\n" \
    "v_mul_f32 v116, v116, v232\n" \
    "v_mul_f32 v117, v117, v233\n" \
    "v_mul_f32 v118, v118, v234\n" \
    "v_mul_f32 v119, v119, v235\n" \
    "v_cvt_pk_bf16_f32 v230, v116, v117\n" \
    "v_cvt_pk_bf16_f32 v231, v118, v119\n" \
    "global_store_dwordx4 v140, v[228:231], s[70:71] sc1\n" \
    "s_add_u32 s70, s70, 57344\n" \
    "s_addc_u32 s71, s71, 0\n" \
    "v_mul_f32 v232, s76, v24\n" \
    "v_mul_f32 v233, s76, v25\n" \
    "v_mul_f32 v234, s76, v26\n" \
    "v_mul_f32 v235, s76, v27\n" \
    "v_exp_f32 v232, v232\n" \
    "v_exp_f32 v233, v233\n" \
    "v_exp_f32 v234, v234\n" \
    "v_exp_f32 v235, v235\n" \
    "v_mul_f32 v88, v88, v24\n" \
    "v_mul_f32 v89, v89, v25\n" \
    "v_mul_f32 v90, v90, v26\n" \
    "v_mul_f32 v91, v91, v27\n" \
    "v_add_f32 v232, 1.0, v232\n" \
    "v_add_f32 v233, 1.0, v233\n" \
    "v_add_f32 v234, 1.0, v234\n" \
    "v_add_f32 v235, 1.0, v235\n" \
    "v_rcp_f32 v232, v232\n" \
    "v_rcp_f32 v233, v233\n" \
    "v_rcp_f32 v234, v234\n" \
    "v_rcp_f32 v235, v235\n" \
    "s_nop 0\n" \
    "v_mul_f32 v88, v88, v232\n" \
    "v_mul_f32 v89, v89, v233\n" \
    "v_mul_f32 v90, v90, v234\n" \
    "v_mul_f32 v91, v91, v235\n" \
    "v_cvt_pk_bf16_f32 v224, v88, v89\n" \
    "v_cvt_pk_bf16_f32 v225, v90, v91\n" \
    "v_mul_f32 v232, s76, v56\n" \
    "v_mul_f32 v233, s76, v57\n" \
    "v_mul_f32 v234, s76, v58\n" \
    "v_mul_f32 v235, s76, v59\n" \
    "v_exp_f32 v232, v232\n" \
    "v_exp_f32 v233, v233\n" \
    "v_exp_f32 v234, v234\n" \
    "v_exp_f32 v235, v235\n" \
    "v_mul_f32 v120, v120, v56\n" \
    "v_mul_f32 v121, v121, v57\n" \
    "v_mul_f32 v122, v122, v58\n" \
    "v_mul_f32 v123, v123, v59\n" \
    "v_add_f32 v232, 1.0, v232\n" \
    "v_add_f32 v233, 1.0, v233\n" \
    "v_add_f32 v234, 1.0, v234\n" \
    "v_add_f32 v235, 1.0, v235\n" \
    "v_rcp_f32 v232, v232\n" \
    "v_rcp_f32 v233, v233\n" \
    "v_rcp_f32 v234, v234\n" \
    "v_rcp_f32 v235, v235\n" \
    "s_nop 0\n" \
    "v_mul_f32 v120, v120, v232\n" \
    "v_mul_f32 v121, v121, v233\n" \
    "v_mul_f32 v122, v122, v234\n" \
    "v_mul_f32 v123, v123, v235\n" \
    "v_cvt_pk_bf16_f32 v226, v120, v121\n" \
    "v_cvt_pk_bf16_f32 v227, v122, v123\n" \
    "global_store_dwordx4 v140, v[224:227], s[70:71] sc1\n" \
    "s_add_u32 s70, s70, 57344\n" \
    "s_addc_u32 s71, s71, 0\n" \
    "v_mul_f32 v232, s76, v28\n" \
    "v_mul_f32 v233, s76, v29\n" \
    "v_mul_f32 v234, s76, v30\n" \
    "v_mul_f32 v235, s76, v31\n" \
    "v_exp_f32 v232, v232\n" \
    "v_exp_f32 v233, v233\n" \
    "v_exp_f32 v234, v234\n" \
    "v_exp_f32 v235, v235\n" \
    "v_mul_f32 v92, v92, v28\n" \
    "v_mul_f32 v93, v93, v29\n" \
    "v_mul_f32 v94, v94, v30\n" \
    "v_mul_f32 v95, v95, v31\n" \
    "v_add_f32 v232, 1.0, v232\n" \
    "v_add_f32 v233, 1.0, v233\n" \
    "v_add_f32 v234, 1.0, v234\n" \
    "v_add_f32 v235, 1.0, v235\n" \
    "v_rcp_f32 v232, v232\n" \
    "v_rcp_f32 v233, v233\n" \
    "v_rcp_f32 v234, v234\n" \
    "v_rcp_f32 v235, v235\n" \
    "s_nop 0\n" \
    "v_mul_f32 v92, v92, v232\n" \
    "v_mul_f32 v93, v93, v233\n" \
    "v_mul_f32 v94, v94, v234\n" \
    "v_mul_f32 v95, v95, v235\n" \
    "v_cvt_pk_bf16_f32 v228, v92, v93\n" \
    "v_cvt_pk_bf16_f32 v229, v94, v95\n" \
    "v_mul_f32 v232, s76, v60\n" \
    "v_mul_f32 v233, s76, v61\n" \
    "v_mul_f32 v234, s76, v62\n" \
    "v_mul_f32 v235, s76, v63\n" \
    "v_exp_f32 v232, v232\n" \
    "v_exp_f32 v233, v233\n" \
    "v_exp_f32 v234, v234\n" \
    "v_exp_f32 v235, v235\n" \
    "v_mul_f32 v124, v124, v60\n" \
    "v_mul_f32 v125, v125, v61\n" \
    "v_mul_f32 v126, v126, v62\n" \
    "v_mul_f32 v127, v127, v63\n" \
    "v_add_f32 v232, 1.0, v232\n" \
    "v_add_f32 v233, 1.0, v233\n" \
    "v_add_f32 v234, 1.0, v234\n" \
    "v_add_f32 v235, 1.0, v235\n" \
    "v_rcp_f32 v232, v232\n" \
    "v_rcp_f32 v233, v233\n" \
    "v_rcp_f32 v234, v234\n" \
    "v_rcp_f32 v235, v235\n" \
    "s_nop 0\n" \
    "v_mul_f32 v124, v124, v232\n" \
    "v_mul_f32 v125, v125, v233\n" \
    "v_mul_f32 v126, v126, v234\n" \
    "v_mul_f32 v127, v127, v235\n" \
    "v_cvt_pk_bf16_f32 v230, v124, v125\n" \
    "v_cvt_pk_bf16_f32 v231, v126, v127\n" \
    "global_store_dwordx4 v140, v[228:231], s[70:71] sc1\n" \
    "s_waitcnt vmcnt(0)\n" \
    "ED_%=:\n" \
    "s_lshl_b32 s58, s55, 2\n" \
    "s_add_u32 s72, s50, s58\n" \
    "s_addc_u32 s73, s51, 0\n" \
    "s_mov_b32 s77, 1\n" \
    "s_add_u32 s55, s55, 2\n" \
    "s_add_u32 s57, s55, 2\n" \
    "s_min_u32 s57, s57, 15\n" \
    "s_lshl_b32 s57, s57, 17\n" \
    "s_lshl_b32 s58, s53, 15\n" \
    "s_add_u32 s57, s57, s58\n" \
    "s_add_u32 s60, s44, s57\n" \
    "s_addc_u32 s61, s45, 0\n" \
    "s_add_u32 s62, s60, 16384\n" \
    "s_addc_u32 s63, s61, 0\n" \
    "s_add_u32 s56, s56, 1\n" \
    "s_cmp_lt_u32 s56, 8\n" \
    "s_cbranch_scc1 TASK_%=\n" \
    "s_waitcnt vmcnt(0) lgkmcnt(0)\n" \
    "s_barrier\n" \
    "v_mov_b32 v141, 0\n" \
    "s_mov_b64 exec, 1\n" \
    "s_cmp_lg_u32 s53, 0\n" \
    "s_cbranch_scc1 END_%=\n" \
    "global_store_dword v141, v141, s[72:73] sc1\n" \
    "END_%=:\n" \
    "s_mov_b64 exec, -1\n" \
    "s_waitcnt vmcnt(0)\n"
#define GEMM_CLOBBERS "v0", "v1", "v2", "v3", "v4", "v5", "v6", "v7", "v8", "v9", "v10", "v11", "v12", "v13", "v14", "v15", "v16", "v17", "v18", "v19", "v20", "v21", "v22", "v23", "v24", "v25", "v26", "v27", "v28", "v29", "v30", "v31", "v32", "v33", "v34", "v35", "v36", "v37", "v38", "v39", "v40", "v41", "v42", "v43", "v44", "v45", "v46", "v47", "v48", "v49", "v50", "v51", "v52", "v53", "v54", "v55", "v56", "v57", "v58", "v59", "v60", "v61", "v62", "v63", "v64", "v65", "v66", "v67", "v68", "v69", "v70", "v71", "v72", "v73", "v74", "v75", "v76", "v77", "v78", "v79", "v80", "v81", "v82", "v83", "v84", "v85", "v86", "v87", "v88", "v89", "v90", "v91", "v92", "v93", "v94", "v95", "v96", "v97", "v98", "v99", "v100", "v101", "v102", "v103", "v104", "v105", "v106", "v107", "v108", "v109", "v110", "v111", "v112", "v113", "v114", "v115", "v116", "v117", "v118", "v119", "v120", "v121", "v122", "v123", "v124", "v125", "v126", "v127", "v128", "v129", "v130", "v131", "v132", "v133", "v134", "v135", "v136", "v137", "v138", "v139", "v140", "v141", "v142", "v143", "v144", "v145", "v146", "v147", "v148", "v149", "v150", "v151", "v152", "v153", "v154", "v155", "v156", "v157", "v158", "v159", "v160", "v161", "v162", "v163", "v164", "v165", "v166", "v167", "v168", "v169", "v170", "v171", "v172", "v173", "v174", "v175", "v176", "v177", "v178", "v179", "v180", "v181", "v182", "v183", "v184", "v185", "v186", "v187", "v188", "v189", "v190", "v191", "v192", "v193", "v194", "v195", "v196", "v197", "v198", "v199", "v200", "v201", "v202", "v203", "v204", "v205", "v206", "v207", "v208", "v209", "v210", "v211", "v212", "v213", "v214", "v215", "v216", "v217", "v218", "v219", "v220", "v221", "v222", "v223", "v224", "v225", "v226", "v227", "v228", "v229", "v230", "v231", "v232", "v233", "v234", "v235", "v236", "v237", "v238", "v239", "v240", "v241", "v242", "v243", "v244", "v245", "v246", "v247", "v248", "v249", "v250", "v251", "v252", "v253", "v254", "v255", "a0", "a1", "a2", "a3", "a4", "a5", "a6", "a7", "a8", "a9", "a10", "a11", "a12", "a13", "a14", "a15", "a16", "a17", "a18", "a19", "a20", "a21", "a22", "a23", "a24", "a25", "a26", "a27", "a28", "a29", "a30", "a31", "a32", "a33", "a34", "a35", "a36", "a37", "a38", "a39", "a40", "a41", "a42", "a43", "a44", "a45", "a46", "a47", "a48", "a49", "a50", "a51", "a52", "a53", "a54", "a55", "a56", "a57", "a58", "a59", "a60", "a61", "a62", "a63", "a64", "a65", "a66", "a67", "a68", "a69", "a70", "a71", "a72", "a73", "a74", "a75", "a76", "a77", "a78", "a79", "a80", "a81", "a82", "a83", "a84", "a85", "a86", "a87", "a88", "a89", "a90", "a91", "a92", "a93", "a94", "a95", "a96", "a97", "a98", "a99", "a100", "a101", "a102", "a103", "a104", "a105", "a106", "a107", "a108", "a109", "a110", "a111", "a112", "a113", "a114", "a115", "a116", "a117", "a118", "a119", "a120", "a121", "a122", "a123", "a124", "a125", "a126", "a127", "a128", "a129", "a130", "a131", "a132", "a133", "a134", "a135", "a136", "a137", "a138", "a139", "a140", "a141", "a142", "a143", "a144", "a145", "a146", "a147", "a148", "a149", "a150", "a151", "a152", "a153", "a154", "a155", "a156", "a157", "a158", "a159", "a160", "a161", "a162", "a163", "a164", "a165", "a166", "a167", "a168", "a169", "a170", "a171", "a172", "a173", "a174", "a175", "a176", "a177", "a178", "a179", "a180", "a181", "a182", "a183", "a184", "a185", "a186", "a187", "a188", "a189", "a190", "a191", "a192", "a193", "a194", "a195", "a196", "a197", "a198", "a199", "a200", "a201", "a202", "a203", "a204", "a205", "a206", "a207", "a208", "a209", "a210", "a211", "a212", "a213", "a214", "a215", "a216", "a217", "a218", "a219", "a220", "a221", "a222", "a223", "a224", "a225", "a226", "a227", "a228", "a229", "a230", "a231", "a232", "a233", "a234", "a235", "a236", "a237", "a238", "a239", "a240", "a241", "a242", "a243", "a244", "a245", "a246", "a247", "a248", "a249", "a250", "a251", "a252", "a253", "a254", "a255", "s40", "s41", "s42", "s43", "s44", "s45", "s46", "s47", "s48", "s49", "s50", "s51", "s52", "s53", "s54", "s55", "s56", "s57", "s58", "s59", "s60", "s61", "s62", "s63", "s64", "s65", "s66", "s67", "s68", "s69", "s70", "s71", "s72", "s73", "s74", "s75", "s76", "s77", "s78", "s79", "s80", "s81", "s82", "s83", "s84", "s85", "s86", "s87", "s88", "s89", "s90", "s91", "s92", "s93", "s94", "s95", "s96", "s97", "s98", "s99", "vcc", "scc", "memory"

#define ATT_ASM \
    "s_mov_b64 s[40:41], %1\n" \
    "s_mov_b64 s[42:43], %2\n" \
    "s_mov_b64 s[44:45], %3\n" \
    "s_mov_b64 s[46:47], %4\n" \
    "s_mov_b64 s[48:49], %5\n" \
    "s_mov_b64 s[50:51], %6\n" \
    "s_mov_b64 s[52:53], %7\n" \
    "s_mov_b64 s[54:55], %8\n" \
    "s_mov_b64 s[56:57], %9\n" \
    "s_mov_b64 s[58:59], %10\n" \
    "s_mov_b32 s60, %11\n" \
    "s_mov_b32 s61, %12\n" \
    "s_mov_b32 s62, %13\n" \
    "s_mov_b32 s67, 0x3e0293ee\n" \
    "s_mov_b32 s68, 0xbe0293ee\n" \
    "s_mov_b32 s69, 0xbfb8aa3b\n" \
    "s_mov_b32 s66, 0\n" \
    "v_mbcnt_lo_u32_b32 v0, -1, 0\n" \
    "v_mbcnt_hi_u32_b32 v0, -1, v0\n" \
    "v_and_b32 v1, 15, v0\n" \
    "v_lshrrev_b32 v2, 4, v0\n" \
    "v_lshlrev_b32 v180, 4, v0\n" \
    "v_lshl_add_u32 v3, v2, 2, 0\n" \
    "v_cmp_le_u32 s[70:71], v3, v1\n" \
    "v_lshl_add_u32 v3, v2, 2, 1\n" \
    "v_cmp_le_u32 s[72:73], v3, v1\n" \
    "v_lshl_add_u32 v3, v2, 2, 2\n" \
    "v_cmp_le_u32 s[74:75], v3, v1\n" \
    "v_lshl_add_u32 v3, v2, 2, 3\n" \
    "v_cmp_le_u32 s[76:77], v3, v1\n" \
    "v_lshrrev_b32 v5, 2, v1\n" \
    "v_lshl_add_u32 v5, v2, 2, v5\n" \
    "v_lshlrev_b32 v5, 4, v5\n" \
    "v_and_b32 v6, 1, v1\n" \
    "v_lshl_add_u32 v5, v6, 3, v5\n" \
    "v_add_u32 v181, s62, v5\n" \
    "v_and_b32 v6, 1, v2\n" \
    "v_lshlrev_b32 v5, 4, v1\n" \
    "v_lshl_add_u32 v182, v6, 3, v5\n" \
    "v_mov_b32 v185, 1\n" \
    "v_mov_b32 v186, 0\n" \
    "global_load_dwordx4 v[100:103], v180, s[54:55]\n" \
    "s_lshl_b32 s63, s61, 18\n" \
    "s_add_u32 s40, s40, s63\n" \
    "s_addc_u32 s41, s41, 0\n" \
    "s_add_u32 s42, s42, s63\n" \
    "s_addc_u32 s43, s43, 0\n" \
    "s_add_u32 s44, s44, s63\n" \
    "s_addc_u32 s45, s45, 0\n" \
    "s_lshl_b32 s63, s61, 16\n" \
    "s_add_u32 s46, s46, s63\n" \
    "s_addc_u32 s47, s47, 0\n" \
    "s_add_u32 s48, s48, s63\n" \
    "s_addc_u32 s49, s49, 0\n" \
    "s_lshl_b32 s63, s61, 18\n" \
    "s_add_u32 s50, s50, s63\n" \
    "s_addc_u32 s51, s51, 0\n" \
    "s_lshl_b32 s63, s61, 5\n" \
    "s_add_u32 s63, s63, s60\n" \
    "s_lshl_b32 s64, s63, 10\n" \
    "s_add_u32 s52, s52, s64\n" \
    "s_addc_u32 s53, s53, 0\n" \
    "s_lshl_b32 s64, s63, 2\n" \
    "s_add_u32 s56, s56, s64\n" \
    "s_addc_u32 s57, s57, 0\n" \
    "s_waitcnt vmcnt(0)\n" \
    "SEG_%=:\n" \
    "s_lshr_b32 s63, s61, 2\n" \
    "s_lshl_b32 s63, s63, 2\n" \
    "s_lshl_b32 s64, s60, 6\n" \
    "s_add_u32 s64, s64, s63\n" \
    "s_add_u32 s64, s64, 16384\n" \
    "v_mov_b32 v166, s64\n" \
    "s_lshr_b32 s64, s60, 1\n" \
    "s_lshl_b32 s64, s64, 6\n" \
    "s_add_u32 s64, s64, s63\n" \
    "s_add_u32 s64, s64, 20480\n" \
    "v_mov_b32 v167, s64\n" \
    "s_mov_b32 vcc_lo, 0x55555555\n" \
    "s_mov_b32 vcc_hi, 0x55555555\n" \
    "v_cndmask_b32 v183, v167, v166, vcc\n" \
    "s_lshl_b32 s64, s60, 9\n" \
    "s_lshl_b32 s63, s61, 2\n" \
    "s_add_u32 s64, s64, s63\n" \
    "v_mov_b32 v166, s64\n" \
    "v_add_u32 v167, 256, v166\n" \
    "v_cndmask_b32 v184, v167, v166, vcc\n" \
    "s_mov_b32 s65, 0x80000\n" \
    "P1_%=:\n" \
    "global_load_dword v166, v183, s[58:59] sc1\n" \
    "s_waitcnt vmcnt(0)\n" \
    "v_cmp_ne_u32 vcc, 0, v166\n" \
    "s_nop 1\n" \
    "s_cbranch_vccz P1_ok_%=\n" \
    "s_sleep 2\n" \
    "s_sub_u32 s65, s65, 1\n" \
    "s_cmp_lg_u32 s65, 0\n" \
    "s_cbranch_scc1 P1_%=\n" \
    "s_mov_b32 s66, 3\n" \
    "s_branch FIN_%=\n" \
    "P1_ok_%=:\n" \
    "global_load_dwordx4 v[0:3], v180, s[40:41] offset:0 sc1\n" \
    "global_load_dwordx4 v[4:7], v180, s[40:41] offset:1024 sc1\n" \
    "global_load_dwordx4 v[8:11], v180, s[40:41] offset:2048 sc1\n" \
    "global_load_dwordx4 v[12:15], v180, s[40:41] offset:3072 sc1\n" \
    "s_add_u32 s78, s40, 4096\n" \
    "s_addc_u32 s79, s41, 0\n" \
    "global_load_dwordx4 v[16:19], v180, s[78:79] offset:0 sc1\n" \
    "global_load_dwordx4 v[20:23], v180, s[78:79] offset:1024 sc1\n" \
    "global_load_dwordx4 v[24:27], v180, s[78:79] offset:2048 sc1\n" \
    "global_load_dwordx4 v[28:31], v180, s[78:79] offset:3072 sc1\n" \
    "global_load_dwordx4 v[32:35], v180, s[42:43] offset:0 sc1\n" \
    "global_load_dwordx4 v[36:39], v180, s[42:43] offset:1024 sc1\n" \
    "global_load_dwordx4 v[40:43], v180, s[42:43] offset:2048 sc1\n" \
    "global_load_dwordx4 v[44:47], v180, s[42:43] offset:3072 sc1\n" \
    "s_add_u32 s80, s42, 4096\n" \
    "s_addc_u32 s81, s43, 0\n" \
    "global_load_dwordx4 v[48:51], v180, s[80:81] offset:0 sc1\n" \
    "global_load_dwordx4 v[52:55], v180, s[80:81] offset:1024 sc1\n" \
    "global_load_dwordx4 v[56:59], v180, s[80:81] offset:2048 sc1\n" \
    "global_load_dwordx4 v[60:63], v180, s[80:81] offset:3072 sc1\n" \
    "global_load_dwordx4 v[64:67], v180, s[44:45] offset:0 sc1\n" \
    "global_load_dwordx4 v[68:71], v180, s[44:45] offset:1024 sc1\n" \
    "global_load_dwordx4 v[72:75], v180, s[44:45] offset:2048 sc1\n" \
    "global_load_dwordx4 v[76:79], v180, s[44:45] offset:3072 sc1\n" \
    "s_add_u32 s80, s44, 4096\n" \
    "s_addc_u32 s81, s45, 0\n" \
    "global_load_dwordx4 v[80:83], v180, s[80:81] offset:0 sc1\n" \
    "global_load_dwordx4 v[84:87], v180, s[80:81] offset:1024 sc1\n" \
    "global_load_dwordx4 v[88:91], v180, s[80:81] offset:2048 sc1\n" \
    "global_load_dwordx4 v[92:95], v180, s[80:81] offset:3072 sc1\n" \
    "global_load_dwordx4 v[96:99], v180, s[52:53]\n" \
    "s_waitcnt vmcnt(9)\n" \
    "v_mfma_f32_16x16x32_bf16 v[136:139], v[32:35], v[0:3], 0\n" \
    "v_mfma_f32_16x16x32_bf16 v[140:143], v[32:35], v[16:19], 0\n" \
    "v_mfma_f32_16x16x32_bf16 v[144:147], v[48:51], v[16:19], 0\n" \
    "v_mfma_f32_16x16x32_bf16 v[136:139], v[36:39], v[4:7], v[136:139]\n" \
    "v_mfma_f32_16x16x32_bf16 v[140:143], v[36:39], v[20:23], v[140:143]\n" \
    "v_mfma_f32_16x16x32_bf16 v[144:147], v[52:55], v[20:23], v[144:147]\n" \
    "v_mfma_f32_16x16x32_bf16 v[136:139], v[40:43], v[8:11], v[136:139]\n" \
    "v_mfma_f32_16x16x32_bf16 v[140:143], v[40:43], v[24:27], v[140:143]\n" \
    "v_mfma_f32_16x16x32_bf16 v[144:147], v[56:59], v[24:27], v[144:147]\n" \
    "v_mfma_f32_16x16x32_bf16 v[136:139], v[44:47], v[12:15], v[136:139]\n" \
    "v_mfma_f32_16x16x32_bf16 v[140:143], v[44:47], v[28:31], v[140:143]\n" \
    "v_mfma_f32_16x16x32_bf16 v[144:147], v[60:63], v[28:31], v[144:147]\n" \
    "s_waitcnt vmcnt(0)\n" \
    "v_mov_b32 v166, 0\n" \
    "v_dot2c_f32_bf16 v166, v96, v100\n" \
    "v_dot2c_f32_bf16 v166, v97, v101\n" \
    "v_dot2c_f32_bf16 v166, v98, v102\n" \
    "v_dot2c_f32_bf16 v166, v99, v103\n" \
    "s_nop 3\n" \
    "v_mov_b32 v167, v166\n" \
    "s_nop 1\n" \
    "v_permlane16_swap_b32 v166, v167\n" \
    "v_add_f32 v166, v166, v167\n" \
    "v_mov_b32 v167, v166\n" \
    "s_nop 1\n" \
    "v_permlane32_swap_b32 v166, v167\n" \
    "v_add_f32 v166, v166, v167\n" \
    "s_nop 1\n" \
    "v_add_f32_dpp v166, v166, v166 row_ror:8 row_mask:0xf bank_mask:0xf\n" \
    "s_nop 1\n" \
    "v_add_f32_dpp v166, v166, v166 row_ror:4 row_mask:0xf bank_mask:0xf\n" \
    "s_nop 1\n" \
    "v_add_f32_dpp v166, v166, v166 row_ror:2 row_mask:0xf bank_mask:0xf\n" \
    "s_nop 1\n" \
    "v_add_f32_dpp v166, v166, v166 row_ror:1 row_mask:0xf bank_mask:0xf\n" \
    "v_mul_f32 v166, s69, v166\n" \
    "v_exp_f32 v166, v166\n" \
    "s_nop 0\n" \
    "v_add_f32 v166, 1.0, v166\n" \
    "v_rcp_f32 v166, v166\n" \
    "s_mov_b64 exec, 1\n" \
    "global_store_dword v186, v166, s[56:57] sc1\n" \
    "s_mov_b64 exec, -1\n" \
    "s_mov_b32 s65, 0x80000\n" \
    "P2_%=:\n" \
    "global_load_dword v166, v184, s[58:59] sc1\n" \
    "s_waitcnt vmcnt(0)\n" \
    "v_cmp_ne_u32 vcc, 0, v166\n" \
    "s_nop 1\n" \
    "s_cbranch_vccz P2_ok_%=\n" \
    "s_sleep 2\n" \
    "s_sub_u32 s65, s65, 1\n" \
    "s_cmp_lg_u32 s65, 0\n" \
    "s_cbranch_scc1 P2_%=\n" \
    "s_mov_b32 s66, 4\n" \
    "s_branch FIN_%=\n" \
    "P2_ok_%=:\n" \
    "s_mov_b32 m0, s62\n" \
    "s_nop 0\n" \
    "global_load_lds_dwordx4 v180, s[46:47] sc1\n" \
    "global_load_lds_dwordx4 v180, s[46:47] offset:1024 sc1\n" \
    "global_load_dwordx2 v[120:121], v182, s[48:49] offset:0 sc1\n" \
    "global_load_dwordx2 v[122:123], v182, s[48:49] offset:256 sc1\n" \
    "global_load_dwordx2 v[124:125], v182, s[48:49] offset:512 sc1\n" \
    "global_load_dwordx2 v[126:127], v182, s[48:49] offset:768 sc1\n" \
    "global_load_dwordx2 v[128:129], v182, s[48:49] offset:1024 sc1\n" \
    "global_load_dwordx2 v[130:131], v182, s[48:49] offset:1280 sc1\n" \
    "global_load_dwordx2 v[132:133], v182, s[48:49] offset:1536 sc1\n" \
    "global_load_dwordx2 v[134:135], v182, s[48:49] offset:1792 sc1\n" \
    "s_waitcnt vmcnt(8)\n" \
    "ds_read_b64_tr_b16 v[104:105], v181 offset:0\n" \
    "ds_read_b64_tr_b16 v[106:107], v181 offset:256\n" \
    "ds_read_b64_tr_b16 v[108:109], v181 offset:512\n" \
    "ds_read_b64_tr_b16 v[110:111], v181 offset:768\n" \
    "ds_read_b64_tr_b16 v[112:113], v181 offset:1024\n" \
    "ds_read_b64_tr_b16 v[114:115], v181 offset:1280\n" \
    "ds_read_b64_tr_b16 v[116:117], v181 offset:1536\n" \
    "ds_read_b64_tr_b16 v[118:119], v181 offset:1792\n" \
    "s_waitcnt lgkmcnt(6)\n" \
    "v_mfma_f32_16x16x32_bf16 v[148:151], v[104:107], v[0:3], 0\n" \
    "v_mfma_f32_16x16x32_bf16 v[152:155], v[104:107], v[16:19], 0\n" \
    "s_waitcnt lgkmcnt(4)\n" \
    "v_mfma_f32_16x16x32_bf16 v[148:151], v[108:111], v[4:7], v[148:151]\n" \
    "v_mfma_f32_16x16x32_bf16 v[152:155], v[108:111], v[20:23], v[152:155]\n" \
    "s_waitcnt lgkmcnt(2)\n" \
    "v_mfma_f32_16x16x32_bf16 v[148:151], v[112:115], v[8:11], v[148:151]\n" \
    "v_mfma_f32_16x16x32_bf16 v[152:155], v[112:115], v[24:27], v[152:155]\n" \
    "s_waitcnt lgkmcnt(0)\n" \
    "v_mfma_f32_16x16x32_bf16 v[148:151], v[116:119], v[12:15], v[148:151]\n" \
    "v_mfma_f32_16x16x32_bf16 v[152:155], v[116:119], v[28:31], v[152:155]\n" \
    "v_mov_b32 v173, 0xf149f2ca\n" \
    "v_cndmask_b32 v136, v173, v136, s[70:71]\n" \
    "v_cndmask_b32 v137, v173, v137, s[72:73]\n" \
    "v_cndmask_b32 v138, v173, v138, s[74:75]\n" \
    "v_cndmask_b32 v139, v173, v139, s[76:77]\n" \
    "s_nop 1\n" \
    "v_max3_f32 v166, v148, v149, v150\n" \
    "v_max3_f32 v166, v166, v151, v136\n" \
    "v_max3_f32 v166, v166, v137, v138\n" \
    "v_max_f32 v166, v166, v139\n" \
    "v_mov_b32 v172, v166\n" \
    "s_nop 1\n" \
    "v_permlane16_swap_b32 v166, v172\n" \
    "v_max_f32 v166, v166, v172\n" \
    "v_mov_b32 v172, v166\n" \
    "s_nop 1\n" \
    "v_permlane32_swap_b32 v166, v172\n" \
    "v_max_f32 v166, v166, v172\n" \
    "v_mul_f32 v166, s68, v166\n" \
    "v_add_f32 v173, -1.0, v166\n" \
    "v_fma_f32 v148, v148, s67, v173\n" \
    "v_fma_f32 v149, v149, s67, v173\n" \
    "v_fma_f32 v150, v150, s67, v173\n" \
    "v_fma_f32 v151, v151, s67, v173\n" \
    "v_fma_f32 v136, v136, s67, v166\n" \
    "v_fma_f32 v137, v137, s67, v166\n" \
    "v_fma_f32 v138, v138, s67, v166\n" \
    "v_fma_f32 v139, v139, s67, v166\n" \
    "v_exp_f32 v148, v148\n" \
    "v_exp_f32 v149, v149\n" \
    "v_exp_f32 v150, v150\n" \
    "v_exp_f32 v151, v151\n" \
    "v_exp_f32 v136, v136\n" \
    "v_exp_f32 v137, v137\n" \
    "v_exp_f32 v138, v138\n" \
    "v_exp_f32 v139, v139\n" \
    "v_add_f32 v167, v148, v149\n" \
    "v_add_f32 v167, v167, v150\n" \
    "v_add_f32 v167, v167, v151\n" \
    "v_add_f32 v167, v167, v136\n" \
    "v_add_f32 v167, v167, v137\n" \
    "v_add_f32 v167, v167, v138\n" \
    "v_add_f32 v167, v167, v139\n" \
    "v_cvt_pk_bf16_f32 v158, v148, v149\n" \
    "v_cvt_pk_bf16_f32 v159, v150, v151\n" \
    "v_cvt_pk_bf16_f32 v156, v136, v137\n" \
    "v_cvt_pk_bf16_f32 v157, v138, v139\n" \
    "v_mov_b32 v172, v167\n" \
    "s_nop 1\n" \
    "v_permlane16_swap_b32 v167, v172\n" \
    "v_add_f32 v167, v167, v172\n" \
    "v_mov_b32 v172, v167\n" \
    "s_nop 1\n" \
    "v_permlane32_swap_b32 v167, v172\n" \
    "v_add_f32 v167, v167, v172\n" \
    "v_rcp_f32 v167, v167\n" \
    "v_mov_b32 v173, 0xf149f2ca\n" \
    "v_cndmask_b32 v144, v173, v144, s[70:71]\n" \
    "v_cndmask_b32 v145, v173, v145, s[72:73]\n" \
    "v_cndmask_b32 v146, v173, v146, s[74:75]\n" \
    "v_cndmask_b32 v147, v173, v147, s[76:77]\n" \
    "v_max3_f32 v168, v152, v153, v154\n" \
    "v_max3_f32 v168, v168, v155, v140\n" \
    "v_max3_f32 v168, v168, v141, v142\n" \
    "v_max3_f32 v168, v168, v143, v144\n" \
    "v_max3_f32 v168, v168, v145, v146\n" \
    "v_max_f32 v168, v168, v147\n" \
    "v_mov_b32 v172, v168\n" \
    "s_nop 1\n" \
    "v_permlane16_swap_b32 v168, v172\n" \
    "v_max_f32 v168, v168, v172\n" \
    "v_mov_b32 v172, v168\n" \
    "s_nop 1\n" \
    "v_permlane32_swap_b32 v168, v172\n" \
    "v_max_f32 v168, v168, v172\n" \
    "v_mul_f32 v168, s68, v168\n" \
    "v_add_f32 v173, -1.0, v168\n" \
    "v_fma_f32 v152, v152, s67, v173\n" \
    "v_fma_f32 v153, v153, s67, v173\n" \
    "v_fma_f32 v154, v154, s67, v173\n" \
    "v_fma_f32 v155, v155, s67, v173\n" \
    "v_fma_f32 v140, v140, s67, v168\n" \
    "v_fma_f32 v141, v141, s67, v168\n" \
    "v_fma_f32 v142, v142, s67, v168\n" \
    "v_fma_f32 v143, v143, s67, v168\n" \
    "v_fma_f32 v144, v144, s67, v168\n" \
    "v_fma_f32 v145, v145, s67, v168\n" \
    "v_fma_f32 v146, v146, s67, v168\n" \
    "v_fma_f32 v147, v147, s67, v168\n" \
    "v_exp_f32 v152, v152\n" \
    "v_exp_f32 v153, v153\n" \
    "v_exp_f32 v154, v154\n" \
    "v_exp_f32 v155, v155\n" \
    "v_exp_f32 v140, v140\n" \
    "v_exp_f32 v141, v141\n" \
    "v_exp_f32 v142, v142\n" \
    "v_exp_f32 v143, v143\n" \
    "v_exp_f32 v144, v144\n" \
    "v_exp_f32 v145, v145\n" \
    "v_exp_f32 v146, v146\n" \
    "v_exp_f32 v147, v147\n" \
    "v_add_f32 v169, v152, v153\n" \
    "v_add_f32 v169, v169, v154\n" \
    "v_add_f32 v169, v169, v155\n" \
    "v_add_f32 v169, v169, v140\n" \
    "v_add_f32 v169, v169, v141\n" \
    "v_add_f32 v169, v169, v142\n" \
    "v_add_f32 v169, v169, v143\n" \
    "v_add_f32 v169, v169, v144\n" \
    "v_add_f32 v169, v169, v145\n" \
    "v_add_f32 v169, v169, v146\n" \
    "v_add_f32 v169, v169, v147\n" \
    "v_cvt_pk_bf16_f32 v164, v152, v153\n" \
    "v_cvt_pk_bf16_f32 v165, v154, v155\n" \
    "v_cvt_pk_bf16_f32 v160, v140, v141\n" \
    "v_cvt_pk_bf16_f32 v161, v142, v143\n" \
    "v_cvt_pk_bf16_f32 v162, v144, v145\n" \
    "v_cvt_pk_bf16_f32 v163, v146, v147\n" \
    "v_mov_b32 v172, v169\n" \
    "s_nop 1\n" \
    "v_permlane16_swap_b32 v169, v172\n" \
    "v_add_f32 v169, v169, v172\n" \
    "v_mov_b32 v172, v169\n" \
    "s_nop 1\n" \
    "v_permlane32_swap_b32 v169, v172\n" \
    "v_add_f32 v169, v169, v172\n" \
    "v_rcp_f32 v169, v169\n" \
    "s_waitcnt vmcnt(0)\n" \
    "v_mfma_f32_16x16x16_bf16 v[192:195], v[64:65], v[156:157], 0\n" \
    "v_mfma_f32_16x16x32_bf16 v[196:199], v[64:67], v[160:163], 0\n" \
    "v_mfma_f32_16x16x16_bf16 v[192:195], v[120:121], v[158:159], v[192:195]\n" \
    "v_mfma_f32_16x16x16_bf16 v[196:199], v[120:121], v[164:165], v[196:199]\n" \
    "v_mfma_f32_16x16x16_bf16 v[200:203], v[68:69], v[156:157], 0\n" \
    "v_mfma_f32_16x16x32_bf16 v[204:207], v[68:71], v[160:163], 0\n" \
    "v_mfma_f32_16x16x16_bf16 v[200:203], v[122:123], v[158:159], v[200:203]\n" \
    "v_mfma_f32_16x16x16_bf16 v[204:207], v[122:123], v[164:165], v[204:207]\n" \
    "v_mfma_f32_16x16x16_bf16 v[208:211], v[72:73], v[156:157], 0\n" \
    "v_mfma_f32_16x16x32_bf16 v[212:215], v[72:75], v[160:163], 0\n" \
    "v_mfma_f32_16x16x16_bf16 v[208:211], v[124:125], v[158:159], v[208:211]\n" \
    "v_mfma_f32_16x16x16_bf16 v[212:215], v[124:125], v[164:165], v[212:215]\n" \
    "v_mfma_f32_16x16x16_bf16 v[216:219], v[76:77], v[156:157], 0\n" \
    "v_mfma_f32_16x16x32_bf16 v[220:223], v[76:79], v[160:163], 0\n" \
    "v_mfma_f32_16x16x16_bf16 v[216:219], v[126:127], v[158:159], v[216:219]\n" \
    "v_mfma_f32_16x16x16_bf16 v[220:223], v[126:127], v[164:165], v[220:223]\n" \
    "v_mfma_f32_16x16x16_bf16 v[224:227], v[80:81], v[156:157], 0\n" \
    "v_mfma_f32_16x16x32_bf16 v[228:231], v[80:83], v[160:163], 0\n" \
    "v_mfma_f32_16x16x16_bf16 v[224:227], v[128:129], v[158:159], v[224:227]\n" \
    "v_mfma_f32_16x16x16_bf16 v[228:231], v[128:129], v[164:165], v[228:231]\n" \
    "v_mfma_f32_16x16x16_bf16 v[232:235], v[84:85], v[156:157], 0\n" \
    "v_mfma_f32_16x16x32_bf16 v[236:239], v[84:87], v[160:163], 0\n" \
    "v_mfma_f32_16x16x16_bf16 v[232:235], v[130:131], v[158:159], v[232:235]\n" \
    "v_mfma_f32_16x16x16_bf16 v[236:239], v[130:131], v[164:165], v[236:239]\n" \
    "v_mfma_f32_16x16x16_bf16 v[240:243], v[88:89], v[156:157], 0\n" \
    "v_mfma_f32_16x16x32_bf16 v[244:247], v[88:91], v[160:163], 0\n" \
    "v_mfma_f32_16x16x16_bf16 v[240:243], v[132:133], v[158:159], v[240:243]\n" \
    "v_mfma_f32_16x16x16_bf16 v[244:247], v[132:133], v[164:165], v[244:247]\n" \
    "v_mfma_f32_16x16x16_bf16 v[248:251], v[92:93], v[156:157], 0\n" \
    "v_mfma_f32_16x16x32_bf16 v[252:255], v[92:95], v[160:163], 0\n" \
    "v_mfma_f32_16x16x16_bf16 v[248:251], v[134:135], v[158:159], v[248:251]\n" \
    "v_mfma_f32_16x16x16_bf16 v[252:255], v[134:135], v[164:165], v[252:255]\n" \
    "v_mul_f32 v192, v192, v167\n" \
    "v_mul_f32 v193, v193, v167\n" \
    "v_mul_f32 v194, v194, v167\n" \
    "v_mul_f32 v195, v195, v167\n" \
    "v_cvt_pk_bf16_f32 v0, v192, v193\n" \
    "v_cvt_pk_bf16_f32 v1, v194, v195\n" \
    "v_mul_f32 v200, v200, v167\n" \
    "v_mul_f32 v201, v201, v167\n" \
    "v_mul_f32 v202, v202, v167\n" \
    "v_mul_f32 v203, v203, v167\n" \
    "v_cvt_pk_bf16_f32 v2, v200, v201\n" \
    "v_cvt_pk_bf16_f32 v3, v202, v203\n" \
    "global_store_dwordx4 v180, v[0:3], s[50:51] offset:0 sc1\n" \
    "v_mul_f32 v208, v208, v167\n" \
    "v_mul_f32 v209, v209, v167\n" \
    "v_mul_f32 v210, v210, v167\n" \
    "v_mul_f32 v211, v211, v167\n" \
    "v_cvt_pk_bf16_f32 v4, v208, v209\n" \
    "v_cvt_pk_bf16_f32 v5, v210, v211\n" \
    "v_mul_f32 v216, v216, v167\n" \
    "v_mul_f32 v217, v217, v167\n" \
    "v_mul_f32 v218, v218, v167\n" \
    "v_mul_f32 v219, v219, v167\n" \
    "v_cvt_pk_bf16_f32 v6, v216, v217\n" \
    "v_cvt_pk_bf16_f32 v7, v218, v219\n" \
    "global_store_dwordx4 v180, v[4:7], s[50:51] offset:1024 sc1\n" \
    "v_mul_f32 v224, v224, v167\n" \
    "v_mul_f32 v225, v225, v167\n" \
    "v_mul_f32 v226, v226, v167\n" \
    "v_mul_f32 v227, v227, v167\n" \
    "v_cvt_pk_bf16_f32 v8, v224, v225\n" \
    "v_cvt_pk_bf16_f32 v9, v226, v227\n" \
    "v_mul_f32 v232, v232, v167\n" \
    "v_mul_f32 v233, v233, v167\n" \
    "v_mul_f32 v234, v234, v167\n" \
    "v_mul_f32 v235, v235, v167\n" \
    "v_cvt_pk_bf16_f32 v10, v232, v233\n" \
    "v_cvt_pk_bf16_f32 v11, v234, v235\n" \
    "global_store_dwordx4 v180, v[8:11], s[50:51] offset:2048 sc1\n" \
    "v_mul_f32 v240, v240, v167\n" \
    "v_mul_f32 v241, v241, v167\n" \
    "v_mul_f32 v242, v242, v167\n" \
    "v_mul_f32 v243, v243, v167\n" \
    "v_cvt_pk_bf16_f32 v12, v240, v241\n" \
    "v_cvt_pk_bf16_f32 v13, v242, v243\n" \
    "v_mul_f32 v248, v248, v167\n" \
    "v_mul_f32 v249, v249, v167\n" \
    "v_mul_f32 v250, v250, v167\n" \
    "v_mul_f32 v251, v251, v167\n" \
    "v_cvt_pk_bf16_f32 v14, v248, v249\n" \
    "v_cvt_pk_bf16_f32 v15, v250, v251\n" \
    "global_store_dwordx4 v180, v[12:15], s[50:51] offset:3072 sc1\n" \
    "s_add_u32 s50, s50, 131072\n" \
    "s_addc_u32 s51, s51, 0\n" \
    "v_mul_f32 v196, v196, v169\n" \
    "v_mul_f32 v197, v197, v169\n" \
    "v_mul_f32 v198, v198, v169\n" \
    "v_mul_f32 v199, v199, v169\n" \
    "v_cvt_pk_bf16_f32 v16, v196, v197\n" \
    "v_cvt_pk_bf16_f32 v17, v198, v199\n" \
    "v_mul_f32 v204, v204, v169\n" \
    "v_mul_f32 v205, v205, v169\n" \
    "v_mul_f32 v206, v206, v169\n" \
    "v_mul_f32 v207, v207, v169\n" \
    "v_cvt_pk_bf16_f32 v18, v204, v205\n" \
    "v_cvt_pk_bf16_f32 v19, v206, v207\n" \
    "global_store_dwordx4 v180, v[16:19], s[50:51] offset:0 sc1\n" \
    "v_mul_f32 v212, v212, v169\n" \
    "v_mul_f32 v213, v213, v169\n" \
    "v_mul_f32 v214, v214, v169\n" \
    "v_mul_f32 v215, v215, v169\n" \
    "v_cvt_pk_bf16_f32 v20, v212, v213\n" \
    "v_cvt_pk_bf16_f32 v21, v214, v215\n" \
    "v_mul_f32 v220, v220, v169\n" \
    "v_mul_f32 v221, v221, v169\n" \
    "v_mul_f32 v222, v222, v169\n" \
    "v_mul_f32 v223, v223, v169\n" \
    "v_cvt_pk_bf16_f32 v22, v220, v221\n" \
    "v_cvt_pk_bf16_f32 v23, v222, v223\n" \
    "global_store_dwordx4 v180, v[20:23], s[50:51] offset:1024 sc1\n" \
    "v_mul_f32 v228, v228, v169\n" \
    "v_mul_f32 v229, v229, v169\n" \
    "v_mul_f32 v230, v230, v169\n" \
    "v_mul_f32 v231, v231, v169\n" \
    "v_cvt_pk_bf16_f32 v24, v228, v229\n" \
    "v_cvt_pk_bf16_f32 v25, v230, v231\n" \
    "v_mul_f32 v236, v236, v169\n" \
    "v_mul_f32 v237, v237, v169\n" \
    "v_mul_f32 v238, v238, v169\n" \
    "v_mul_f32 v239, v239, v169\n" \
    "v_cvt_pk_bf16_f32 v26, v236, v237\n" \
    "v_cvt_pk_bf16_f32 v27, v238, v239\n" \
    "global_store_dwordx4 v180, v[24:27], s[50:51] offset:2048 sc1\n" \
    "v_mul_f32 v244, v244, v169\n" \
    "v_mul_f32 v245, v245, v169\n" \
    "v_mul_f32 v246, v246, v169\n" \
    "v_mul_f32 v247, v247, v169\n" \
    "v_cvt_pk_bf16_f32 v28, v244, v245\n" \
    "v_cvt_pk_bf16_f32 v29, v246, v247\n" \
    "v_mul_f32 v252, v252, v169\n" \
    "v_mul_f32 v253, v253, v169\n" \
    "v_mul_f32 v254, v254, v169\n" \
    "v_mul_f32 v255, v255, v169\n" \
    "v_cvt_pk_bf16_f32 v30, v252, v253\n" \
    "v_cvt_pk_bf16_f32 v31, v254, v255\n" \
    "global_store_dwordx4 v180, v[28:31], s[50:51] offset:3072 sc1\n" \
    "s_add_u32 s50, s50, 393216\n" \
    "s_addc_u32 s51, s51, 0\n" \
    "s_lshl_b32 s63, s61, 7\n" \
    "s_add_u32 s63, s63, 32768\n" \
    "s_add_u32 s82, s58, s63\n" \
    "s_addc_u32 s83, s59, 0\n" \
    "s_waitcnt vmcnt(0)\n" \
    "s_mov_b64 exec, 1\n" \
    "global_atomic_add v186, v185, s[82:83] sc1\n" \
    "s_mov_b64 exec, -1\n" \
    "s_add_u32 s40, s40, 524288\n" \
    "s_addc_u32 s41, s41, 0\n" \
    "s_add_u32 s42, s42, 524288\n" \
    "s_addc_u32 s43, s43, 0\n" \
    "s_add_u32 s44, s44, 524288\n" \
    "s_addc_u32 s45, s45, 0\n" \
    "s_add_u32 s46, s46, 131072\n" \
    "s_addc_u32 s47, s47, 0\n" \
    "s_add_u32 s48, s48, 131072\n" \
    "s_addc_u32 s49, s49, 0\n" \
    "s_add_u32 s52, s52, 65536\n" \
    "s_addc_u32 s53, s53, 0\n" \
    "s_add_u32 s56, s56, 256\n" \
    "s_addc_u32 s57, s57, 0\n" \
    "s_add_u32 s61, s61, 2\n" \
    "s_cmp_lt_u32 s61, 64\n" \
    "s_cbranch_scc1 SEG_%=\n" \
    "FIN_%=:\n" \
    "s_waitcnt vmcnt(0) lgkmcnt(0)\n" \
    "s_mov_b32 %0, s66\n"
#define ATT_CLOBBERS "v0", "v1", "v2", "v3", "v4", "v5", "v6", "v7", "v8", "v9", "v10", "v11", "v12", "v13", "v14", "v15", "v16", "v17", "v18", "v19", "v20", "v21", "v22", "v23", "v24", "v25", "v26", "v27", "v28", "v29", "v30", "v31", "v32", "v33", "v34", "v35", "v36", "v37", "v38", "v39", "v40", "v41", "v42", "v43", "v44", "v45", "v46", "v47", "v48", "v49", "v50", "v51", "v52", "v53", "v54", "v55", "v56", "v57", "v58", "v59", "v60", "v61", "v62", "v63", "v64", "v65", "v66", "v67", "v68", "v69", "v70", "v71", "v72", "v73", "v74", "v75", "v76", "v77", "v78", "v79", "v80", "v81", "v82", "v83", "v84", "v85", "v86", "v87", "v88", "v89", "v90", "v91", "v92", "v93", "v94", "v95", "v96", "v97", "v98", "v99", "v100", "v101", "v102", "v103", "v104", "v105", "v106", "v107", "v108", "v109", "v110", "v111", "v112", "v113", "v114", "v115", "v116", "v117", "v118", "v119", "v120", "v121", "v122", "v123", "v124", "v125", "v126", "v127", "v128", "v129", "v130", "v131", "v132", "v133", "v134", "v135", "v136", "v137", "v138", "v139", "v140", "v141", "v142", "v143", "v144", "v145", "v146", "v147", "v148", "v149", "v150", "v151", "v152", "v153", "v154", "v155", "v156", "v157", "v158", "v159", "v160", "v161", "v162", "v163", "v164", "v165", "v166", "v167", "v168", "v169", "v170", "v171", "v172", "v173", "v174", "v175", "v176", "v177", "v178", "v179", "v180", "v181", "v182", "v183", "v184", "v185", "v186", "v187", "v188", "v189", "v190", "v191", "v192", "v193", "v194", "v195", "v196", "v197", "v198", "v199", "v200", "v201", "v202", "v203", "v204", "v205", "v206", "v207", "v208", "v209", "v210", "v211", "v212", "v213", "v214", "v215", "v216", "v217", "v218", "v219", "v220", "v221", "v222", "v223", "v224", "v225", "v226", "v227", "v228", "v229", "v230", "v231", "v232", "v233", "v234", "v235", "v236", "v237", "v238", "v239", "v240", "v241", "v242", "v243", "v244", "v245", "v246", "v247", "v248", "v249", "v250", "v251", "v252", "v253", "v254", "v255", "s40", "s41", "s42", "s43", "s44", "s45", "s46", "s47", "s48", "s49", "s50", "s51", "s52", "s53", "s54", "s55", "s56", "s57", "s58", "s59", "s60", "s61", "s62", "s63", "s64", "s65", "s66", "s67", "s68", "s69", "s70", "s71", "s72", "s73", "s74", "s75", "s76", "s77", "s78", "s79", "s80", "s81", "s82", "s83", "s84", "s85", "s86", "s87", "s88", "s89", "s90", "s91", "s92", "s93", "s94", "s95", "s96", "s97", "s98", "s99", "vcc", "scc", "memory"

#define OUT_ASM \
    "s_mov_b64 s[40:41], %1\n" \
    "s_mov_b64 s[42:43], %2\n" \
    "s_mov_b64 s[44:45], %3\n" \
    "s_mov_b64 s[46:47], %4\n" \
    "s_mov_b64 s[48:49], %5\n" \
    "s_mov_b64 s[50:51], %6\n" \
    "s_mov_b64 s[52:53], %7\n" \
    "s_mov_b32 s54, %8\n" \
    "s_mov_b32 s55, %9\n" \
    "s_mov_b32 s59, 0\n" \
    "s_mov_b32 s66, 0\n" \
    "v_mbcnt_lo_u32_b32 v0, -1, 0\n" \
    "v_mbcnt_hi_u32_b32 v0, -1, v0\n" \
    "v_and_b32 v1, 15, v0\n" \
    "v_lshrrev_b32 v2, 4, v0\n" \
    "v_lshlrev_b32 v168, 4, v0\n" \
    "v_lshrrev_b32 v3, 2, v0\n" \
    "v_and_b32 v4, 3, v0\n" \
    "v_lshlrev_b32 v4, 4, v4\n" \
    "v_lshl_add_u32 v182, v3, 10, v4\n" \
    "s_lshl_b32 s57, s54, 12\n" \
    "s_add_u32 s57, s57, 114688\n" \
    "v_lshrrev_b32 v5, 2, v1\n" \
    "v_lshl_add_u32 v5, v2, 2, v5\n" \
    "v_lshlrev_b32 v5, 6, v5\n" \
    "v_and_b32 v6, 3, v1\n" \
    "v_lshl_add_u32 v5, v6, 3, v5\n" \
    "v_add_u32 v181, s57, v5\n" \
    "; ---- Wo slice: k rows [1024 w, 1024 w + 1024) -> AGPR fragments\n" \
    "s_lshl_b32 s56, s54, 20\n" \
    "s_add_u32 s40, s40, s56\n" \
    "s_addc_u32 s41, s41, 0\n" \
    "s_add_u32 s56, s57, 0\n" \
    "s_add_u32 m0, s57, 0\n" \
    "s_nop 0\n" \
    "global_load_lds_dwordx4 v182, s[40:41]\n" \
    "s_add_u32 s40, s40, 16384\n" \
    "s_addc_u32 s41, s41, 0\n" \
    "s_add_u32 m0, s57, 1024\n" \
    "s_nop 0\n" \
    "global_load_lds_dwordx4 v182, s[40:41]\n" \
    "s_add_u32 s40, s40, 16384\n" \
    "s_addc_u32 s41, s41, 0\n" \
    "s_add_u32 m0, s57, 2048\n" \
    "s_nop 0\n" \
    "global_load_lds_dwordx4 v182, s[40:41]\n" \
    "s_add_u32 s40, s40, 16384\n" \
    "s_addc_u32 s41, s41, 0\n" \
    "s_add_u32 m0, s57, 3072\n" \
    "s_nop 0\n" \
    "global_load_lds_dwordx4 v182, s[40:41]\n" \
    "s_add_u32 s40, s40, 16384\n" \
    "s_addc_u32 s41, s41, 0\n" \
    "s_waitcnt vmcnt(0)\n" \
    "ds_read_b64_tr_b16 a[0:1], v181 offset:0\n" \
    "ds_read_b64_tr_b16 a[2:3], v181 offset:1024\n" \
    "ds_read_b64_tr_b16 a[4:5], v181 offset:32\n" \
    "ds_read_b64_tr_b16 a[6:7], v181 offset:1056\n" \
    "ds_read_b64_tr_b16 a[8:9], v181 offset:2048\n" \
    "ds_read_b64_tr_b16 a[10:11], v181 offset:3072\n" \
    "ds_read_b64_tr_b16 a[12:13], v181 offset:2080\n" \
    "ds_read_b64_tr_b16 a[14:15], v181 offset:3104\n" \
    "s_waitcnt lgkmcnt(0)\n" \
    "s_add_u32 s56, s57, 0\n" \
    "s_add_u32 m0, s57, 0\n" \
    "s_nop 0\n" \
    "global_load_lds_dwordx4 v182, s[40:41]\n" \
    "s_add_u32 s40, s40, 16384\n" \
    "s_addc_u32 s41, s41, 0\n" \
    "s_add_u32 m0, s57, 1024\n" \
    "s_nop 0\n" \
    "global_load_lds_dwordx4 v182, s[40:41]\n" \
    "s_add_u32 s40, s40, 16384\n" \
    "s_addc_u32 s41, s41, 0\n" \
    "s_add_u32 m0, s57, 2048\n" \
    "s_nop 0\n" \
    "global_load_lds_dwordx4 v182, s[40:41]\n" \
    "s_add_u32 s40, s40, 16384\n" \
    "s_addc_u32 s41, s41, 0\n" \
    "s_add_u32 m0, s57, 3072\n" \
    "s_nop 0\n" \
    "global_load_lds_dwordx4 v182, s[40:41]\n" \
    "s_add_u32 s40, s40, 16384\n" \
    "s_addc_u32 s41, s41, 0\n" \
    "s_waitcnt vmcnt(0)\n" \
    "ds_read_b64_tr_b16 a[16:17], v181 offset:0\n" \
    "ds_read_b64_tr_b16 a[18:19], v181 offset:1024\n" \
    "ds_read_b64_tr_b16 a[20:21], v181 offset:32\n" \
    "ds_read_b64_tr_b16 a[22:23], v181 offset:1056\n" \
    "ds_read_b64_tr_b16 a[24:25], v181 offset:2048\n" \
    "ds_read_b64_tr_b16 a[26:27], v181 offset:3072\n" \
    "ds_read_b64_tr_b16 a[28:29], v181 offset:2080\n" \
    "ds_read_b64_tr_b16 a[30:31], v181 offset:3104\n" \
    "s_waitcnt lgkmcnt(0)\n" \
    "s_add_u32 s56, s57, 0\n" \
    "s_add_u32 m0, s57, 0\n" \
    "s_nop 0\n" \
    "global_load_lds_dwordx4 v182, s[40:41]\n" \
    "s_add_u32 s40, s40, 16384\n" \
    "s_addc_u32 s41, s41, 0\n" \
    "s_add_u32 m0, s57, 1024\n" \
    "s_nop 0\n" \
    "global_load_lds_dwordx4 v182, s[40:41]\n" \
    "s_add_u32 s40, s40, 16384\n" \
    "s_addc_u32 s41, s41, 0\n" \
    "s_add_u32 m0, s57, 2048\n" \
    "s_nop 0\n" \
    "global_load_lds_dwordx4 v182, s[40:41]\n" \
    "s_add_u32 s40, s40, 16384\n" \
    "s_addc_u32 s41, s41, 0\n" \
    "s_add_u32 m0, s57, 3072\n" \
    "s_nop 0\n" \
    "global_load_lds_dwordx4 v182, s[40:41]\n" \
    "s_add_u32 s40, s40, 16384\n" \
    "s_addc_u32 s41, s41, 0\n" \
    "s_waitcnt vmcnt(0)\n" \
    "ds_read_b64_tr_b16 a[32:33], v181 offset:0\n" \
    "ds_read_b64_tr_b16 a[34:35], v181 offset:1024\n" \
    "ds_read_b64_tr_b16 a[36:37], v181 offset:32\n" \
    "ds_read_b64_tr_b16 a[38:39], v181 offset:1056\n" \
    "ds_read_b64_tr_b16 a[40:41], v181 offset:2048\n" \
    "ds_read_b64_tr_b16 a[42:43], v181 offset:3072\n" \
    "ds_read_b64_tr_b16 a[44:45], v181 offset:2080\n" \
    "ds_read_b64_tr_b16 a[46:47], v181 offset:3104\n" \
    "s_waitcnt lgkmcnt(0)\n" \
    "s_add_u32 s56, s57, 0\n" \
    "s_add_u32 m0, s57, 0\n" \
    "s_nop 0\n" \
    "global_load_lds_dwordx4 v182, s[40:41]\n" \
    "s_add_u32 s40, s40, 16384\n" \
    "s_addc_u32 s41, s41, 0\n" \
    "s_add_u32 m0, s57, 1024\n" \
    "s_nop 0\n" \
    "global_load_lds_dwordx4 v182, s[40:41]\n" \
    "s_add_u32 s40, s40, 16384\n" \
    "s_addc_u32 s41, s41, 0\n" \
    "s_add_u32 m0, s57, 2048\n" \
    "s_nop 0\n" \
    "global_load_lds_dwordx4 v182, s[40:41]\n" \
    "s_add_u32 s40, s40, 16384\n" \
    "s_addc_u32 s41, s41, 0\n" \
    "s_add_u32 m0, s57, 3072\n" \
    "s_nop 0\n" \
    "global_load_lds_dwordx4 v182, s[40:41]\n" \
    "s_add_u32 s40, s40, 16384\n" \
    "s_addc_u32 s41, s41, 0\n" \
    "s_waitcnt vmcnt(0)\n" \
    "ds_read_b64_tr_b16 a[48:49], v181 offset:0\n" \
    "ds_read_b64_tr_b16 a[50:51], v181 offset:1024\n" \
    "ds_read_b64_tr_b16 a[52:53], v181 offset:32\n" \
    "ds_read_b64_tr_b16 a[54:55], v181 offset:1056\n" \
    "ds_read_b64_tr_b16 a[56:57], v181 offset:2048\n" \
    "ds_read_b64_tr_b16 a[58:59], v181 offset:3072\n" \
    "ds_read_b64_tr_b16 a[60:61], v181 offset:2080\n" \
    "ds_read_b64_tr_b16 a[62:63], v181 offset:3104\n" \
    "s_waitcnt lgkmcnt(0)\n" \
    "s_add_u32 s56, s57, 0\n" \
    "s_add_u32 m0, s57, 0\n" \
    "s_nop 0\n" \
    "global_load_lds_dwordx4 v182, s[40:41]\n" \
    "s_add_u32 s40, s40, 16384\n" \
    "s_addc_u32 s41, s41, 0\n" \
    "s_add_u32 m0, s57, 1024\n" \
    "s_nop 0\n" \
    "global_load_lds_dwordx4 v182, s[40:41]\n" \
    "s_add_u32 s40, s40, 16384\n" \
    "s_addc_u32 s41, s41, 0\n" \
    "s_add_u32 m0, s57, 2048\n" \
    "s_nop 0\n" \
    "global_load_lds_dwordx4 v182, s[40:41]\n" \
    "s_add_u32 s40, s40, 16384\n" \
    "s_addc_u32 s41, s41, 0\n" \
    "s_add_u32 m0, s57, 3072\n" \
    "s_nop 0\n" \
    "global_load_lds_dwordx4 v182, s[40:41]\n" \
    "s_add_u32 s40, s40, 16384\n" \
    "s_addc_u32 s41, s41, 0\n" \
    "s_waitcnt vmcnt(0)\n" \
    "ds_read_b64_tr_b16 a[64:65], v181 offset:0\n" \
    "ds_read_b64_tr_b16 a[66:67], v181 offset:1024\n" \
    "ds_read_b64_tr_b16 a[68:69], v181 offset:32\n" \
    "ds_read_b64_tr_b16 a[70:71], v181 offset:1056\n" \
    "ds_read_b64_tr_b16 a[72:73], v181 offset:2048\n" \
    "ds_read_b64_tr_b16 a[74:75], v181 offset:3072\n" \
    "ds_read_b64_tr_b16 a[76:77], v181 offset:2080\n" \
    "ds_read_b64_tr_b16 a[78:79], v181 offset:3104\n" \
    "s_waitcnt lgkmcnt(0)\n" \
    "s_add_u32 s56, s57, 0\n" \
    "s_add_u32 m0, s57, 0\n" \
    "s_nop 0\n" \
    "global_load_lds_dwordx4 v182, s[40:41]\n" \
    "s_add_u32 s40, s40, 16384\n" \
    "s_addc_u32 s41, s41, 0\n" \
    "s_add_u32 m0, s57, 1024\n" \
    "s_nop 0\n" \
    "global_load_lds_dwordx4 v182, s[40:41]\n" \
    "s_add_u32 s40, s40, 16384\n" \
    "s_addc_u32 s41, s41, 0\n" \
    "s_add_u32 m0, s57, 2048\n" \
    "s_nop 0\n" \
    "global_load_lds_dwordx4 v182, s[40:41]\n" \
    "s_add_u32 s40, s40, 16384\n" \
    "s_addc_u32 s41, s41, 0\n" \
    "s_add_u32 m0, s57, 3072\n" \
    "s_nop 0\n" \
    "global_load_lds_dwordx4 v182, s[40:41]\n" \
    "s_add_u32 s40, s40, 16384\n" \
    "s_addc_u32 s41, s41, 0\n" \
    "s_waitcnt vmcnt(0)\n" \
    "ds_read_b64_tr_b16 a[80:81], v181 offset:0\n" \
    "ds_read_b64_tr_b16 a[82:83], v181 offset:1024\n" \
    "ds_read_b64_tr_b16 a[84:85], v181 offset:32\n" \
    "ds_read_b64_tr_b16 a[86:87], v181 offset:1056\n" \
    "ds_read_b64_tr_b16 a[88:89], v181 offset:2048\n" \
    "ds_read_b64_tr_b16 a[90:91], v181 offset:3072\n" \
    "ds_read_b64_tr_b16 a[92:93], v181 offset:2080\n" \
    "ds_read_b64_tr_b16 a[94:95], v181 offset:3104\n" \
    "s_waitcnt lgkmcnt(0)\n" \
    "s_add_u32 s56, s57, 0\n" \
    "s_add_u32 m0, s57, 0\n" \
    "s_nop 0\n" \
    "global_load_lds_dwordx4 v182, s[40:41]\n" \
    "s_add_u32 s40, s40, 16384\n" \
    "s_addc_u32 s41, s41, 0\n" \
    "s_add_u32 m0, s57, 1024\n" \
    "s_nop 0\n" \
    "global_load_lds_dwordx4 v182, s[40:41]\n" \
    "s_add_u32 s40, s40, 16384\n" \
    "s_addc_u32 s41, s41, 0\n" \
    "s_add_u32 m0, s57, 2048\n" \
    "s_nop 0\n" \
    "global_load_lds_dwordx4 v182, s[40:41]\n" \
    "s_add_u32 s40, s40, 16384\n" \
    "s_addc_u32 s41, s41, 0\n" \
    "s_add_u32 m0, s57, 3072\n" \
    "s_nop 0\n" \
    "global_load_lds_dwordx4 v182, s[40:41]\n" \
    "s_add_u32 s40, s40, 16384\n" \
    "s_addc_u32 s41, s41, 0\n" \
    "s_waitcnt vmcnt(0)\n" \
    "ds_read_b64_tr_b16 a[96:97], v181 offset:0\n" \
    "ds_read_b64_tr_b16 a[98:99], v181 offset:1024\n" \
    "ds_read_b64_tr_b16 a[100:101], v181 offset:32\n" \
    "ds_read_b64_tr_b16 a[102:103], v181 offset:1056\n" \
    "ds_read_b64_tr_b16 a[104:105], v181 offset:2048\n" \
    "ds_read_b64_tr_b16 a[106:107], v181 offset:3072\n" \
    "ds_read_b64_tr_b16 a[108:109], v181 offset:2080\n" \
    "ds_read_b64_tr_b16 a[110:111], v181 offset:3104\n" \
    "s_waitcnt lgkmcnt(0)\n" \
    "s_add_u32 s56, s57, 0\n" \
    "s_add_u32 m0, s57, 0\n" \
    "s_nop 0\n" \
    "global_load_lds_dwordx4 v182, s[40:41]\n" \
    "s_add_u32 s40, s40, 16384\n" \
    "s_addc_u32 s41, s41, 0\n" \
    "s_add_u32 m0, s57, 1024\n" \
    "s_nop 0\n" \
    "global_load_lds_dwordx4 v182, s[40:41]\n" \
    "s_add_u32 s40, s40, 16384\n" \
    "s_addc_u32 s41, s41, 0\n" \
    "s_add_u32 m0, s57, 2048\n" \
    "s_nop 0\n" \
    "global_load_lds_dwordx4 v182, s[40:41]\n" \
    "s_add_u32 s40, s40, 16384\n" \
    "s_addc_u32 s41, s41, 0\n" \
    "s_add_u32 m0, s57, 3072\n" \
    "s_nop 0\n" \
    "global_load_lds_dwordx4 v182, s[40:41]\n" \
    "s_add_u32 s40, s40, 16384\n" \
    "s_addc_u32 s41, s41, 0\n" \
    "s_waitcnt vmcnt(0)\n" \
    "ds_read_b64_tr_b16 a[112:113], v181 offset:0\n" \
    "ds_read_b64_tr_b16 a[114:115], v181 offset:1024\n" \
    "ds_read_b64_tr_b16 a[116:117], v181 offset:32\n" \
    "ds_read_b64_tr_b16 a[118:119], v181 offset:1056\n" \
    "ds_read_b64_tr_b16 a[120:121], v181 offset:2048\n" \
    "ds_read_b64_tr_b16 a[122:123], v181 offset:3072\n" \
    "ds_read_b64_tr_b16 a[124:125], v181 offset:2080\n" \
    "ds_read_b64_tr_b16 a[126:127], v181 offset:3104\n" \
    "s_waitcnt lgkmcnt(0)\n" \
    "s_add_u32 s56, s57, 0\n" \
    "s_add_u32 m0, s57, 0\n" \
    "s_nop 0\n" \
    "global_load_lds_dwordx4 v182, s[40:41]\n" \
    "s_add_u32 s40, s40, 16384\n" \
    "s_addc_u32 s41, s41, 0\n" \
    "s_add_u32 m0, s57, 1024\n" \
    "s_nop 0\n" \
    "global_load_lds_dwordx4 v182, s[40:41]\n" \
    "s_add_u32 s40, s40, 16384\n" \
    "s_addc_u32 s41, s41, 0\n" \
    "s_add_u32 m0, s57, 2048\n" \
    "s_nop 0\n" \
    "global_load_lds_dwordx4 v182, s[40:41]\n" \
    "s_add_u32 s40, s40, 16384\n" \
    "s_addc_u32 s41, s41, 0\n" \
    "s_add_u32 m0, s57, 3072\n" \
    "s_nop 0\n" \
    "global_load_lds_dwordx4 v182, s[40:41]\n" \
    "s_add_u32 s40, s40, 16384\n" \
    "s_addc_u32 s41, s41, 0\n" \
    "s_waitcnt vmcnt(0)\n" \
    "ds_read_b64_tr_b16 a[128:129], v181 offset:0\n" \
    "ds_read_b64_tr_b16 a[130:131], v181 offset:1024\n" \
    "ds_read_b64_tr_b16 a[132:133], v181 offset:32\n" \
    "ds_read_b64_tr_b16 a[134:135], v181 offset:1056\n" \
    "ds_read_b64_tr_b16 a[136:137], v181 offset:2048\n" \
    "ds_read_b64_tr_b16 a[138:139], v181 offset:3072\n" \
    "ds_read_b64_tr_b16 a[140:141], v181 offset:2080\n" \
    "ds_read_b64_tr_b16 a[142:143], v181 offset:3104\n" \
    "s_waitcnt lgkmcnt(0)\n" \
    "s_add_u32 s56, s57, 0\n" \
    "s_add_u32 m0, s57, 0\n" \
    "s_nop 0\n" \
    "global_load_lds_dwordx4 v182, s[40:41]\n" \
    "s_add_u32 s40, s40, 16384\n" \
    "s_addc_u32 s41, s41, 0\n" \
    "s_add_u32 m0, s57, 1024\n" \
    "s_nop 0\n" \
    "global_load_lds_dwordx4 v182, s[40:41]\n" \
    "s_add_u32 s40, s40, 16384\n" \
    "s_addc_u32 s41, s41, 0\n" \
    "s_add_u32 m0, s57, 2048\n" \
    "s_nop 0\n" \
    "global_load_lds_dwordx4 v182, s[40:41]\n" \
    "s_add_u32 s40, s40, 16384\n" \
    "s_addc_u32 s41, s41, 0\n" \
    "s_add_u32 m0, s57, 3072\n" \
    "s_nop 0\n" \
    "global_load_lds_dwordx4 v182, s[40:41]\n" \
    "s_add_u32 s40, s40, 16384\n" \
    "s_addc_u32 s41, s41, 0\n" \
    "s_waitcnt vmcnt(0)\n" \
    "ds_read_b64_tr_b16 a[144:145], v181 offset:0\n" \
    "ds_read_b64_tr_b16 a[146:147], v181 offset:1024\n" \
    "ds_read_b64_tr_b16 a[148:149], v181 offset:32\n" \
    "ds_read_b64_tr_b16 a[150:151], v181 offset:1056\n" \
    "ds_read_b64_tr_b16 a[152:153], v181 offset:2048\n" \
    "ds_read_b64_tr_b16 a[154:155], v181 offset:3072\n" \
    "ds_read_b64_tr_b16 a[156:157], v181 offset:2080\n" \
    "ds_read_b64_tr_b16 a[158:159], v181 offset:3104\n" \
    "s_waitcnt lgkmcnt(0)\n" \
    "s_add_u32 s56, s57, 0\n" \
    "s_add_u32 m0, s57, 0\n" \
    "s_nop 0\n" \
    "global_load_lds_dwordx4 v182, s[40:41]\n" \
    "s_add_u32 s40, s40, 16384\n" \
    "s_addc_u32 s41, s41, 0\n" \
    "s_add_u32 m0, s57, 1024\n" \
    "s_nop 0\n" \
    "global_load_lds_dwordx4 v182, s[40:41]\n" \
    "s_add_u32 s40, s40, 16384\n" \
    "s_addc_u32 s41, s41, 0\n" \
    "s_add_u32 m0, s57, 2048\n" \
    "s_nop 0\n" \
    "global_load_lds_dwordx4 v182, s[40:41]\n" \
    "s_add_u32 s40, s40, 16384\n" \
    "s_addc_u32 s41, s41, 0\n" \
    "s_add_u32 m0, s57, 3072\n" \
    "s_nop 0\n" \
    "global_load_lds_dwordx4 v182, s[40:41]\n" \
    "s_add_u32 s40, s40, 16384\n" \
    "s_addc_u32 s41, s41, 0\n" \
    "s_waitcnt vmcnt(0)\n" \
    "ds_read_b64_tr_b16 a[160:161], v181 offset:0\n" \
    "ds_read_b64_tr_b16 a[162:163], v181 offset:1024\n" \
    "ds_read_b64_tr_b16 a[164:165], v181 offset:32\n" \
    "ds_read_b64_tr_b16 a[166:167], v181 offset:1056\n" \
    "ds_read_b64_tr_b16 a[168:169], v181 offset:2048\n" \
    "ds_read_b64_tr_b16 a[170:171], v181 offset:3072\n" \
    "ds_read_b64_tr_b16 a[172:173], v181 offset:2080\n" \
    "ds_read_b64_tr_b16 a[174:175], v181 offset:3104\n" \
    "s_waitcnt lgkmcnt(0)\n" \
    "s_add_u32 s56, s57, 0\n" \
    "s_add_u32 m0, s57, 0\n" \
    "s_nop 0\n" \
    "global_load_lds_dwordx4 v182, s[40:41]\n" \
    "s_add_u32 s40, s40, 16384\n" \
    "s_addc_u32 s41, s41, 0\n" \
    "s_add_u32 m0, s57, 1024\n" \
    "s_nop 0\n" \
    "global_load_lds_dwordx4 v182, s[40:41]\n" \
    "s_add_u32 s40, s40, 16384\n" \
    "s_addc_u32 s41, s41, 0\n" \
    "s_add_u32 m0, s57, 2048\n" \
    "s_nop 0\n" \
    "global_load_lds_dwordx4 v182, s[40:41]\n" \
    "s_add_u32 s40, s40, 16384\n" \
    "s_addc_u32 s41, s41, 0\n" \
    "s_add_u32 m0, s57, 3072\n" \
    "s_nop 0\n" \
    "global_load_lds_dwordx4 v182, s[40:41]\n" \
    "s_add_u32 s40, s40, 16384\n" \
    "s_addc_u32 s41, s41, 0\n" \
    "s_waitcnt vmcnt(0)\n" \
    "ds_read_b64_tr_b16 a[176:177], v181 offset:0\n" \
    "ds_read_b64_tr_b16 a[178:179], v181 offset:1024\n" \
    "ds_read_b64_tr_b16 a[180:181], v181 offset:32\n" \
    "ds_read_b64_tr_b16 a[182:183], v181 offset:1056\n" \
    "ds_read_b64_tr_b16 a[184:185], v181 offset:2048\n" \
    "ds_read_b64_tr_b16 a[186:187], v181 offset:3072\n" \
    "ds_read_b64_tr_b16 a[188:189], v181 offset:2080\n" \
    "ds_read_b64_tr_b16 a[190:191], v181 offset:3104\n" \
    "s_waitcnt lgkmcnt(0)\n" \
    "s_add_u32 s56, s57, 0\n" \
    "s_add_u32 m0, s57, 0\n" \
    "s_nop 0\n" \
    "global_load_lds_dwordx4 v182, s[40:41]\n" \
    "s_add_u32 s40, s40, 16384\n" \
    "s_addc_u32 s41, s41, 0\n" \
    "s_add_u32 m0, s57, 1024\n" \
    "s_nop 0\n" \
    "global_load_lds_dwordx4 v182, s[40:41]\n" \
    "s_add_u32 s40, s40, 16384\n" \
    "s_addc_u32 s41, s41, 0\n" \
    "s_add_u32 m0, s57, 2048\n" \
    "s_nop 0\n" \
    "global_load_lds_dwordx4 v182, s[40:41]\n" \
    "s_add_u32 s40, s40, 16384\n" \
    "s_addc_u32 s41, s41, 0\n" \
    "s_add_u32 m0, s57, 3072\n" \
    "s_nop 0\n" \
    "global_load_lds_dwordx4 v182, s[40:41]\n" \
    "s_add_u32 s40, s40, 16384\n" \
    "s_addc_u32 s41, s41, 0\n" \
    "s_waitcnt vmcnt(0)\n" \
    "ds_read_b64_tr_b16 a[192:193], v181 offset:0\n" \
    "ds_read_b64_tr_b16 a[194:195], v181 offset:1024\n" \
    "ds_read_b64_tr_b16 a[196:197], v181 offset:32\n" \
    "ds_read_b64_tr_b16 a[198:199], v181 offset:1056\n" \
    "ds_read_b64_tr_b16 a[200:201], v181 offset:2048\n" \
    "ds_read_b64_tr_b16 a[202:203], v181 offset:3072\n" \
    "ds_read_b64_tr_b16 a[204:205], v181 offset:2080\n" \
    "ds_read_b64_tr_b16 a[206:207], v181 offset:3104\n" \
    "s_waitcnt lgkmcnt(0)\n" \
    "s_add_u32 s56, s57, 0\n" \
    "s_add_u32 m0, s57, 0\n" \
    "s_nop 0\n" \
    "global_load_lds_dwordx4 v182, s[40:41]\n" \
    "s_add_u32 s40, s40, 16384\n" \
    "s_addc_u32 s41, s41, 0\n" \
    "s_add_u32 m0, s57, 1024\n" \
    "s_nop 0\n" \
    "global_load_lds_dwordx4 v182, s[40:41]\n" \
    "s_add_u32 s40, s40, 16384\n" \
    "s_addc_u32 s41, s41, 0\n" \
    "s_add_u32 m0, s57, 2048\n" \
    "s_nop 0\n" \
    "global_load_lds_dwordx4 v182, s[40:41]\n" \
    "s_add_u32 s40, s40, 16384\n" \
    "s_addc_u32 s41, s41, 0\n" \
    "s_add_u32 m0, s57, 3072\n" \
    "s_nop 0\n" \
    "global_load_lds_dwordx4 v182, s[40:41]\n" \
    "s_add_u32 s40, s40, 16384\n" \
    "s_addc_u32 s41, s41, 0\n" \
    "s_waitcnt vmcnt(0)\n" \
    "ds_read_b64_tr_b16 a[208:209], v181 offset:0\n" \
    "ds_read_b64_tr_b16 a[210:211], v181 offset:1024\n" \
    "ds_read_b64_tr_b16 a[212:213], v181 offset:32\n" \
    "ds_read_b64_tr_b16 a[214:215], v181 offset:1056\n" \
    "ds_read_b64_tr_b16 a[216:217], v181 offset:2048\n" \
    "ds_read_b64_tr_b16 a[218:219], v181 offset:3072\n" \
    "ds_read_b64_tr_b16 a[220:221], v181 offset:2080\n" \
    "ds_read_b64_tr_b16 a[222:223], v181 offset:3104\n" \
    "s_waitcnt lgkmcnt(0)\n" \
    "s_add_u32 s56, s57, 0\n" \
    "s_add_u32 m0, s57, 0\n" \
    "s_nop 0\n" \
    "global_load_lds_dwordx4 v182, s[40:41]\n" \
    "s_add_u32 s40, s40, 16384\n" \
    "s_addc_u32 s41, s41, 0\n" \
    "s_add_u32 m0, s57, 1024\n" \
    "s_nop 0\n" \
    "global_load_lds_dwordx4 v182, s[40:41]\n" \
    "s_add_u32 s40, s40, 16384\n" \
    "s_addc_u32 s41, s41, 0\n" \
    "s_add_u32 m0, s57, 2048\n" \
    "s_nop 0\n" \
    "global_load_lds_dwordx4 v182, s[40:41]\n" \
    "s_add_u32 s40, s40, 16384\n" \
    "s_addc_u32 s41, s41, 0\n" \
    "s_add_u32 m0, s57, 3072\n" \
    "s_nop 0\n" \
    "global_load_lds_dwordx4 v182, s[40:41]\n" \
    "s_add_u32 s40, s40, 16384\n" \
    "s_addc_u32 s41, s41, 0\n" \
    "s_waitcnt vmcnt(0)\n" \
    "ds_read_b64_tr_b16 a[224:225], v181 offset:0\n" \
    "ds_read_b64_tr_b16 a[226:227], v181 offset:1024\n" \
    "ds_read_b64_tr_b16 a[228:229], v181 offset:32\n" \
    "ds_read_b64_tr_b16 a[230:231], v181 offset:1056\n" \
    "ds_read_b64_tr_b16 a[232:233], v181 offset:2048\n" \
    "ds_read_b64_tr_b16 a[234:235], v181 offset:3072\n" \
    "ds_read_b64_tr_b16 a[236:237], v181 offset:2080\n" \
    "ds_read_b64_tr_b16 a[238:239], v181 offset:3104\n" \
    "s_waitcnt lgkmcnt(0)\n" \
    "s_add_u32 s56, s57, 0\n" \
    "s_add_u32 m0, s57, 0\n" \
    "s_nop 0\n" \
    "global_load_lds_dwordx4 v182, s[40:41]\n" \
    "s_add_u32 s40, s40, 16384\n" \
    "s_addc_u32 s41, s41, 0\n" \
    "s_add_u32 m0, s57, 1024\n" \
    "s_nop 0\n" \
    "global_load_lds_dwordx4 v182, s[40:41]\n" \
    "s_add_u32 s40, s40, 16384\n" \
    "s_addc_u32 s41, s41, 0\n" \
    "s_add_u32 m0, s57, 2048\n" \
    "s_nop 0\n" \
    "global_load_lds_dwordx4 v182, s[40:41]\n" \
    "s_add_u32 s40, s40, 16384\n" \
    "s_addc_u32 s41, s41, 0\n" \
    "s_add_u32 m0, s57, 3072\n" \
    "s_nop 0\n" \
    "global_load_lds_dwordx4 v182, s[40:41]\n" \
    "s_add_u32 s40, s40, 16384\n" \
    "s_addc_u32 s41, s41, 0\n" \
    "s_waitcnt vmcnt(0)\n" \
    "ds_read_b64_tr_b16 a[240:241], v181 offset:0\n" \
    "ds_read_b64_tr_b16 a[242:243], v181 offset:1024\n" \
    "ds_read_b64_tr_b16 a[244:245], v181 offset:32\n" \
    "ds_read_b64_tr_b16 a[246:247], v181 offset:1056\n" \
    "ds_read_b64_tr_b16 a[248:249], v181 offset:2048\n" \
    "ds_read_b64_tr_b16 a[250:251], v181 offset:3072\n" \
    "ds_read_b64_tr_b16 a[252:253], v181 offset:2080\n" \
    "ds_read_b64_tr_b16 a[254:255], v181 offset:3104\n" \
    "s_waitcnt lgkmcnt(0)\n" \
    "; ---- w2 slice: k rows [448 w, 448 w + 448) -> LDS fragments [kk][dt]\n" \
    "s_mul_i32 s56, s54, 458752\n" \
    "s_add_u32 s42, s42, s56\n" \
    "s_addc_u32 s43, s43, 0\n" \
    "s_mul_i32 s56, s54, 28672\n" \
    "v_add_u32 v178, s56, v168\n" \
    "s_add_u32 s56, s57, 0\n" \
    "s_add_u32 m0, s57, 0\n" \
    "s_nop 0\n" \
    "global_load_lds_dwordx4 v182, s[42:43]\n" \
    "s_add_u32 s42, s42, 16384\n" \
    "s_addc_u32 s43, s43, 0\n" \
    "s_add_u32 m0, s57, 1024\n" \
    "s_nop 0\n" \
    "global_load_lds_dwordx4 v182, s[42:43]\n" \
    "s_add_u32 s42, s42, 16384\n" \
    "s_addc_u32 s43, s43, 0\n" \
    "s_add_u32 m0, s57, 2048\n" \
    "s_nop 0\n" \
    "global_load_lds_dwordx4 v182, s[42:43]\n" \
    "s_add_u32 s42, s42, 16384\n" \
    "s_addc_u32 s43, s43, 0\n" \
    "s_add_u32 m0, s57, 3072\n" \
    "s_nop 0\n" \
    "global_load_lds_dwordx4 v182, s[42:43]\n" \
    "s_add_u32 s42, s42, 16384\n" \
    "s_addc_u32 s43, s43, 0\n" \
    "s_waitcnt vmcnt(0)\n" \
    "ds_read_b64_tr_b16 v[8:9], v181 offset:0\n" \
    "ds_read_b64_tr_b16 v[10:11], v181 offset:1024\n" \
    "ds_read_b64_tr_b16 v[12:13], v181 offset:32\n" \
    "ds_read_b64_tr_b16 v[14:15], v181 offset:1056\n" \
    "ds_read_b64_tr_b16 v[16:17], v181 offset:2048\n" \
    "ds_read_b64_tr_b16 v[18:19], v181 offset:3072\n" \
    "ds_read_b64_tr_b16 v[20:21], v181 offset:2080\n" \
    "ds_read_b64_tr_b16 v[22:23], v181 offset:3104\n" \
    "s_waitcnt lgkmcnt(0)\n" \
    "ds_write_b128 v178, v[8:11] offset:0\n" \
    "ds_write_b128 v178, v[12:15] offset:1024\n" \
    "ds_write_b128 v178, v[16:19] offset:2048\n" \
    "ds_write_b128 v178, v[20:23] offset:3072\n" \
    "s_waitcnt lgkmcnt(0)\n" \
    "s_add_u32 s56, s57, 0\n" \
    "s_add_u32 m0, s57, 0\n" \
    "s_nop 0\n" \
    "global_load_lds_dwordx4 v182, s[42:43]\n" \
    "s_add_u32 s42, s42, 16384\n" \
    "s_addc_u32 s43, s43, 0\n" \
    "s_add_u32 m0, s57, 1024\n" \
    "s_nop 0\n" \
    "global_load_lds_dwordx4 v182, s[42:43]\n" \
    "s_add_u32 s42, s42, 16384\n" \
    "s_addc_u32 s43, s43, 0\n" \
    "s_add_u32 m0, s57, 2048\n" \
    "s_nop 0\n" \
    "global_load_lds_dwordx4 v182, s[42:43]\n" \
    "s_add_u32 s42, s42, 16384\n" \
    "s_addc_u32 s43, s43, 0\n" \
    "s_add_u32 m0, s57, 3072\n" \
    "s_nop 0\n" \
    "global_load_lds_dwordx4 v182, s[42:43]\n" \
    "s_add_u32 s42, s42, 16384\n" \
    "s_addc_u32 s43, s43, 0\n" \
    "s_waitcnt vmcnt(0)\n" \
    "ds_read_b64_tr_b16 v[8:9], v181 offset:0\n" \
    "ds_read_b64_tr_b16 v[10:11], v181 offset:1024\n" \
    "ds_read_b64_tr_b16 v[12:13], v181 offset:32\n" \
    "ds_read_b64_tr_b16 v[14:15], v181 offset:1056\n" \
    "ds_read_b64_tr_b16 v[16:17], v181 offset:2048\n" \
    "ds_read_b64_tr_b16 v[18:19], v181 offset:3072\n" \
    "ds_read_b64_tr_b16 v[20:21], v181 offset:2080\n" \
    "ds_read_b64_tr_b16 v[22:23], v181 offset:3104\n" \
    "s_waitcnt lgkmcnt(0)\n" \
    "ds_write_b128 v178, v[8:11] offset:4096\n" \
    "ds_write_b128 v178, v[12:15] offset:5120\n" \
    "ds_write_b128 v178, v[16:19] offset:6144\n" \
    "ds_write_b128 v178, v[20:23] offset:7168\n" \
    "s_waitcnt lgkmcnt(0)\n" \
    "s_add_u32 s56, s57, 0\n" \
    "s_add_u32 m0, s57, 0\n" \
    "s_nop 0\n" \
    "global_load_lds_dwordx4 v182, s[42:43]\n" \
    "s_add_u32 s42, s42, 16384\n" \
    "s_addc_u32 s43, s43, 0\n" \
    "s_add_u32 m0, s57, 1024\n" \
    "s_nop 0\n" \
    "global_load_lds_dwordx4 v182, s[42:43]\n" \
    "s_add_u32 s42, s42, 16384\n" \
    "s_addc_u32 s43, s43, 0\n" \
    "s_add_u32 m0, s57, 2048\n" \
    "s_nop 0\n" \
    "global_load_lds_dwordx4 v182, s[42:43]\n" \
    "s_add_u32 s42, s42, 16384\n" \
    "s_addc_u32 s43, s43, 0\n" \
    "s_add_u32 m0, s57, 3072\n" \
    "s_nop 0\n" \
    "global_load_lds_dwordx4 v182, s[42:43]\n" \
    "s_add_u32 s42, s42, 16384\n" \
    "s_addc_u32 s43, s43, 0\n" \
    "s_waitcnt vmcnt(0)\n" \
    "ds_read_b64_tr_b16 v[8:9], v181 offset:0\n" \
    "ds_read_b64_tr_b16 v[10:11], v181 offset:1024\n" \
    "ds_read_b64_tr_b16 v[12:13], v181 offset:32\n" \
    "ds_read_b64_tr_b16 v[14:15], v181 offset:1056\n" \
    "ds_read_b64_tr_b16 v[16:17], v181 offset:2048\n" \
    "ds_read_b64_tr_b16 v[18:19], v181 offset:3072\n" \
    "ds_read_b64_tr_b16 v[20:21], v181 offset:2080\n" \
    "ds_read_b64_tr_b16 v[22:23], v181 offset:3104\n" \
    "s_waitcnt lgkmcnt(0)\n" \
    "ds_write_b128 v178, v[8:11] offset:8192\n" \
    "ds_write_b128 v178, v[12:15] offset:9216\n" \
    "ds_write_b128 v178, v[16:19] offset:10240\n" \
    "ds_write_b128 v178, v[20:23] offset:11264\n" \
    "s_waitcnt lgkmcnt(0)\n" \
    "s_add_u32 s56, s57, 0\n" \
    "s_add_u32 m0, s57, 0\n" \
    "s_nop 0\n" \
    "global_load_lds_dwordx4 v182, s[42:43]\n" \
    "s_add_u32 s42, s42, 16384\n" \
    "s_addc_u32 s43, s43, 0\n" \
    "s_add_u32 m0, s57, 1024\n" \
    "s_nop 0\n" \
    "global_load_lds_dwordx4 v182, s[42:43]\n" \
    "s_add_u32 s42, s42, 16384\n" \
    "s_addc_u32 s43, s43, 0\n" \
    "s_add_u32 m0, s57, 2048\n" \
    "s_nop 0\n" \
    "global_load_lds_dwordx4 v182, s[42:43]\n" \
    "s_add_u32 s42, s42, 16384\n" \
    "s_addc_u32 s43, s43, 0\n" \
    "s_add_u32 m0, s57, 3072\n" \
    "s_nop 0\n" \
    "global_load_lds_dwordx4 v182, s[42:43]\n" \
    "s_add_u32 s42, s42, 16384\n" \
    "s_addc_u32 s43, s43, 0\n" \
    "s_waitcnt vmcnt(0)\n" \
    "ds_read_b64_tr_b16 v[8:9], v181 offset:0\n" \
    "ds_read_b64_tr_b16 v[10:11], v181 offset:1024\n" \
    "ds_read_b64_tr_b16 v[12:13], v181 offset:32\n" \
    "ds_read_b64_tr_b16 v[14:15], v181 offset:1056\n" \
    "ds_read_b64_tr_b16 v[16:17], v181 offset:2048\n" \
    "ds_read_b64_tr_b16 v[18:19], v181 offset:3072\n" \
    "ds_read_b64_tr_b16 v[20:21], v181 offset:2080\n" \
    "ds_read_b64_tr_b16 v[22:23], v181 offset:3104\n" \
    "s_waitcnt lgkmcnt(0)\n" \
    "ds_write_b128 v178, v[8:11] offset:12288\n" \
    "ds_write_b128 v178, v[12:15] offset:13312\n" \
    "ds_write_b128 v178, v[16:19] offset:14336\n" \
    "ds_write_b128 v178, v[20:23] offset:15360\n" \
    "s_waitcnt lgkmcnt(0)\n" \
    "s_add_u32 s56, s57, 0\n" \
    "s_add_u32 m0, s57, 0\n" \
    "s_nop 0\n" \
    "global_load_lds_dwordx4 v182, s[42:43]\n" \
    "s_add_u32 s42, s42, 16384\n" \
    "s_addc_u32 s43, s43, 0\n" \
    "s_add_u32 m0, s57, 1024\n" \
    "s_nop 0\n" \
    "global_load_lds_dwordx4 v182, s[42:43]\n" \
    "s_add_u32 s42, s42, 16384\n" \
    "s_addc_u32 s43, s43, 0\n" \
    "s_add_u32 m0, s57, 2048\n" \
    "s_nop 0\n" \
    "global_load_lds_dwordx4 v182, s[42:43]\n" \
    "s_add_u32 s42, s42, 16384\n" \
    "s_addc_u32 s43, s43, 0\n" \
    "s_add_u32 m0, s57, 3072\n" \
    "s_nop 0\n" \
    "global_load_lds_dwordx4 v182, s[42:43]\n" \
    "s_add_u32 s42, s42, 16384\n" \
    "s_addc_u32 s43, s43, 0\n" \
    "s_waitcnt vmcnt(0)\n" \
    "ds_read_b64_tr_b16 v[8:9], v181 offset:0\n" \
    "ds_read_b64_tr_b16 v[10:11], v181 offset:1024\n" \
    "ds_read_b64_tr_b16 v[12:13], v181 offset:32\n" \
    "ds_read_b64_tr_b16 v[14:15], v181 offset:1056\n" \
    "ds_read_b64_tr_b16 v[16:17], v181 offset:2048\n" \
    "ds_read_b64_tr_b16 v[18:19], v181 offset:3072\n" \
    "ds_read_b64_tr_b16 v[20:21], v181 offset:2080\n" \
    "ds_read_b64_tr_b16 v[22:23], v181 offset:3104\n" \
    "s_waitcnt lgkmcnt(0)\n" \
    "ds_write_b128 v178, v[8:11] offset:16384\n" \
    "ds_write_b128 v178, v[12:15] offset:17408\n" \
    "ds_write_b128 v178, v[16:19] offset:18432\n" \
    "ds_write_b128 v178, v[20:23] offset:19456\n" \
    "s_waitcnt lgkmcnt(0)\n" \
    "s_add_u32 s56, s57, 0\n" \
    "s_add_u32 m0, s57, 0\n" \
    "s_nop 0\n" \
    "global_load_lds_dwordx4 v182, s[42:43]\n" \
    "s_add_u32 s42, s42, 16384\n" \
    "s_addc_u32 s43, s43, 0\n" \
    "s_add_u32 m0, s57, 1024\n" \
    "s_nop 0\n" \
    "global_load_lds_dwordx4 v182, s[42:43]\n" \
    "s_add_u32 s42, s42, 16384\n" \
    "s_addc_u32 s43, s43, 0\n" \
    "s_add_u32 m0, s57, 2048\n" \
    "s_nop 0\n" \
    "global_load_lds_dwordx4 v182, s[42:43]\n" \
    "s_add_u32 s42, s42, 16384\n" \
    "s_addc_u32 s43, s43, 0\n" \
    "s_add_u32 m0, s57, 3072\n" \
    "s_nop 0\n" \
    "global_load_lds_dwordx4 v182, s[42:43]\n" \
    "s_add_u32 s42, s42, 16384\n" \
    "s_addc_u32 s43, s43, 0\n" \
    "s_waitcnt vmcnt(0)\n" \
    "ds_read_b64_tr_b16 v[8:9], v181 offset:0\n" \
    "ds_read_b64_tr_b16 v[10:11], v181 offset:1024\n" \
    "ds_read_b64_tr_b16 v[12:13], v181 offset:32\n" \
    "ds_read_b64_tr_b16 v[14:15], v181 offset:1056\n" \
    "ds_read_b64_tr_b16 v[16:17], v181 offset:2048\n" \
    "ds_read_b64_tr_b16 v[18:19], v181 offset:3072\n" \
    "ds_read_b64_tr_b16 v[20:21], v181 offset:2080\n" \
    "ds_read_b64_tr_b16 v[22:23], v181 offset:3104\n" \
    "s_waitcnt lgkmcnt(0)\n" \
    "ds_write_b128 v178, v[8:11] offset:20480\n" \
    "ds_write_b128 v178, v[12:15] offset:21504\n" \
    "ds_write_b128 v178, v[16:19] offset:22528\n" \
    "ds_write_b128 v178, v[20:23] offset:23552\n" \
    "s_waitcnt lgkmcnt(0)\n" \
    "s_add_u32 s56, s57, 0\n" \
    "s_add_u32 m0, s57, 0\n" \
    "s_nop 0\n" \
    "global_load_lds_dwordx4 v182, s[42:43]\n" \
    "s_add_u32 s42, s42, 16384\n" \
    "s_addc_u32 s43, s43, 0\n" \
    "s_add_u32 m0, s57, 1024\n" \
    "s_nop 0\n" \
    "global_load_lds_dwordx4 v182, s[42:43]\n" \
    "s_add_u32 s42, s42, 16384\n" \
    "s_addc_u32 s43, s43, 0\n" \
    "s_add_u32 m0, s57, 2048\n" \
    "s_nop 0\n" \
    "global_load_lds_dwordx4 v182, s[42:43]\n" \
    "s_add_u32 s42, s42, 16384\n" \
    "s_addc_u32 s43, s43, 0\n" \
    "s_add_u32 m0, s57, 3072\n" \
    "s_nop 0\n" \
    "global_load_lds_dwordx4 v182, s[42:43]\n" \
    "s_add_u32 s42, s42, 16384\n" \
    "s_addc_u32 s43, s43, 0\n" \
    "s_waitcnt vmcnt(0)\n" \
    "ds_read_b64_tr_b16 v[8:9], v181 offset:0\n" \
    "ds_read_b64_tr_b16 v[10:11], v181 offset:1024\n" \
    "ds_read_b64_tr_b16 v[12:13], v181 offset:32\n" \
    "ds_read_b64_tr_b16 v[14:15], v181 offset:1056\n" \
    "ds_read_b64_tr_b16 v[16:17], v181 offset:2048\n" \
    "ds_read_b64_tr_b16 v[18:19], v181 offset:3072\n" \
    "ds_read_b64_tr_b16 v[20:21], v181 offset:2080\n" \
    "ds_read_b64_tr_b16 v[22:23], v181 offset:3104\n" \
    "s_waitcnt lgkmcnt(0)\n" \
    "ds_write_b128 v178, v[8:11] offset:24576\n" \
    "ds_write_b128 v178, v[12:15] offset:25600\n" \
    "ds_write_b128 v178, v[16:19] offset:26624\n" \
    "ds_write_b128 v178, v[20:23] offset:27648\n" \
    "s_waitcnt lgkmcnt(0)\n" \
    "v_mov_b32 v170, v178\n" \
    "s_lshl_b32 s56, s54, 12\n" \
    "s_add_u32 s56, s56, 131072\n" \
    "v_add_u32 v178, s56, v168\n" \
    "s_lshl_b32 s56, s54, 10\n" \
    "s_add_u32 s56, s56, 131072\n" \
    "v_add_u32 v179, s56, v168\n" \
    "s_lshr_b32 s56, s54, 1\n" \
    "s_lshl_b32 s56, s56, 14\n" \
    "s_and_b32 s57, s54, 1\n" \
    "s_lshl_b32 s57, s57, 5\n" \
    "s_add_u32 s56, s56, s57\n" \
    "v_lshlrev_b32 v5, 10, v1\n" \
    "v_lshl_add_u32 v5, v2, 3, v5\n" \
    "v_add_u32 v177, s56, v5\n" \
    "v_lshlrev_b32 v180, 2, v1\n" \
    "s_lshl_b32 s56, s54, 15\n" \
    "s_lshl_b32 s57, s55, 18\n" \
    "s_add_u32 s56, s56, s57\n" \
    "s_add_u32 s44, s44, s56\n" \
    "s_addc_u32 s45, s45, 0\n" \
    "s_mul_i32 s56, s54, 14336\n" \
    "s_mul_i32 s57, s55, 114688\n" \
    "s_add_u32 s56, s56, s57\n" \
    "s_add_u32 s46, s46, s56\n" \
    "s_addc_u32 s47, s47, 0\n" \
    "s_lshl_b32 s56, s55, 7\n" \
    "s_add_u32 s48, s48, s56\n" \
    "s_addc_u32 s49, s49, 0\n" \
    "s_lshl_b32 s56, s55, 15\n" \
    "s_add_u32 s50, s50, s56\n" \
    "s_addc_u32 s51, s51, 0\n" \
    "s_barrier\n" \
    "SEG_%=:\n" \
    "v_mbcnt_lo_u32_b32 v0, -1, 0\n" \
    "v_mbcnt_hi_u32_b32 v0, -1, v0\n" \
    "v_cmp_gt_u32 vcc, 14, v0\n" \
    "v_lshlrev_b32 v0, 6, v0\n" \
    "s_nop 0\n" \
    "v_cndmask_b32 v0, 0, v0, vcc\n" \
    "s_lshr_b32 s56, s55, 2\n" \
    "s_lshl_b32 s56, s56, 2\n" \
    "s_add_u32 s56, s56, 24576\n" \
    "v_add_u32 v176, s56, v0\n" \
    "s_mov_b32 s58, 0x80000\n" \
    "P1_%=:\n" \
    "global_load_dword v169, v176, s[52:53] sc1\n" \
    "s_waitcnt vmcnt(0)\n" \
    "v_cmp_ne_u32 vcc, 0, v169\n" \
    "s_nop 1\n" \
    "s_cbranch_vccz P1_ok_%=\n" \
    "s_sleep 2\n" \
    "s_sub_u32 s58, s58, 1\n" \
    "s_cmp_lg_u32 s58, 0\n" \
    "s_cbranch_scc1 P1_%=\n" \
    "s_mov_b32 s59, 5\n" \
    "s_branch FIN_%=\n" \
    "P1_ok_%=:\n" \
    "s_add_u32 s62, s46, 57344\n" \
    "s_addc_u32 s63, s47, 0\n" \
    "s_mov_b64 s[64:65], s[46:47]\n" \
    "global_load_dwordx4 v[0:3], v168, s[64:65] offset:0 sc1\n" \
    "global_load_dwordx4 v[4:7], v168, s[62:63] offset:0 sc1\n" \
    "global_load_dwordx4 v[8:11], v168, s[64:65] offset:1024 sc1\n" \
    "global_load_dwordx4 v[12:15], v168, s[62:63] offset:1024 sc1\n" \
    "global_load_dwordx4 v[16:19], v168, s[64:65] offset:2048 sc1\n" \
    "global_load_dwordx4 v[20:23], v168, s[62:63] offset:2048 sc1\n" \
    "global_load_dwordx4 v[24:27], v168, s[64:65] offset:3072 sc1\n" \
    "global_load_dwordx4 v[28:31], v168, s[62:63] offset:3072 sc1\n" \
    "s_add_u32 s64, s64, 4096\n" \
    "s_addc_u32 s65, s65, 0\n" \
    "s_add_u32 s62, s62, 4096\n" \
    "s_addc_u32 s63, s63, 0\n" \
    "global_load_dwordx4 v[32:35], v168, s[64:65] offset:0 sc1\n" \
    "global_load_dwordx4 v[36:39], v168, s[62:63] offset:0 sc1\n" \
    "global_load_dwordx4 v[40:43], v168, s[64:65] offset:1024 sc1\n" \
    "global_load_dwordx4 v[44:47], v168, s[62:63] offset:1024 sc1\n" \
    "global_load_dwordx4 v[48:51], v168, s[64:65] offset:2048 sc1\n" \
    "global_load_dwordx4 v[52:55], v168, s[62:63] offset:2048 sc1\n" \
    "global_load_dwordx4 v[56:59], v168, s[64:65] offset:3072 sc1\n" \
    "global_load_dwordx4 v[60:63], v168, s[62:63] offset:3072 sc1\n" \
    "s_add_u32 s64, s64, 4096\n" \
    "s_addc_u32 s65, s65, 0\n" \
    "s_add_u32 s62, s62, 4096\n" \
    "s_addc_u32 s63, s63, 0\n" \
    "global_load_dwordx4 v[64:67], v168, s[64:65] offset:0 sc1\n" \
    "global_load_dwordx4 v[68:71], v168, s[62:63] offset:0 sc1\n" \
    "global_load_dwordx4 v[72:75], v168, s[64:65] offset:1024 sc1\n" \
    "global_load_dwordx4 v[76:79], v168, s[62:63] offset:1024 sc1\n" \
    "global_load_dwordx4 v[80:83], v168, s[64:65] offset:2048 sc1\n" \
    "global_load_dwordx4 v[84:87], v168, s[62:63] offset:2048 sc1\n" \
    "global_load_dwordx4 v[88:91], v168, s[64:65] offset:3072 sc1\n" \
    "global_load_dwordx4 v[92:95], v168, s[62:63] offset:3072 sc1\n" \
    "s_add_u32 s64, s64, 4096\n" \
    "s_addc_u32 s65, s65, 0\n" \
    "s_add_u32 s62, s62, 4096\n" \
    "s_addc_u32 s63, s63, 0\n" \
    "global_load_dwordx4 v[96:99], v168, s[64:65] offset:0 sc1\n" \
    "global_load_dwordx4 v[100:103], v168, s[62:63] offset:0 sc1\n" \
    "global_load_dwordx4 v[104:107], v168, s[64:65] offset:1024 sc1\n" \
    "global_load_dwordx4 v[108:111], v168, s[62:63] offset:1024 sc1\n" \
    "ds_read_b128 v[112:115], v170 offset:0\n" \
    "ds_read_b128 v[116:119], v170 offset:1024\n" \
    "ds_read_b128 v[120:123], v170 offset:2048\n" \
    "ds_read_b128 v[124:127], v170 offset:3072\n" \
    "s_waitcnt vmcnt(26) lgkmcnt(2)\n" \
    "v_mfma_f32_16x16x32_bf16 v[144:147], v[112:115], v[0:3], 0\n" \
    "v_mfma_f32_16x16x32_bf16 v[148:151], v[112:115], v[4:7], 0\n" \
    "v_mfma_f32_16x16x32_bf16 v[152:155], v[116:119], v[0:3], 0\n" \
    "v_mfma_f32_16x16x32_bf16 v[156:159], v[116:119], v[4:7], 0\n" \
    "ds_read_b128 v[112:115], v170 offset:4096\n" \
    "ds_read_b128 v[116:119], v170 offset:5120\n" \
    "s_waitcnt vmcnt(24) lgkmcnt(2)\n" \
    "v_mfma_f32_16x16x32_bf16 v[144:147], v[120:123], v[8:11], v[144:147]\n" \
    "v_mfma_f32_16x16x32_bf16 v[148:151], v[120:123], v[12:15], v[148:151]\n" \
    "v_mfma_f32_16x16x32_bf16 v[152:155], v[124:127], v[8:11], v[152:155]\n" \
    "v_mfma_f32_16x16x32_bf16 v[156:159], v[124:127], v[12:15], v[156:159]\n" \
    "ds_read_b128 v[120:123], v170 offset:6144\n" \
    "ds_read_b128 v[124:127], v170 offset:7168\n" \
    "s_waitcnt vmcnt(22) lgkmcnt(2)\n" \
    "v_mfma_f32_16x16x32_bf16 v[144:147], v[112:115], v[16:19], v[144:147]\n" \
    "v_mfma_f32_16x16x32_bf16 v[148:151], v[112:115], v[20:23], v[148:151]\n" \
    "v_mfma_f32_16x16x32_bf16 v[152:155], v[116:119], v[16:19], v[152:155]\n" \
    "v_mfma_f32_16x16x32_bf16 v[156:159], v[116:119], v[20:23], v[156:159]\n" \
    "ds_read_b128 v[112:115], v170 offset:8192\n" \
    "ds_read_b128 v[116:119], v170 offset:9216\n" \
    "s_waitcnt vmcnt(20) lgkmcnt(2)\n" \
    "v_mfma_f32_16x16x32_bf16 v[144:147], v[120:123], v[24:27], v[144:147]\n" \
    "v_mfma_f32_16x16x32_bf16 v[148:151], v[120:123], v[28:31], v[148:151]\n" \
    "v_mfma_f32_16x16x32_bf16 v[152:155], v[124:127], v[24:27], v[152:155]\n" \
    "v_mfma_f32_16x16x32_bf16 v[156:159], v[124:127], v[28:31], v[156:159]\n" \
    "ds_read_b128 v[120:123], v170 offset:10240\n" \
    "ds_read_b128 v[124:127], v170 offset:11264\n" \
    "s_waitcnt vmcnt(18) lgkmcnt(2)\n" \
    "v_mfma_f32_16x16x32_bf16 v[144:147], v[112:115], v[32:35], v[144:147]\n" \
    "v_mfma_f32_16x16x32_bf16 v[148:151], v[112:115], v[36:39], v[148:151]\n" \
    "v_mfma_f32_16x16x32_bf16 v[152:155], v[116:119], v[32:35], v[152:155]\n" \
    "v_mfma_f32_16x16x32_bf16 v[156:159], v[116:119], v[36:39], v[156:159]\n" \
    "ds_read_b128 v[112:115], v170 offset:12288\n" \
    "ds_read_b128 v[116:119], v170 offset:13312\n" \
    "s_waitcnt vmcnt(16) lgkmcnt(2)\n" \
    "v_mfma_f32_16x16x32_bf16 v[144:147], v[120:123], v[40:43], v[144:147]\n" \
    "v_mfma_f32_16x16x32_bf16 v[148:151], v[120:123], v[44:47], v[148:151]\n" \
    "v_mfma_f32_16x16x32_bf16 v[152:155], v[124:127], v[40:43], v[152:155]\n" \
    "v_mfma_f32_16x16x32_bf16 v[156:159], v[124:127], v[44:47], v[156:159]\n" \
    "ds_read_b128 v[120:123], v170 offset:14336\n" \
    "ds_read_b128 v[124:127], v170 offset:15360\n" \
    "s_waitcnt vmcnt(14) lgkmcnt(2)\n" \
    "v_mfma_f32_16x16x32_bf16 v[144:147], v[112:115], v[48:51], v[144:147]\n" \
    "v_mfma_f32_16x16x32_bf16 v[148:151], v[112:115], v[52:55], v[148:151]\n" \
    "v_mfma_f32_16x16x32_bf16 v[152:155], v[116:119], v[48:51], v[152:155]\n" \
    "v_mfma_f32_16x16x32_bf16 v[156:159], v[116:119], v[52:55], v[156:159]\n" \
    "ds_read_b128 v[112:115], v170 offset:16384\n" \
    "ds_read_b128 v[116:119], v170 offset:17408\n" \
    "s_waitcnt vmcnt(12) lgkmcnt(2)\n" \
    "v_mfma_f32_16x16x32_bf16 v[144:147], v[120:123], v[56:59], v[144:147]\n" \
    "v_mfma_f32_16x16x32_bf16 v[148:151], v[120:123], v[60:63], v[148:151]\n" \
    "v_mfma_f32_16x16x32_bf16 v[152:155], v[124:127], v[56:59], v[152:155]\n" \
    "v_mfma_f32_16x16x32_bf16 v[156:159], v[124:127], v[60:63], v[156:159]\n" \
    "ds_read_b128 v[120:123], v170 offset:18432\n" \
    "ds_read_b128 v[124:127], v170 offset:19456\n" \
    "s_waitcnt vmcnt(10) lgkmcnt(2)\n" \
    "v_mfma_f32_16x16x32_bf16 v[144:147], v[112:115], v[64:67], v[144:147]\n" \
    "v_mfma_f32_16x16x32_bf16 v[148:151], v[112:115], v[68:71], v[148:151]\n" \
    "v_mfma_f32_16x16x32_bf16 v[152:155], v[116:119], v[64:67], v[152:155]\n" \
    "v_mfma_f32_16x16x32_bf16 v[156:159], v[116:119], v[68:71], v[156:159]\n" \
    "ds_read_b128 v[112:115], v170 offset:20480\n" \
    "ds_read_b128 v[116:119], v170 offset:21504\n" \
    "s_waitcnt vmcnt(8) lgkmcnt(2)\n" \
    "v_mfma_f32_16x16x32_bf16 v[144:147], v[120:123], v[72:75], v[144:147]\n" \
    "v_mfma_f32_16x16x32_bf16 v[148:151], v[120:123], v[76:79], v[148:151]\n" \
    "v_mfma_f32_16x16x32_bf16 v[152:155], v[124:127], v[72:75], v[152:155]\n" \
    "v_mfma_f32_16x16x32_bf16 v[156:159], v[124:127], v[76:79], v[156:159]\n" \
    "ds_read_b128 v[120:123], v170 offset:22528\n" \
    "ds_read_b128 v[124:127], v170 offset:23552\n" \
    "s_waitcnt vmcnt(6) lgkmcnt(2)\n" \
    "v_mfma_f32_16x16x32_bf16 v[144:147], v[112:115], v[80:83], v[144:147]\n" \
    "v_mfma_f32_16x16x32_bf16 v[148:151], v[112:115], v[84:87], v[148:151]\n" \
    "v_mfma_f32_16x16x32_bf16 v[152:155], v[116:119], v[80:83], v[152:155]\n" \
    "v_mfma_f32_16x16x32_bf16 v[156:159], v[116:119], v[84:87], v[156:159]\n" \
    "ds_read_b128 v[112:115], v170 offset:24576\n" \
    "ds_read_b128 v[116:119], v170 offset:25600\n" \
    "s_waitcnt vmcnt(4) lgkmcnt(2)\n" \
    "v_mfma_f32_16x16x32_bf16 v[144:147], v[120:123], v[88:91], v[144:147]\n" \
    "v_mfma_f32_16x16x32_bf16 v[148:151], v[120:123], v[92:95], v[148:151]\n" \
    "v_mfma_f32_16x16x32_bf16 v[152:155], v[124:127], v[88:91], v[152:155]\n" \
    "v_mfma_f32_16x16x32_bf16 v[156:159], v[124:127], v[92:95], v[156:159]\n" \
    "ds_read_b128 v[120:123], v170 offset:26624\n" \
    "ds_read_b128 v[124:127], v170 offset:27648\n" \
    "s_waitcnt vmcnt(2) lgkmcnt(2)\n" \
    "v_mfma_f32_16x16x32_bf16 v[144:147], v[112:115], v[96:99], v[144:147]\n" \
    "v_mfma_f32_16x16x32_bf16 v[148:151], v[112:115], v[100:103], v[148:151]\n" \
    "v_mfma_f32_16x16x32_bf16 v[152:155], v[116:119], v[96:99], v[152:155]\n" \
    "v_mfma_f32_16x16x32_bf16 v[156:159], v[116:119], v[100:103], v[156:159]\n" \
    "s_waitcnt vmcnt(0) lgkmcnt(0)\n" \
    "v_mfma_f32_16x16x32_bf16 v[144:147], v[120:123], v[104:107], v[144:147]\n" \
    "v_mfma_f32_16x16x32_bf16 v[148:151], v[120:123], v[108:111], v[148:151]\n" \
    "v_mfma_f32_16x16x32_bf16 v[152:155], v[124:127], v[104:107], v[152:155]\n" \
    "v_mfma_f32_16x16x32_bf16 v[156:159], v[124:127], v[108:111], v[156:159]\n" \
    "s_lshl_b32 s56, s55, 7\n" \
    "s_add_u32 s56, s56, 32768\n" \
    "v_mov_b32 v176, s56\n" \
    "s_mov_b32 s58, 0x80000\n" \
    "P2_%=:\n" \
    "global_load_dword v169, v176, s[52:53] sc1\n" \
    "s_waitcnt vmcnt(0)\n" \
    "v_cmp_ne_u32 vcc, 31, v169\n" \
    "s_nop 1\n" \
    "s_cbranch_vccz P2_ok_%=\n" \
    "s_sleep 2\n" \
    "s_sub_u32 s58, s58, 1\n" \
    "s_cmp_lg_u32 s58, 0\n" \
    "s_cbranch_scc1 P2_%=\n" \
    "s_mov_b32 s59, 6\n" \
    "s_branch FIN_%=\n" \
    "P2_ok_%=:\n" \
    "s_mov_b64 s[64:65], s[44:45]\n" \
    "s_add_u32 s62, s44, 131072\n" \
    "s_addc_u32 s63, s45, 0\n" \
    "global_load_dwordx4 v[0:3], v168, s[64:65] offset:0 sc1\n" \
    "global_load_dwordx4 v[4:7], v168, s[62:63] offset:0 sc1\n" \
    "global_load_dwordx4 v[8:11], v168, s[64:65] offset:1024 sc1\n" \
    "global_load_dwordx4 v[12:15], v168, s[62:63] offset:1024 sc1\n" \
    "global_load_dwordx4 v[16:19], v168, s[64:65] offset:2048 sc1\n" \
    "global_load_dwordx4 v[20:23], v168, s[62:63] offset:2048 sc1\n" \
    "global_load_dwordx4 v[24:27], v168, s[64:65] offset:3072 sc1\n" \
    "global_load_dwordx4 v[28:31], v168, s[62:63] offset:3072 sc1\n" \
    "s_add_u32 s64, s64, 4096\n" \
    "s_addc_u32 s65, s65, 0\n" \
    "s_add_u32 s62, s62, 4096\n" \
    "s_addc_u32 s63, s63, 0\n" \
    "global_load_dwordx4 v[32:35], v168, s[64:65] offset:0 sc1\n" \
    "global_load_dwordx4 v[36:39], v168, s[62:63] offset:0 sc1\n" \
    "global_load_dwordx4 v[40:43], v168, s[64:65] offset:1024 sc1\n" \
    "global_load_dwordx4 v[44:47], v168, s[62:63] offset:1024 sc1\n" \
    "global_load_dwordx4 v[48:51], v168, s[64:65] offset:2048 sc1\n" \
    "global_load_dwordx4 v[52:55], v168, s[62:63] offset:2048 sc1\n" \
    "global_load_dwordx4 v[56:59], v168, s[64:65] offset:3072 sc1\n" \
    "global_load_dwordx4 v[60:63], v168, s[62:63] offset:3072 sc1\n" \
    "s_add_u32 s64, s64, 4096\n" \
    "s_addc_u32 s65, s65, 0\n" \
    "s_add_u32 s62, s62, 4096\n" \
    "s_addc_u32 s63, s63, 0\n" \
    "global_load_dwordx4 v[64:67], v168, s[64:65] offset:0 sc1\n" \
    "global_load_dwordx4 v[68:71], v168, s[62:63] offset:0 sc1\n" \
    "global_load_dwordx4 v[72:75], v168, s[64:65] offset:1024 sc1\n" \
    "global_load_dwordx4 v[76:79], v168, s[62:63] offset:1024 sc1\n" \
    "global_load_dwordx4 v[80:83], v168, s[64:65] offset:2048 sc1\n" \
    "global_load_dwordx4 v[84:87], v168, s[62:63] offset:2048 sc1\n" \
    "global_load_dwordx4 v[88:91], v168, s[64:65] offset:3072 sc1\n" \
    "global_load_dwordx4 v[92:95], v168, s[62:63] offset:3072 sc1\n" \
    "s_add_u32 s64, s64, 4096\n" \
    "s_addc_u32 s65, s65, 0\n" \
    "s_add_u32 s62, s62, 4096\n" \
    "s_addc_u32 s63, s63, 0\n" \
    "global_load_dwordx4 v[96:99], v168, s[64:65] offset:0 sc1\n" \
    "global_load_dwordx4 v[100:103], v168, s[62:63] offset:0 sc1\n" \
    "global_load_dwordx4 v[104:107], v168, s[64:65] offset:1024 sc1\n" \
    "global_load_dwordx4 v[108:111], v168, s[62:63] offset:1024 sc1\n" \
    "global_load_dword v160, v180, s[48:49] offset:0 sc1\n" \
    "global_load_dword v161, v180, s[48:49] offset:64 sc1\n" \
    "s_waitcnt vmcnt(29)\n" \
    "v_mfma_f32_16x16x32_bf16 v[128:131], a[0:3], v[0:3], 0\n" \
    "v_mfma_f32_16x16x32_bf16 v[136:139], a[4:7], v[0:3], 0\n" \
    "global_load_dwordx4 v[0:3], v168, s[64:65] offset:2048 sc1\n" \
    "s_waitcnt vmcnt(29)\n" \
    "v_mfma_f32_16x16x32_bf16 v[132:135], a[0:3], v[4:7], 0\n" \
    "v_mfma_f32_16x16x32_bf16 v[140:143], a[4:7], v[4:7], 0\n" \
    "global_load_dwordx4 v[4:7], v168, s[62:63] offset:2048 sc1\n" \
    "s_waitcnt vmcnt(29)\n" \
    "v_mfma_f32_16x16x32_bf16 v[128:131], a[8:11], v[8:11], v[128:131]\n" \
    "v_mfma_f32_16x16x32_bf16 v[136:139], a[12:15], v[8:11], v[136:139]\n" \
    "global_load_dwordx4 v[8:11], v168, s[64:65] offset:3072 sc1\n" \
    "s_waitcnt vmcnt(29)\n" \
    "v_mfma_f32_16x16x32_bf16 v[132:135], a[8:11], v[12:15], v[132:135]\n" \
    "v_mfma_f32_16x16x32_bf16 v[140:143], a[12:15], v[12:15], v[140:143]\n" \
    "global_load_dwordx4 v[12:15], v168, s[62:63] offset:3072 sc1\n" \
    "s_waitcnt vmcnt(29)\n" \
    "v_mfma_f32_16x16x32_bf16 v[128:131], a[16:19], v[16:19], v[128:131]\n" \
    "v_mfma_f32_16x16x32_bf16 v[136:139], a[20:23], v[16:19], v[136:139]\n" \
    "s_add_u32 s64, s64, 4096\n" \
    "s_addc_u32 s65, s65, 0\n" \
    "s_add_u32 s62, s62, 4096\n" \
    "s_addc_u32 s63, s63, 0\n" \
    "global_load_dwordx4 v[16:19], v168, s[64:65] offset:0 sc1\n" \
    "s_waitcnt vmcnt(29)\n" \
    "v_mfma_f32_16x16x32_bf16 v[132:135], a[16:19], v[20:23], v[132:135]\n" \
    "v_mfma_f32_16x16x32_bf16 v[140:143], a[20:23], v[20:23], v[140:143]\n" \
    "global_load_dwordx4 v[20:23], v168, s[62:63] offset:0 sc1\n" \
    "s_waitcnt vmcnt(29)\n" \
    "v_mfma_f32_16x16x32_bf16 v[128:131], a[24:27], v[24:27], v[128:131]\n" \
    "v_mfma_f32_16x16x32_bf16 v[136:139], a[28:31], v[24:27], v[136:139]\n" \
    "global_load_dwordx4 v[24:27], v168, s[64:65] offset:1024 sc1\n" \
    "s_waitcnt vmcnt(29)\n" \
    "v_mfma_f32_16x16x32_bf16 v[132:135], a[24:27], v[28:31], v[132:135]\n" \
    "v_mfma_f32_16x16x32_bf16 v[140:143], a[28:31], v[28:31], v[140:143]\n" \
    "global_load_dwordx4 v[28:31], v168, s[62:63] offset:1024 sc1\n" \
    "s_waitcnt vmcnt(29)\n" \
    "v_mfma_f32_16x16x32_bf16 v[128:131], a[32:35], v[32:35], v[128:131]\n" \
    "v_mfma_f32_16x16x32_bf16 v[136:139], a[36:39], v[32:35], v[136:139]\n" \
    "global_load_dwordx4 v[32:35], v168, s[64:65] offset:2048 sc1\n" \
    "s_waitcnt vmcnt(29)\n" \
    "v_mfma_f32_16x16x32_bf16 v[132:135], a[32:35], v[36:39], v[132:135]\n" \
    "v_mfma_f32_16x16x32_bf16 v[140:143], a[36:39], v[36:39], v[140:143]\n" \
    "global_load_dwordx4 v[36:39], v168, s[62:63] offset:2048 sc1\n" \
    "s_waitcnt vmcnt(29)\n" \
    "v_mfma_f32_16x16x32_bf16 v[128:131], a[40:43], v[40:43], v[128:131]\n" \
    "v_mfma_f32_16x16x32_bf16 v[136:139], a[44:47], v[40:43], v[136:139]\n" \
    "global_load_dwordx4 v[40:43], v168, s[64:65] offset:3072 sc1\n" \
    "s_waitcnt vmcnt(29)\n" \
    "v_mfma_f32_16x16x32_bf16 v[132:135], a[40:43], v[44:47], v[132:135]\n" \
    "v_mfma_f32_16x16x32_bf16 v[140:143], a[44:47], v[44:47], v[140:143]\n" \
    "global_load_dwordx4 v[44:47], v168, s[62:63] offset:3072 sc1\n" \
    "s_waitcnt vmcnt(29)\n" \
    "v_mfma_f32_16x16x32_bf16 v[128:131], a[48:51], v[48:51], v[128:131]\n" \
    "v_mfma_f32_16x16x32_bf16 v[136:139], a[52:55], v[48:51], v[136:139]\n" \
    "s_add_u32 s64, s64, 4096\n" \
    "s_addc_u32 s65, s65, 0\n" \
    "s_add_u32 s62, s62, 4096\n" \
    "s_addc_u32 s63, s63, 0\n" \
    "global_load_dwordx4 v[48:51], v168, s[64:65] offset:0 sc1\n" \
    "s_waitcnt vmcnt(29)\n" \
    "v_mfma_f32_16x16x32_bf16 v[132:135], a[48:51], v[52:55], v[132:135]\n" \
    "v_mfma_f32_16x16x32_bf16 v[140:143], a[52:55], v[52:55], v[140:143]\n" \
    "global_load_dwordx4 v[52:55], v168, s[62:63] offset:0 sc1\n" \
    "s_waitcnt vmcnt(29)\n" \
    "v_mfma_f32_16x16x32_bf16 v[128:131], a[56:59], v[56:59], v[128:131]\n" \
    "v_mfma_f32_16x16x32_bf16 v[136:139], a[60:63], v[56:59], v[136:139]\n" \
    "global_load_dwordx4 v[56:59], v168, s[64:65] offset:1024 sc1\n" \
    "s_waitcnt vmcnt(29)\n" \
    "v_mfma_f32_16x16x32_bf16 v[132:135], a[56:59], v[60:63], v[132:135]\n" \
    "v_mfma_f32_16x16x32_bf16 v[140:143], a[60:63], v[60:63], v[140:143]\n" \
    "global_load_dwordx4 v[60:63], v168, s[62:63] offset:1024 sc1\n" \
    "s_waitcnt vmcnt(29)\n" \
    "v_mfma_f32_16x16x32_bf16 v[128:131], a[64:67], v[64:67], v[128:131]\n" \
    "v_mfma_f32_16x16x32_bf16 v[136:139], a[68:71], v[64:67], v[136:139]\n" \
    "global_load_dwordx4 v[64:67], v168, s[64:65] offset:2048 sc1\n" \
    "s_waitcnt vmcnt(29)\n" \
    "v_mfma_f32_16x16x32_bf16 v[132:135], a[64:67], v[68:71], v[132:135]\n" \
    "v_mfma_f32_16x16x32_bf16 v[140:143], a[68:71], v[68:71], v[140:143]\n" \
    "global_load_dwordx4 v[68:71], v168, s[62:63] offset:2048 sc1\n" \
    "s_waitcnt vmcnt(29)\n" \
    "v_mfma_f32_16x16x32_bf16 v[128:131], a[72:75], v[72:75], v[128:131]\n" \
    "v_mfma_f32_16x16x32_bf16 v[136:139], a[76:79], v[72:75], v[136:139]\n" \
    "global_load_dwordx4 v[72:75], v168, s[64:65] offset:3072 sc1\n" \
    "s_waitcnt vmcnt(29)\n" \
    "v_mfma_f32_16x16x32_bf16 v[132:135], a[72:75], v[76:79], v[132:135]\n" \
    "v_mfma_f32_16x16x32_bf16 v[140:143], a[76:79], v[76:79], v[140:143]\n" \
    "global_load_dwordx4 v[76:79], v168, s[62:63] offset:3072 sc1\n" \
    "s_waitcnt vmcnt(29)\n" \
    "v_mfma_f32_16x16x32_bf16 v[128:131], a[80:83], v[80:83], v[128:131]\n" \
    "v_mfma_f32_16x16x32_bf16 v[136:139], a[84:87], v[80:83], v[136:139]\n" \
    "s_add_u32 s64, s64, 4096\n" \
    "s_addc_u32 s65, s65, 0\n" \
    "s_add_u32 s62, s62, 4096\n" \
    "s_addc_u32 s63, s63, 0\n" \
    "global_load_dwordx4 v[80:83], v168, s[64:65] offset:0 sc1\n" \
    "s_waitcnt vmcnt(29)\n" \
    "v_mfma_f32_16x16x32_bf16 v[132:135], a[80:83], v[84:87], v[132:135]\n" \
    "v_mfma_f32_16x16x32_bf16 v[140:143], a[84:87], v[84:87], v[140:143]\n" \
    "global_load_dwordx4 v[84:87], v168, s[62:63] offset:0 sc1\n" \
    "s_waitcnt vmcnt(29)\n" \
    "v_mfma_f32_16x16x32_bf16 v[128:131], a[88:91], v[88:91], v[128:131]\n" \
    "v_mfma_f32_16x16x32_bf16 v[136:139], a[92:95], v[88:91], v[136:139]\n" \
    "global_load_dwordx4 v[88:91], v168, s[64:65] offset:1024 sc1\n" \
    "s_waitcnt vmcnt(29)\n" \
    "v_mfma_f32_16x16x32_bf16 v[132:135], a[88:91], v[92:95], v[132:135]\n" \
    "v_mfma_f32_16x16x32_bf16 v[140:143], a[92:95], v[92:95], v[140:143]\n" \
    "global_load_dwordx4 v[92:95], v168, s[62:63] offset:1024 sc1\n" \
    "s_waitcnt vmcnt(29)\n" \
    "v_mfma_f32_16x16x32_bf16 v[128:131], a[96:99], v[96:99], v[128:131]\n" \
    "v_mfma_f32_16x16x32_bf16 v[136:139], a[100:103], v[96:99], v[136:139]\n" \
    "global_load_dwordx4 v[96:99], v168, s[64:65] offset:2048 sc1\n" \
    "s_waitcnt vmcnt(29)\n" \
    "v_mfma_f32_16x16x32_bf16 v[132:135], a[96:99], v[100:103], v[132:135]\n" \
    "v_mfma_f32_16x16x32_bf16 v[140:143], a[100:103], v[100:103], v[140:143]\n" \
    "global_load_dwordx4 v[100:103], v168, s[62:63] offset:2048 sc1\n" \
    "s_waitcnt vmcnt(29)\n" \
    "v_mfma_f32_16x16x32_bf16 v[128:131], a[104:107], v[104:107], v[128:131]\n" \
    "v_mfma_f32_16x16x32_bf16 v[136:139], a[108:111], v[104:107], v[136:139]\n" \
    "global_load_dwordx4 v[104:107], v168, s[64:65] offset:3072 sc1\n" \
    "s_waitcnt vmcnt(29)\n" \
    "v_mfma_f32_16x16x32_bf16 v[132:135], a[104:107], v[108:111], v[132:135]\n" \
    "v_mfma_f32_16x16x32_bf16 v[140:143], a[108:111], v[108:111], v[140:143]\n" \
    "global_load_dwordx4 v[108:111], v168, s[62:63] offset:3072 sc1\n" \
    "s_waitcnt vmcnt(27)\n" \
    "v_mfma_f32_16x16x32_bf16 v[128:131], a[112:115], v[0:3], v[128:131]\n" \
    "v_mfma_f32_16x16x32_bf16 v[136:139], a[116:119], v[0:3], v[136:139]\n" \
    "s_add_u32 s64, s64, 4096\n" \
    "s_addc_u32 s65, s65, 0\n" \
    "s_add_u32 s62, s62, 4096\n" \
    "s_addc_u32 s63, s63, 0\n" \
    "global_load_dwordx4 v[0:3], v168, s[64:65] offset:0 sc1\n" \
    "s_waitcnt vmcnt(27)\n" \
    "v_mfma_f32_16x16x32_bf16 v[132:135], a[112:115], v[4:7], v[132:135]\n" \
    "v_mfma_f32_16x16x32_bf16 v[140:143], a[116:119], v[4:7], v[140:143]\n" \
    "global_load_dwordx4 v[4:7], v168, s[62:63] offset:0 sc1\n" \
    "s_waitcnt vmcnt(27)\n" \
    "v_mfma_f32_16x16x32_bf16 v[128:131], a[120:123], v[8:11], v[128:131]\n" \
    "v_mfma_f32_16x16x32_bf16 v[136:139], a[124:127], v[8:11], v[136:139]\n" \
    "global_load_dwordx4 v[8:11], v168, s[64:65] offset:1024 sc1\n" \
    "s_waitcnt vmcnt(27)\n" \
    "v_mfma_f32_16x16x32_bf16 v[132:135], a[120:123], v[12:15], v[132:135]\n" \
    "v_mfma_f32_16x16x32_bf16 v[140:143], a[124:127], v[12:15], v[140:143]\n" \
    "global_load_dwordx4 v[12:15], v168, s[62:63] offset:1024 sc1\n" \
    "s_waitcnt vmcnt(27)\n" \
    "v_mfma_f32_16x16x32_bf16 v[128:131], a[128:131], v[16:19], v[128:131]\n" \
    "v_mfma_f32_16x16x32_bf16 v[136:139], a[132:135], v[16:19], v[136:139]\n" \
    "global_load_dwordx4 v[16:19], v168, s[64:65] offset:2048 sc1\n" \
    "s_waitcnt vmcnt(27)\n" \
    "v_mfma_f32_16x16x32_bf16 v[132:135], a[128:131], v[20:23], v[132:135]\n" \
    "v_mfma_f32_16x16x32_bf16 v[140:143], a[132:135], v[20:23], v[140:143]\n" \
    "global_load_dwordx4 v[20:23], v168, s[62:63] offset:2048 sc1\n" \
    "s_waitcnt vmcnt(27)\n" \
    "v_mfma_f32_16x16x32_bf16 v[128:131], a[136:139], v[24:27], v[128:131]\n" \
    "v_mfma_f32_16x16x32_bf16 v[136:139], a[140:143], v[24:27], v[136:139]\n" \
    "global_load_dwordx4 v[24:27], v168, s[64:65] offset:3072 sc1\n" \
    "s_waitcnt vmcnt(27)\n" \
    "v_mfma_f32_16x16x32_bf16 v[132:135], a[136:139], v[28:31], v[132:135]\n" \
    "v_mfma_f32_16x16x32_bf16 v[140:143], a[140:143], v[28:31], v[140:143]\n" \
    "global_load_dwordx4 v[28:31], v168, s[62:63] offset:3072 sc1\n" \
    "s_waitcnt vmcnt(27)\n" \
    "v_mfma_f32_16x16x32_bf16 v[128:131], a[144:147], v[32:35], v[128:131]\n" \
    "v_mfma_f32_16x16x32_bf16 v[136:139], a[148:151], v[32:35], v[136:139]\n" \
    "s_waitcnt vmcnt(26)\n" \
    "v_mfma_f32_16x16x32_bf16 v[132:135], a[144:147], v[36:39], v[132:135]\n" \
    "v_mfma_f32_16x16x32_bf16 v[140:143], a[148:151], v[36:39], v[140:143]\n" \
    "s_waitcnt vmcnt(25)\n" \
    "v_mfma_f32_16x16x32_bf16 v[128:131], a[152:155], v[40:43], v[128:131]\n" \
    "v_mfma_f32_16x16x32_bf16 v[136:139], a[156:159], v[40:43], v[136:139]\n" \
    "s_waitcnt vmcnt(24)\n" \
    "v_mfma_f32_16x16x32_bf16 v[132:135], a[152:155], v[44:47], v[132:135]\n" \
    "v_mfma_f32_16x16x32_bf16 v[140:143], a[156:159], v[44:47], v[140:143]\n" \
    "s_waitcnt vmcnt(23)\n" \
    "v_mfma_f32_16x16x32_bf16 v[128:131], a[160:163], v[48:51], v[128:131]\n" \
    "v_mfma_f32_16x16x32_bf16 v[136:139], a[164:167], v[48:51], v[136:139]\n" \
    "s_waitcnt vmcnt(22)\n" \
    "v_mfma_f32_16x16x32_bf16 v[132:135], a[160:163], v[52:55], v[132:135]\n" \
    "v_mfma_f32_16x16x32_bf16 v[140:143], a[164:167], v[52:55], v[140:143]\n" \
    "s_waitcnt vmcnt(21)\n" \
    "v_mfma_f32_16x16x32_bf16 v[128:131], a[168:171], v[56:59], v[128:131]\n" \
    "v_mfma_f32_16x16x32_bf16 v[136:139], a[172:175], v[56:59], v[136:139]\n" \
    "s_waitcnt vmcnt(20)\n" \
    "v_mfma_f32_16x16x32_bf16 v[132:135], a[168:171], v[60:63], v[132:135]\n" \
    "v_mfma_f32_16x16x32_bf16 v[140:143], a[172:175], v[60:63], v[140:143]\n" \
    "s_waitcnt vmcnt(19)\n" \
    "v_mfma_f32_16x16x32_bf16 v[128:131], a[176:179], v[64:67], v[128:131]\n" \
    "v_mfma_f32_16x16x32_bf16 v[136:139], a[180:183], v[64:67], v[136:139]\n" \
    "s_waitcnt vmcnt(18)\n" \
    "v_mfma_f32_16x16x32_bf16 v[132:135], a[176:179], v[68:71], v[132:135]\n" \
    "v_mfma_f32_16x16x32_bf16 v[140:143], a[180:183], v[68:71], v[140:143]\n" \
    "s_waitcnt vmcnt(17)\n" \
    "v_mfma_f32_16x16x32_bf16 v[128:131], a[184:187], v[72:75], v[128:131]\n" \
    "v_mfma_f32_16x16x32_bf16 v[136:139], a[188:191], v[72:75], v[136:139]\n" \
    "s_waitcnt vmcnt(16)\n" \
    "v_mfma_f32_16x16x32_bf16 v[132:135], a[184:187], v[76:79], v[132:135]\n" \
    "v_mfma_f32_16x16x32_bf16 v[140:143], a[188:191], v[76:79], v[140:143]\n" \
    "s_waitcnt vmcnt(15)\n" \
    "v_mfma_f32_16x16x32_bf16 v[128:131], a[192:195], v[80:83], v[128:131]\n" \
    "v_mfma_f32_16x16x32_bf16 v[136:139], a[196:199], v[80:83], v[136:139]\n" \
    "s_waitcnt vmcnt(14)\n" \
    "v_mfma_f32_16x16x32_bf16 v[132:135], a[192:195], v[84:87], v[132:135]\n" \
    "v_mfma_f32_16x16x32_bf16 v[140:143], a[196:199], v[84:87], v[140:143]\n" \
    "s_waitcnt vmcnt(13)\n" \
    "v_mfma_f32_16x16x32_bf16 v[128:131], a[200:203], v[88:91], v[128:131]\n" \
    "v_mfma_f32_16x16x32_bf16 v[136:139], a[204:207], v[88:91], v[136:139]\n" \
    "s_waitcnt vmcnt(12)\n" \
    "v_mfma_f32_16x16x32_bf16 v[132:135], a[200:203], v[92:95], v[132:135]\n" \
    "v_mfma_f32_16x16x32_bf16 v[140:143], a[204:207], v[92:95], v[140:143]\n" \
    "s_waitcnt vmcnt(11)\n" \
    "v_mfma_f32_16x16x32_bf16 v[128:131], a[208:211], v[96:99], v[128:131]\n" \
    "v_mfma_f32_16x16x32_bf16 v[136:139], a[212:215], v[96:99], v[136:139]\n" \
    "s_waitcnt vmcnt(10)\n" \
    "v_mfma_f32_16x16x32_bf16 v[132:135], a[208:211], v[100:103], v[132:135]\n" \
    "v_mfma_f32_16x16x32_bf16 v[140:143], a[212:215], v[100:103], v[140:143]\n" \
    "s_waitcnt vmcnt(9)\n" \
    "v_mfma_f32_16x16x32_bf16 v[128:131], a[216:219], v[104:107], v[128:131]\n" \
    "v_mfma_f32_16x16x32_bf16 v[136:139], a[220:223], v[104:107], v[136:139]\n" \
    "s_waitcnt vmcnt(8)\n" \
    "v_mfma_f32_16x16x32_bf16 v[132:135], a[216:219], v[108:111], v[132:135]\n" \
    "v_mfma_f32_16x16x32_bf16 v[140:143], a[220:223], v[108:111], v[140:143]\n" \
    "s_waitcnt vmcnt(7)\n" \
    "v_mfma_f32_16x16x32_bf16 v[128:131], a[224:227], v[0:3], v[128:131]\n" \
    "v_mfma_f32_16x16x32_bf16 v[136:139], a[228:231], v[0:3], v[136:139]\n" \
    "s_waitcnt vmcnt(6)\n" \
    "v_mfma_f32_16x16x32_bf16 v[132:135], a[224:227], v[4:7], v[132:135]\n" \
    "v_mfma_f32_16x16x32_bf16 v[140:143], a[228:231], v[4:7], v[140:143]\n" \
    "s_waitcnt vmcnt(5)\n" \
    "v_mfma_f32_16x16x32_bf16 v[128:131], a[232:235], v[8:11], v[128:131]\n" \
    "v_mfma_f32_16x16x32_bf16 v[136:139], a[236:239], v[8:11], v[136:139]\n" \
    "s_waitcnt vmcnt(4)\n" \
    "v_mfma_f32_16x16x32_bf16 v[132:135], a[232:235], v[12:15], v[132:135]\n" \
    "v_mfma_f32_16x16x32_bf16 v[140:143], a[236:239], v[12:15], v[140:143]\n" \
    "s_waitcnt vmcnt(3)\n" \
    "v_mfma_f32_16x16x32_bf16 v[128:131], a[240:243], v[16:19], v[128:131]\n" \
    "v_mfma_f32_16x16x32_bf16 v[136:139], a[244:247], v[16:19], v[136:139]\n" \
    "s_waitcnt vmcnt(2)\n" \
    "v_mfma_f32_16x16x32_bf16 v[132:135], a[240:243], v[20:23], v[132:135]\n" \
    "v_mfma_f32_16x16x32_bf16 v[140:143], a[244:247], v[20:23], v[140:143]\n" \
    "s_waitcnt vmcnt(1)\n" \
    "v_mfma_f32_16x16x32_bf16 v[128:131], a[248:251], v[24:27], v[128:131]\n" \
    "v_mfma_f32_16x16x32_bf16 v[136:139], a[252:255], v[24:27], v[136:139]\n" \
    "s_waitcnt vmcnt(0)\n" \
    "v_mfma_f32_16x16x32_bf16 v[132:135], a[248:251], v[28:31], v[132:135]\n" \
    "v_mfma_f32_16x16x32_bf16 v[140:143], a[252:255], v[28:31], v[140:143]\n" \
    "v_sub_f32 v162, 1.0, v160\n" \
    "v_sub_f32 v163, 1.0, v161\n" \
    "v_mul_f32 v144, v144, v162\n" \
    "v_mul_f32 v145, v145, v162\n" \
    "v_mul_f32 v146, v146, v162\n" \
    "v_mul_f32 v147, v147, v162\n" \
    "v_fma_f32 v144, v128, v160, v144\n" \
    "v_fma_f32 v145, v129, v160, v145\n" \
    "v_fma_f32 v146, v130, v160, v146\n" \
    "v_fma_f32 v147, v131, v160, v147\n" \
    "ds_write_b128 v178, v[144:147] offset:0\n" \
    "v_mul_f32 v148, v148, v163\n" \
    "v_mul_f32 v149, v149, v163\n" \
    "v_mul_f32 v150, v150, v163\n" \
    "v_mul_f32 v151, v151, v163\n" \
    "v_fma_f32 v148, v132, v161, v148\n" \
    "v_fma_f32 v149, v133, v161, v149\n" \
    "v_fma_f32 v150, v134, v161, v150\n" \
    "v_fma_f32 v151, v135, v161, v151\n" \
    "ds_write_b128 v178, v[148:151] offset:2048\n" \
    "v_mul_f32 v152, v152, v162\n" \
    "v_mul_f32 v153, v153, v162\n" \
    "v_mul_f32 v154, v154, v162\n" \
    "v_mul_f32 v155, v155, v162\n" \
    "v_fma_f32 v152, v136, v160, v152\n" \
    "v_fma_f32 v153, v137, v160, v153\n" \
    "v_fma_f32 v154, v138, v160, v154\n" \
    "v_fma_f32 v155, v139, v160, v155\n" \
    "ds_write_b128 v178, v[152:155] offset:1024\n" \
    "v_mul_f32 v156, v156, v163\n" \
    "v_mul_f32 v157, v157, v163\n" \
    "v_mul_f32 v158, v158, v163\n" \
    "v_mul_f32 v159, v159, v163\n" \
    "v_fma_f32 v156, v140, v161, v156\n" \
    "v_fma_f32 v157, v141, v161, v157\n" \
    "v_fma_f32 v158, v142, v161, v158\n" \
    "v_fma_f32 v159, v143, v161, v159\n" \
    "ds_write_b128 v178, v[156:159] offset:3072\n" \
    "s_waitcnt lgkmcnt(0)\n" \
    "s_barrier\n" \
    "ds_read_b128 v[0:3], v179 offset:0\n" \
    "ds_read_b128 v[4:7], v179 offset:4096\n" \
    "ds_read_b128 v[8:11], v179 offset:8192\n" \
    "ds_read_b128 v[12:15], v179 offset:12288\n" \
    "v_xor_b32 v178, 0x4000, v178\n" \
    "v_xor_b32 v179, 0x4000, v179\n" \
    "s_waitcnt lgkmcnt(0)\n" \
    "v_add_f32 v0, v0, v4\n" \
    "v_add_f32 v8, v8, v12\n" \
    "v_add_f32 v1, v1, v5\n" \
    "v_add_f32 v9, v9, v13\n" \
    "v_add_f32 v2, v2, v6\n" \
    "v_add_f32 v10, v10, v14\n" \
    "v_add_f32 v3, v3, v7\n" \
    "v_add_f32 v11, v11, v15\n" \
    "v_add_f32 v0, v0, v8\n" \
    "v_add_f32 v1, v1, v9\n" \
    "v_add_f32 v2, v2, v10\n" \
    "v_add_f32 v3, v3, v11\n" \
    "v_cvt_pk_bf16_f32 v0, v0, v1\n" \
    "v_cvt_pk_bf16_f32 v1, v2, v3\n" \
    "global_store_dwordx2 v177, v[0:1], s[50:51]\n" \
    "s_add_u32 s44, s44, 786432\n" \
    "s_addc_u32 s45, s45, 0\n" \
    "s_add_u32 s46, s46, 344064\n" \
    "s_addc_u32 s47, s47, 0\n" \
    "s_add_u32 s48, s48, 384\n" \
    "s_addc_u32 s49, s49, 0\n" \
    "s_add_u32 s50, s50, 98304\n" \
    "s_addc_u32 s51, s51, 0\n" \
    "s_add_u32 s55, s55, 3\n" \
    "s_cmp_lt_u32 s55, 64\n" \
    "s_cbranch_scc1 SEG_%=\n" \
    "FIN_%=:\n" \
    "s_waitcnt vmcnt(0) lgkmcnt(0)\n" \
    "s_mov_b32 %0, s59\n"
#define OUT_CLOBBERS "v0", "v1", "v2", "v3", "v4", "v5", "v6", "v7", "v8", "v9", "v10", "v11", "v12", "v13", "v14", "v15", "v16", "v17", "v18", "v19", "v20", "v21", "v22", "v23", "v24", "v25", "v26", "v27", "v28", "v29", "v30", "v31", "v32", "v33", "v34", "v35", "v36", "v37", "v38", "v39", "v40", "v41", "v42", "v43", "v44", "v45", "v46", "v47", "v48", "v49", "v50", "v51", "v52", "v53", "v54", "v55", "v56", "v57", "v58", "v59", "v60", "v61", "v62", "v63", "v64", "v65", "v66", "v67", "v68", "v69", "v70", "v71", "v72", "v73", "v74", "v75", "v76", "v77", "v78", "v79", "v80", "v81", "v82", "v83", "v84", "v85", "v86", "v87", "v88", "v89", "v90", "v91", "v92", "v93", "v94", "v95", "v96", "v97", "v98", "v99", "v100", "v101", "v102", "v103", "v104", "v105", "v106", "v107", "v108", "v109", "v110", "v111", "v112", "v113", "v114", "v115", "v116", "v117", "v118", "v119", "v120", "v121", "v122", "v123", "v124", "v125", "v126", "v127", "v128", "v129", "v130", "v131", "v132", "v133", "v134", "v135", "v136", "v137", "v138", "v139", "v140", "v141", "v142", "v143", "v144", "v145", "v146", "v147", "v148", "v149", "v150", "v151", "v152", "v153", "v154", "v155", "v156", "v157", "v158", "v159", "v160", "v161", "v162", "v163", "v164", "v165", "v166", "v167", "v168", "v169", "v170", "v171", "v172", "v173", "v174", "v175", "v176", "v177", "v178", "v179", "v180", "v181", "v182", "v183", "v184", "v185", "v186", "v187", "v188", "v189", "v190", "v191", "v192", "v193", "v194", "v195", "v196", "v197", "v198", "v199", "v200", "v201", "v202", "v203", "v204", "v205", "v206", "v207", "v208", "v209", "v210", "v211", "v212", "v213", "v214", "v215", "v216", "v217", "v218", "v219", "v220", "v221", "v222", "v223", "v224", "v225", "v226", "v227", "v228", "v229", "v230", "v231", "v232", "v233", "v234", "v235", "v236", "v237", "v238", "v239", "v240", "v241", "v242", "v243", "v244", "v245", "v246", "v247", "v248", "v249", "v250", "v251", "v252", "v253", "v254", "v255", "a0", "a1", "a2", "a3", "a4", "a5", "a6", "a7", "a8", "a9", "a10", "a11", "a12", "a13", "a14", "a15", "a16", "a17", "a18", "a19", "a20", "a21", "a22", "a23", "a24", "a25", "a26", "a27", "a28", "a29", "a30", "a31", "a32", "a33", "a34", "a35", "a36", "a37", "a38", "a39", "a40", "a41", "a42", "a43", "a44", "a45", "a46", "a47", "a48", "a49", "a50", "a51", "a52", "a53", "a54", "a55", "a56", "a57", "a58", "a59", "a60", "a61", "a62", "a63", "a64", "a65", "a66", "a67", "a68", "a69", "a70", "a71", "a72", "a73", "a74", "a75", "a76", "a77", "a78", "a79", "a80", "a81", "a82", "a83", "a84", "a85", "a86", "a87", "a88", "a89", "a90", "a91", "a92", "a93", "a94", "a95", "a96", "a97", "a98", "a99", "a100", "a101", "a102", "a103", "a104", "a105", "a106", "a107", "a108", "a109", "a110", "a111", "a112", "a113", "a114", "a115", "a116", "a117", "a118", "a119", "a120", "a121", "a122", "a123", "a124", "a125", "a126", "a127", "a128", "a129", "a130", "a131", "a132", "a133", "a134", "a135", "a136", "a137", "a138", "a139", "a140", "a141", "a142", "a143", "a144", "a145", "a146", "a147", "a148", "a149", "a150", "a151", "a152", "a153", "a154", "a155", "a156", "a157", "a158", "a159", "a160", "a161", "a162", "a163", "a164", "a165", "a166", "a167", "a168", "a169", "a170", "a171", "a172", "a173", "a174", "a175", "a176", "a177", "a178", "a179", "a180", "a181", "a182", "a183", "a184", "a185", "a186", "a187", "a188", "a189", "a190", "a191", "a192", "a193", "a194", "a195", "a196", "a197", "a198", "a199", "a200", "a201", "a202", "a203", "a204", "a205", "a206", "a207", "a208", "a209", "a210", "a211", "a212", "a213", "a214", "a215", "a216", "a217", "a218", "a219", "a220", "a221", "a222", "a223", "a224", "a225", "a226", "a227", "a228", "a229", "a230", "a231", "a232", "a233", "a234", "a235", "a236", "a237", "a238", "a239", "a240", "a241", "a242", "a243", "a244", "a245", "a246", "a247", "a248", "a249", "a250", "a251", "a252", "a253", "a254", "a255", "s40", "s41", "s42", "s43", "s44", "s45", "s46", "s47", "s48", "s49", "s50", "s51", "s52", "s53", "s54", "s55", "s56", "s57", "s58", "s59", "s60", "s61", "s62", "s63", "s64", "s65", "s66", "s67", "s68", "s69", "s70", "s71", "s72", "s73", "s74", "s75", "s76", "s77", "s78", "s79", "s80", "s81", "s82", "s83", "s84", "s85", "s86", "s87", "s88", "s89", "s90", "s91", "s92", "s93", "s94", "s95", "s96", "s97", "s98", "s99", "vcc", "scc", "memory"

__device__ __forceinline__ void gemm_role(int j, int c, int w, const char* x, const char* Wq, const char* Wk, const char* Wv, const char* w1, const char* w3, char* base) {
    const int k = c & 15, tb0 = c >> 4;
    int type; const char *W0, *W1; char *o0, *o1, *fl; int stride;
    if (k < 8) {
        const int h = 8 * j + k;
        type = 0; W0 = Wk + h * 256; W1 = Wv + h * 256; stride = 8192;
        o0 = base + OFF_KX + h * 8192 + (w & 1) * 2048; o1 = base + OFF_VT + h * 8192 + (w & 1) * 4096;
        fl = base + OFF_FLG + F_XF + h * 64;
    } else if (k < 12) {
        const int hp = 4 * j + (k - 8);
        type = 1; W0 = Wq + hp * 512; W1 = W0 + 256; stride = 8192;
        o0 = base + OFF_QX + (2 * hp) * 8192 + (w & 1) * 2048; o1 = o0 + 8192;
        fl = base + OFF_FLG + F_QF + hp * 64;
    } else {
        const int fb = 4 * j + (k - 12);
        if (fb >= 14) return;
        type = 2; W0 = w1 + fb * 256; W1 = w3 + fb * 256; stride = 3584;
        o0 = base + OFF_HF + (4 * fb + w) * 1024; o1 = o0;
        fl = base + OFF_FLG + F_UF + fb * 64;
    }
    asm volatile(GEMM_ASM :: "s"(W0), "s"(W1), "s"(x), "s"(o0), "s"(o1), "s"(fl), "s"(type), "s"(w), "s"(stride), "s"(tb0) : GEMM_CLOBBERS);
}

#define GAS __attribute__((address_space(1)))

struct Params {
    const char* x; const char* mem0; const char* Wq; const char* Wk; const char* Wv; const char* Wo;
    const char* w1; const char* w3; const char* w2; const char* gw; char* out; char* base;

    int rpre; int gpre;
};

__device__ __forceinline__ void calibrate(char* base) {
    unsigned best0 = ~0u, best1 = ~0u;
    for (int r = 0; r < 8; ++r) {
        unsigned long long c0, c1, c2;
        GAS unsigned* q0 = (GAS unsigned*)(base + 4096); GAS unsigned* q1 = (GAS unsigned*)(base + 8192 + 4096);
        asm volatile("s_memtime %0\n\ts_waitcnt lgkmcnt(0)" : "=s"(c0) :: "memory");
        asm volatile("global_store_dword %0, %1, off sc1\n\ts_waitcnt vmcnt(0)" :: "v"(q0), "v"(-1) : "memory");
        asm volatile("s_memtime %0\n\ts_waitcnt lgkmcnt(0)" : "=s"(c1) :: "memory");
        asm volatile("global_store_dword %0, %1, off sc1\n\ts_waitcnt vmcnt(0)" :: "v"(q1), "v"(-1) : "memory");
        asm volatile("s_memtime %0\n\ts_waitcnt lgkmcnt(0)" : "=s"(c2) :: "memory");
        best0 = min(best0, (unsigned)(c1 - c0)); best1 = min(best1, (unsigned)(c2 - c1));
    }
    __hip_atomic_store((GAS unsigned*)(base + OFF_FLG + F_CTL), best1 < best0 ? 1u : 0u, __ATOMIC_RELAXED, __HIP_MEMORY_SCOPE_AGENT);
}

typedef const __attribute__((address_space(4))) Params* KP;
#define ROLE_ARGS KP kp = (KP)__builtin_amdgcn_kernarg_segment_ptr(); asm volatile("" : "+s"(kp)); char* base = kp->base; char* fb = base + OFF_FLG;

__global__ void __launch_bounds__(256, 1) gated_mix_kernel(Params unused_directly) {
    extern __shared__ __attribute__((aligned(16))) unsigned char lds[];
    const int b = blockIdx.x, xcd = b & 7, slot = b >> 3;
    const int w = __builtin_amdgcn_readfirstlane(threadIdx.x >> 6);
    if (xcd < 2) {
        ROLE_ARGS
        const int x = xcd, h = slot;
        const char* wq = kp->Wq + h * 256; const char* wk = kp->Wk + h * 256; const char* wv = kp->Wv + h * 256;
        const char* wo = kp->Wo + (size_t)h * 128 * 1024 + x * 512;
        const char* kx = base + OFF_KX + (size_t)h * 8192; const char* vt = base + OFF_VT + (size_t)h * 8192;
        char* kvm = base + (x == 0 ? OFF_KM : OFF_VM) + (size_t)h * 2048;
        char* stp = base;
        unsigned f = 0;
        asm volatile(CHAIN_ASM : "=s"(f) : "s"(wq), "s"(wk), "s"(wv), "s"(wo), "s"(base), "s"(kp->mem0), "s"(kx), "s"(vt), "s"(kvm), "s"(fb), "s"(h), "s"(x), "s"(w),
                     "s"(kp->rpre), "s"(kp->gpre), "s"(stp) : CHAIN_CLOBBERS);

    } else if (xcd == 2 || (xcd == 3 && slot < 16)) {
        ROLE_ARGS
        const int grp = xcd == 2 ? (slot >> 4) : 2, db = slot & 15;
        const char* wo = kp->Wo + db * 64; const char* w2 = kp->w2 + db * 64; char* out = kp->out + db * 64;
        const char* ox = base + OFF_OX; const char* hf = base + OFF_HF; const char* gz = base + OFF_GZ;
        unsigned f = 0;
        asm volatile(OUT_ASM : "=s"(f) : "s"(wo), "s"(w2), "s"(ox), "s"(hf), "s"(gz), "s"(out), "s"(fb), "s"(w), "s"(grp) : OUT_CLOBBERS);

    } else if (xcd == 3) {
        ROLE_ARGS
        const int ai = slot - 16, grp = ai >> 3, h = (ai & 7) * 4 + w;
        if (ai == 0 && w == 0) {
            unsigned lane;
            asm volatile("v_mbcnt_lo_u32_b32 %0, -1, 0\n\tv_mbcnt_hi_u32_b32 %0, -1, %0" : "=v"(lane));
            if (lane == 0) calibrate(base);
        }
        const char* qx = base + OFF_QX + (size_t)h * 8192; const char* kx = base + OFF_KX + (size_t)h * 8192; const char* vt = base + OFF_VT + (size_t)h * 8192;
        const char* km = base + OFF_KM + (size_t)h * 2048; const char* vm = base + OFF_VM + (size_t)h * 2048;
        char* ox = base + OFF_OX + (size_t)h * 4096; char* gz = base + OFF_GZ;
        const int lb = w * 2048;
        unsigned f = 0;
        asm volatile(ATT_ASM : "=s"(f) : "s"(qx), "s"(kx), "s"(vt), "s"(km), "s"(vm), "s"(ox), "s"(kp->x), "s"(kp->gw), "s"(gz), "s"(fb), "s"(h), "s"(grp), "s"(lb) : ATT_CLOBBERS);

    } else {
        ROLE_ARGS
        (void)fb;
        gemm_role(xcd - 4, slot, w, kp->x, kp->Wq, kp->Wk, kp->Wv, kp->w1, kp->w3, base);
    }
}

extern "C" void kernel_launch(void* const* d_in, const int* in_sizes, int n_in, void* d_out, int out_size, void* d_ws, size_t ws_size, hipStream_t stream) {
    Params p;
    p.x = (const char*)d_in[0]; p.mem0 = (const char*)d_in[1]; p.Wq = (const char*)d_in[2]; p.Wk = (const char*)d_in[3]; p.Wv = (const char*)d_in[4];
    p.Wo = (const char*)d_in[5]; p.w1 = (const char*)d_in[8]; p.w3 = (const char*)d_in[9]; p.w2 = (const char*)d_in[10]; p.gw = (const char*)d_in[11];
    p.out = (char*)d_out;
    p.base = (char*)(((uintptr_t)d_ws + (2u << 20) - 1) & ~(uintptr_t)((2u << 20) - 1));
    p.rpre = 8; p.gpre = 10;
    (void)hipFuncSetAttribute((const void*)gated_mix_kernel, hipFuncAttributeMaxDynamicSharedMemorySize, 160 * 1024);
    (void)hipMemsetAsync(p.base, 0xFF, OFF_FLG + 65536, stream);
    gated_mix_kernel<<<256, 256, 160 * 1024, stream>>>(p);
}
```
